```python
import torch

def _softmax(x, axis=-1):
    return torch.softmax(x, dim=axis)
def _gelu(x):
    return torch.nn.functional.gelu(x, approximate="tanh")
def _silu(x):
    return torch.nn.functional.silu(x)
def _sigmoid(x):
    return torch.sigmoid(x)
def _layernorm(x, axis=-1, eps=1e-5):
    m = x.mean(dim=axis, keepdim=True)
    v = x.var(dim=axis, keepdim=True, unbiased=False)
    return (x - m) / torch.sqrt(v + eps)
def _scatter_add(tgt, idx, src, axis=0):
    return tgt.clone().index_add_(axis, idx, src)
def _segment_sum(src, seg, n):
    out = torch.zeros((n,) + src.shape[1:], dtype=src.dtype)
    return out.index_add_(0, seg, src)
def _pad(x, pw):
    flat = tuple(v for pair in reversed(pw) for v in pair)
    return torch.nn.functional.pad(x, flat)

kernel_name = 'sequential_preLN__gqa_block_cached__mixer_token_mlp__decode'

def setup_inputs():

    _w = (lambda a: (lambda t: t.to(torch.bfloat16) if t.is_floating_point() else t)(torch.as_tensor(a)))
    def _A_setup():
        B, Sq, D = 8, 1, 512
        Hq, Hkv, Sk, Dh = 32, 8, 4096, 128
        assert Hq % Hkv == 0, (Hq, Hkv)
        return {
            "x": _w((lambda *s: torch.randn(*s, dtype=torch.bfloat16) * 0.05)(B, Sq, D)),
            "k_cache": _w((lambda *s: torch.randn(*s, dtype=torch.bfloat16) * 1.6)(B, Hkv, Sk, Dh)),
            "v_cache": _w((lambda *s: torch.randn(*s, dtype=torch.bfloat16) * 0.8)(B, Hkv, Sk, Dh)),
            "wq": _w((lambda *s: torch.randn(*s, dtype=torch.bfloat16) * 1.6)(D, Hq * Dh) * (D ** -0.5)),
            "wo": _w((lambda *s: torch.randn(*s, dtype=torch.bfloat16) * 0.8)(Hq * Dh, D) * ((Hq * Dh) ** -0.5)),
        }
    _A_in = _A_setup()

    def _B_setup():
        B, S, D, F = 8, 1, 512, 1792
        return {
            "x": _w((lambda *s: torch.randn(*s, dtype=torch.bfloat16) * 0.05)(B, S, D)),
            "w1": _w((lambda *s: torch.randn(*s, dtype=torch.bfloat16) * 0.8)(S, F) * (S ** -0.5)),
            "b1": _w((lambda *s: torch.randn(*s, dtype=torch.bfloat16) * 0.8)(F) * 0.02),
            "w2": _w((lambda *s: torch.randn(*s, dtype=torch.bfloat16) * 0.8)(F, S) * (F ** -0.5)),
            "b2": _w((lambda *s: torch.randn(*s, dtype=torch.bfloat16) * 0.4)(S) * 0.02),
        }
    _B_in = _B_setup()

    return {
        "A_x": _A_in["x"],
        "A_k_cache": _A_in["k_cache"],
        "A_v_cache": _A_in["v_cache"],
        "A_wq": _A_in["wq"],
        "A_wo": _A_in["wo"],
        "B_x": _B_in["x"],
        "B_w1": _B_in["w1"],
        "B_b1": _B_in["b1"],
        "B_w2": _B_in["w2"],
        "B_b2": _B_in["b2"],
    }

def reference(A_x, A_k_cache, A_v_cache, A_wq, A_wo, B_x, B_w1, B_b1, B_w2, B_b2):
    def _A_ref(x, k_cache, v_cache, wq, wo):
        B, Sq, D = x.shape
        Hq, Dh = 32, 128
        Hkv = k_cache.shape[1]
        rep = Hq // Hkv
        q = (lambda x, *d: x.permute(*d))((x @ wq).reshape(B, Sq, Hq, Dh), 0, 2, 1, 3)
        k = (lambda x, n, axis: torch.repeat_interleave(x, n, dim=axis))(k_cache, rep, axis=1)
        v = (lambda x, n, axis: torch.repeat_interleave(x, n, dim=axis))(v_cache, rep, axis=1)
        scores = torch.einsum("bhqd,bhkd->bhqk", q, k) * (Dh ** -0.5)
        p = _softmax(scores, axis=-1)
        o = torch.einsum("bhqk,bhkd->bhqd", p, v)
        return (lambda x, *d: x.permute(*d))(o, 0, 2, 1, 3).reshape(B, Sq, Hq * Dh) @ wo

    def _B_ref(x, w1, b1, w2, b2):
        B, S, D = x.shape
        h = _layernorm(x, axis=-1)
        u = torch.einsum("bsd,sf->bdf", h, w1) + b1
        u = _gelu(u)
        y = torch.einsum("bdf,fs->bds", u, w2) + b2
        return (lambda x, *d: x.permute(*d))(y, 0, 2, 1)

    x = A_x
    h = x + _A_ref(_layernorm(x, axis=-1), A_k_cache, A_v_cache, A_wq, A_wo)
    h2 = h + _B_ref(_layernorm(h, axis=-1), B_w1, B_b1, B_w2, B_b2)
    return h2
```

```cpp
#include <hip/hip_runtime.h>
#include <hip/hip_bf16.h>
#include <stdint.h>

#define GAS __attribute__((address_space(1)))
#define LAS __attribute__((address_space(3)))
typedef unsigned v4u __attribute__((ext_vector_type(4)));
typedef unsigned v2u __attribute__((ext_vector_type(2)));
typedef float f32x4 __attribute__((ext_vector_type(4)));
typedef short bf16x8 __attribute__((ext_vector_type(8)));
typedef short v4s __attribute__((ext_vector_type(4)));

constexpr int NB = 8, DM = 512, HQ = 32, HKV = 8, SK = 4096, DH = 128, FF = 1792;
constexpr int NSPLIT = 4, KEYS_WG = SK / NSPLIT, KEYS_W = KEYS_WG / 8;
constexpr float LOG2E = 1.4426950408889634f;
constexpr float SCALE_L2 = 0.08838834764831845f * LOG2E;

#define AUX_KV 2
constexpr size_t WS_Q = 0;
constexpr size_t WS_PO = 65536;
constexpr size_t WS_ML = WS_PO + 256 * 512 * 4;
constexpr size_t WS_OP = WS_ML + 256 * 8 * 4;

__device__ __forceinline__ f32x4 mfma16(v4u a, v4u b, f32x4 c) {
  return __builtin_amdgcn_mfma_f32_16x16x32_bf16(__builtin_bit_cast(bf16x8, a), __builtin_bit_cast(bf16x8, b), c, 0, 0, 0);
}
typedef __bf16 bf16x2_t __attribute__((ext_vector_type(2)));
typedef float f32x2 __attribute__((ext_vector_type(2)));
__device__ __forceinline__ unsigned cvtpk(float lo, float hi) {
  return __builtin_bit_cast(unsigned, __builtin_convertvector(f32x2{lo, hi}, bf16x2_t));
}
__device__ __forceinline__ float bflo(unsigned u) { return __builtin_bit_cast(float, u << 16); }
__device__ __forceinline__ float bfhi(unsigned u) { return __builtin_bit_cast(float, u & 0xffff0000u); }
__device__ __forceinline__ float rbf(float x) { return bflo(cvtpk(x, x)); }
__device__ __forceinline__ v4u ldnt(const GAS v4u* p) { return __builtin_nontemporal_load(p); }
__device__ __forceinline__ float xrow_max(float x) {
  auto s = __builtin_amdgcn_permlane16_swap(__float_as_uint(x), __float_as_uint(x), false, false);
  x = fmaxf(__uint_as_float(s[0]), __uint_as_float(s[1]));
  auto t = __builtin_amdgcn_permlane32_swap(__float_as_uint(x), __float_as_uint(x), false, false);
  return fmaxf(__uint_as_float(t[0]), __uint_as_float(t[1]));
}
__device__ __forceinline__ float xrow_sum(float x) {
  auto s = __builtin_amdgcn_permlane16_swap(__float_as_uint(x), __float_as_uint(x), false, false);
  x = __uint_as_float(s[0]) + __uint_as_float(s[1]);
  auto t = __builtin_amdgcn_permlane32_swap(__float_as_uint(x), __float_as_uint(x), false, false);
  return __uint_as_float(t[0]) + __uint_as_float(t[1]);
}
template <int CTRL> __device__ __forceinline__ float dpp(float x) {
  return __builtin_bit_cast(float, __builtin_amdgcn_mov_dpp(__builtin_bit_cast(int, x), CTRL, 0xf, 0xf, true));
}
__device__ __forceinline__ float wave_sum(float x) {
  x += dpp<0xB1>(x); x += dpp<0x4E>(x); x += dpp<0x141>(x); x += dpp<0x128>(x);
  return xrow_sum(x);
}

__global__ __launch_bounds__(64) void k_qproj(const unsigned short* __restrict__ x, const unsigned short* __restrict__ wq, unsigned char* ws) {
  __shared__ __attribute__((aligned(16))) unsigned char lds[16384 + 256];
  const int lane = threadIdx.x, g = lane >> 4, r = lane & 15;
  const int n0 = blockIdx.x * 16;
  v4u w[16], xa[16];
#pragma unroll
  for (int i = 0; i < 16; ++i) w[i] = *(const GAS v4u*)(wq + (size_t)(32 * i + (lane >> 1)) * (HQ * DH) + n0 + (lane & 1) * 8);
#pragma unroll
  for (int s = 0; s < 16; ++s) xa[s] = *(const GAS v4u*)(x + (r & 7) * DM + 32 * s + 8 * g);
  if (r >= 8) {
#pragma unroll
    for (int s = 0; s < 16; ++s) xa[s] = v4u{0x3f803f80u, 0x3f803f80u, 0x3f803f80u, 0x3f803f80u};
  }
#pragma unroll
  for (int i = 0; i < 16; ++i) *(LAS v4u*)(lds + i * 1024 + lane * 16) = w[i];
  f32x4 d = {0.f, 0.f, 0.f, 0.f}, d2 = {0.f, 0.f, 0.f, 0.f};
  const int troff = (8 * g + (r >> 2)) * 32 + (r & 3) * 8;
#pragma unroll
  for (int s = 0; s < 16; ++s) {
    v4s b0 = __builtin_amdgcn_ds_read_tr16_b64_v4i16((LAS v4s*)(lds + s * 1024 + troff));
    v4s b1 = __builtin_amdgcn_ds_read_tr16_b64_v4i16((LAS v4s*)(lds + s * 1024 + troff + 128));
    v2u u0 = __builtin_bit_cast(v2u, b0), u1 = __builtin_bit_cast(v2u, b1);
    v4u bfr = {u0.x, u0.y, u1.x, u1.y};
    d = mfma16(xa[s], bfr, d);
    d2 = mfma16(xa[s], xa[s], d2);
  }
  LAS float* st = (LAS float*)(lds + 16384);
  if (g < 2) {
#pragma unroll
    for (int i = 0; i < 4; ++i) {
      if (r == 4 * g + i) st[4 * g + i] = d2[i];
      if (r == 8) st[8 + 4 * g + i] = d2[i];
    }
  }
  if (g == 2) st[16 + r] = d[0];
  __syncthreads();
  if (g < 2) {
    const float cs = st[16 + r];
    unsigned short* q = (unsigned short*)(ws + WS_Q);
#pragma unroll
    for (int i = 0; i < 4; ++i) {
      const int b = 4 * g + i;
      const float mean = st[8 + b] * (1.f / DM);
      const float var = fmaxf(st[b] * (1.f / DM) - mean * mean, 0.f);
      const float rstd = rsqrtf(var + 1e-5f);
      const float qv = (d[i] - mean * cs) * rstd;
      q[b * (HQ * DH) + n0 + r] = (unsigned short)(cvtpk(qv, qv) & 0xffff);
    }
  }
}

__global__ __launch_bounds__(512, 2) void k_attn(const unsigned short* __restrict__ kc, const unsigned short* __restrict__ vc, unsigned char* ws) {
  __shared__ __attribute__((aligned(16))) float lds[8 * 9 * 64 + 64];
  const int tid = threadIdx.x, lane = tid & 63, wave = __builtin_amdgcn_readfirstlane(tid >> 6), g = lane >> 4, r = lane & 15;
  const int wg = blockIdx.x, kvh = wg & 7, b = wg >> 5, sp = (wg >> 3) & 3;
  const size_t base = ((size_t)(b * HKV + kvh) * SK + sp * KEYS_WG + wave * KEYS_W) * DH;
  const __amdgpu_buffer_rsrc_t krs = __builtin_amdgcn_make_buffer_rsrc((void*)(kc + base), 0, KEYS_W * DH * 2, 0x00020000);
  const __amdgpu_buffer_rsrc_t vrs = __builtin_amdgcn_make_buffer_rsrc((void*)(vc + base), 0, KEYS_W * DH * 2, 0x00020000);
  const int koff = (4 * (r & 3) + (r >> 2)) * 256 + g * 16;
  const int voff = lane * 16;
  v4u qf[4];
  {
    const GAS v4u* qp = (const GAS v4u*)((const unsigned short*)(ws + WS_Q) + b * (HQ * DH) + (kvh * 4 + (r & 3)) * DH) + g;
#pragma unroll
    for (int u = 0; u < 4; ++u) qf[u] = qp[4 * u];
  }
  __builtin_amdgcn_sched_barrier(0);
  v4u kv[32];
#pragma unroll
  for (int i = 0; i < 32; ++i) { kv[i] = __builtin_amdgcn_raw_buffer_load_b128(krs, koff + (i & 3) * 64, (i >> 2) * 4096, AUX_KV); __builtin_amdgcn_sched_barrier(0); }
#pragma unroll
  for (int u = 0; u < 4; ++u) { if (r >= 4) qf[u] = v4u{0u, 0u, 0u, 0u}; }
  f32x4 s[8];
#pragma unroll
  for (int t = 0; t < 8; ++t) {
    f32x4 a = {0.f, 0.f, 0.f, 0.f};
#pragma unroll
    for (int u = 0; u < 4; ++u) a = mfma16(kv[4 * t + u], qf[u], a);
    s[t] = a;
#pragma unroll
    for (int u = 0; u < 4; ++u) { __builtin_amdgcn_sched_barrier(0); kv[4 * t + u] = __builtin_amdgcn_raw_buffer_load_b128(vrs, voff + u * 1024, t * 4096, AUX_KV); }
    __builtin_amdgcn_sched_barrier(0);
  }
  float m = s[0][0];
#pragma unroll
  for (int t = 0; t < 8; ++t)
#pragma unroll
    for (int i = 0; i < 4; ++i) m = fmaxf(m, s[t][i]);
  m = xrow_max(m);
  LAS float* mx = (LAS float*)lds + 8 * 9 * 64;
  if (lane < 4) mx[wave * 4 + lane] = m;
  __syncthreads();
  {
    float mm = mx[r & 3];
#pragma unroll
    for (int w = 1; w < 8; ++w) mm = fmaxf(mm, mx[w * 4 + (r & 3)]);
    m = (r < 4) ? mm : 0.f;
  }
  const float msc = m * SCALE_L2;
  v4u pf[4];
#pragma unroll
  for (int G = 0; G < 4; ++G) {
    float p[8];
#pragma unroll
    for (int j = 0; j < 8; ++j) p[j] = __builtin_amdgcn_exp2f(fmaf(s[2 * G + (j >> 2)][j & 3], SCALE_L2, -msc));
    pf[G] = v4u{cvtpk(p[0], p[1]), cvtpk(p[2], p[3]), cvtpk(p[4], p[5]), cvtpk(p[6], p[7])};
  }
  f32x4 acc[9];
#pragma unroll
  for (int j = 0; j < 9; ++j) acc[j] = f32x4{0.f, 0.f, 0.f, 0.f};
  const v4u ones = {0x3f803f80u, 0x3f803f80u, 0x3f803f80u, 0x3f803f80u};
#pragma unroll
  for (int G = 0; G < 4; ++G) {
    acc[8] = mfma16(pf[G], ones, acc[8]);
#pragma unroll
    for (int d = 0; d < 4; ++d) {
      v4u lo, hi;
#pragma unroll
      for (int jj = 0; jj < 4; ++jj) {
        lo[jj] = __builtin_amdgcn_perm(kv[8 * G + 2 * jj + 1][d], kv[8 * G + 2 * jj][d], 0x05040100u);
        hi[jj] = __builtin_amdgcn_perm(kv[8 * G + 2 * jj + 1][d], kv[8 * G + 2 * jj][d], 0x07060302u);
      }
      acc[2 * d] = mfma16(pf[G], lo, acc[2 * d]);
      acc[2 * d + 1] = mfma16(pf[G], hi, acc[2 * d + 1]);
    }
    __builtin_amdgcn_sched_barrier(0);
  }
  if (lane < 16) {
#pragma unroll
    for (int j = 0; j < 9; ++j) *(LAS f32x4*)(lds + (wave * 9 + j) * 64 + lane * 4) = acc[j];
  }
  __syncthreads();
  {
    float o = 0.f;
#pragma unroll
    for (int w = 0; w < 8; ++w) o += lds[w * 9 * 64 + tid];
    const int head = tid & 3, c = (tid >> 2) & 15, jp = tid >> 6;
    float* po = (float*)(ws + WS_PO);
    po[wg * 512 + head * 128 + 8 * c + jp] = o;
    if (tid < 4) {
      float l = 0.f;
#pragma unroll
      for (int w = 0; w < 8; ++w) l += lds[(w * 9 + 8) * 64 + tid];
      float* ml = (float*)(ws + WS_ML);
      ml[wg * 8 + tid] = msc;
      ml[wg * 8 + 4 + tid] = l;
    }
  }
}

__global__ __launch_bounds__(512, 2) void k_wo(const unsigned short* __restrict__ wo, unsigned char* ws) {
  __shared__ __attribute__((aligned(16))) unsigned char lds[1024 + 8 * 128 * 4];
  const int tid = threadIdx.x, lane = tid & 63, wave = __builtin_amdgcn_readfirstlane(tid >> 6), g = lane >> 4, r = lane & 15;
  const int wg = blockIdx.x, kvh = wg & 7, b = wg >> 5, sp = (wg >> 3) & 3;
  v4u w[16];
  const GAS v4u* wp = (const GAS v4u*)(wo + (size_t)(kvh * 512 + wave * 64 + 8 * g) * DM + sp * 128) + r;
#pragma unroll
  for (int i = 0; i < 16; ++i) w[i] = wp[((i >> 3) * 32 + (i & 7)) * (DM / 8)];
  const float* po = (const float*)(ws + WS_PO);
  const float* ml = (const float*)(ws + WS_ML);
  const int head = tid >> 7;
  float ov[4], mv[4], lv[4];
#pragma unroll
  for (int s2 = 0; s2 < 4; ++s2) {
    const int src = (b << 5) | (s2 << 3) | kvh;
    ov[s2] = po[src * 512 + tid]; mv[s2] = ml[src * 8 + head]; lv[s2] = ml[src * 8 + 4 + head];
  }
  const float M = fmaxf(fmaxf(mv[0], mv[1]), fmaxf(mv[2], mv[3]));
  float L = 0.f, o = 0.f;
#pragma unroll
  for (int s2 = 0; s2 < 4; ++s2) { const float e = __builtin_amdgcn_exp2f(mv[s2] - M); L = fmaf(e, lv[s2], L); o = fmaf(e, ov[s2], o); }
  o = o / L;
  ((LAS unsigned short*)lds)[tid] = (unsigned short)(cvtpk(o, o) & 0xffff);
  __syncthreads();
  v4u af[2];
  af[0] = *(LAS v4u*)(lds + wave * 128 + g * 16);
  af[1] = *(LAS v4u*)(lds + wave * 128 + 64 + g * 16);
  f32x4 acc[8];
#pragma unroll
  for (int j = 0; j < 8; ++j) acc[j] = f32x4{0.f, 0.f, 0.f, 0.f};
#pragma unroll
  for (int t = 0; t < 2; ++t)
#pragma unroll
    for (int d = 0; d < 4; ++d) {
      v4u lo, hi;
#pragma unroll
      for (int jj = 0; jj < 4; ++jj) {
        lo[jj] = __builtin_amdgcn_perm(w[8 * t + 2 * jj + 1][d], w[8 * t + 2 * jj][d], 0x05040100u);
        hi[jj] = __builtin_amdgcn_perm(w[8 * t + 2 * jj + 1][d], w[8 * t + 2 * jj][d], 0x07060302u);
      }
      acc[2 * d] = mfma16(af[t], lo, acc[2 * d]);
      acc[2 * d + 1] = mfma16(af[t], hi, acc[2 * d + 1]);
    }
  LAS float* red = (LAS float*)(lds + 1024);
  if (lane < 16) {
    *(LAS f32x4*)(red + wave * 128 + lane * 8) = f32x4{acc[0][0], acc[1][0], acc[2][0], acc[3][0]};
    *(LAS f32x4*)(red + wave * 128 + lane * 8 + 4) = f32x4{acc[4][0], acc[5][0], acc[6][0], acc[7][0]};
  }
  __syncthreads();
  if (tid < 128) {
    float a = 0.f;
#pragma unroll
    for (int w2 = 0; w2 < 8; ++w2) a += red[w2 * 128 + tid];
    ((float*)(ws + WS_OP))[(b * 8 + kvh) * 512 + sp * 128 + tid] = a;
  }
}

__device__ __forceinline__ float block_sum(float v, LAS float* sc, int wave, int lane) {
  v = wave_sum(v);
  if (lane == 0) sc[wave] = v;
  __syncthreads();
  float t = 0.f;
#pragma unroll
  for (int w = 0; w < 8; ++w) t += sc[w];
  return t;
}
__global__ __launch_bounds__(512, 2) void k_final(const unsigned short* __restrict__ x, const unsigned short* __restrict__ w1, const unsigned short* __restrict__ b1,
                                                  const unsigned short* __restrict__ w2, const unsigned short* __restrict__ b2, unsigned char* ws, unsigned short* out) {
  __shared__ __attribute__((aligned(16))) float lds[64 + 16 + 16 + 64 + 64];
  const int tid = threadIdx.x, lane = tid & 63, wave = __builtin_amdgcn_readfirstlane(tid >> 6);
  const int wg = blockIdx.x, b = wg >> 5, j = wg & 31;
  float pa[7], pb[7], pc[7], pd[7];
#pragma unroll
  for (int i = 0; i < 7; ++i) {
    const int f = (tid & 255) + 256 * i;
    pa[i] = bflo(w1[f]); pb[i] = bflo(b1[f]); const float w2v = bflo(w2[f]); pc[i] = pa[i] * w2v; pd[i] = pb[i] * w2v;
  }
  const float* op = (const float*)(ws + WS_OP) + b * 8 * 512 + tid;
  float a = 0.f;
#pragma unroll
  for (int k = 0; k < 8; ++k) a += op[k * 512];
  const float h = rbf(bflo(x[b * DM + tid]) + rbf(a));
  LAS float* sc = (LAS float*)lds;
  const float m1 = block_sum(h, sc, wave, lane) * (1.f / DM);
  const float d1 = h - m1;
  const float v1 = block_sum(d1 * d1, sc + 8, wave, lane) * (1.f / DM);
  const float hn = d1 * rsqrtf(v1 + 1e-5f);
  const float m2 = block_sum(hn, sc + 16, wave, lane) * (1.f / DM);
  const float d2 = hn - m2;
  const float v2 = block_sum(d2 * d2, sc + 24, wave, lane) * (1.f / DM);
  const float hn2 = d2 * rsqrtf(v2 + 1e-5f);
  LAS float* tv = (LAS float*)lds + 64;
  LAS float* hv = (LAS float*)lds + 80;
  if ((tid >> 4) == j) { tv[tid & 15] = hn2; hv[tid & 15] = h; }
  __syncthreads();
  constexpr float C0 = -2.f * 0.7978845608028654f * LOG2E, C1 = C0 * 0.044715f;
  float acc[8];
#pragma unroll
  for (int c = 0; c < 8; ++c) {
    const float t = tv[(tid >> 8) * 8 + c];
    float s = 0.f;
#pragma unroll
    for (int i = 0; i < 7; ++i) {
      const float u = fmaf(t, pa[i], pb[i]);
      const float y = u * fmaf(u * u, C1, C0);
      const float e = __builtin_amdgcn_exp2f(y);
      s = fmaf(fmaf(t, pc[i], pd[i]), __builtin_amdgcn_rcpf(1.f + e), s);
    }
    acc[c] = wave_sum(s);
  }
  LAS float* red = (LAS float*)lds + 96;
  if (lane == 0) {
#pragma unroll
    for (int c = 0; c < 8; ++c) red[wave * 8 + c] = acc[c];
  }
  __syncthreads();
  if (tid < 16) {
    const int half = tid >> 3, c = tid & 7;
    float y = 0.f;
#pragma unroll
    for (int w = 0; w < 4; ++w) y += red[(half * 4 + w) * 8 + c];
    y = rbf(rbf(y) + bflo(b2[0]));
    const float o = hv[tid] + y;
    out[b * DM + j * 16 + tid] = (unsigned short)(cvtpk(o, o) & 0xffff);
  }
}

extern "C" void kernel_launch(void* const* d_in, const int* in_sizes, int n_in, void* d_out, int out_size, void* d_ws, size_t ws_size, hipStream_t stream) {
  const unsigned short* x = (const unsigned short*)d_in[0];
  const unsigned short* kc = (const unsigned short*)d_in[1];
  const unsigned short* vc = (const unsigned short*)d_in[2];
  const unsigned short* wq = (const unsigned short*)d_in[3];
  const unsigned short* wo = (const unsigned short*)d_in[4];
  const unsigned short* w1 = (const unsigned short*)d_in[6];
  const unsigned short* b1 = (const unsigned short*)d_in[7];
  const unsigned short* w2 = (const unsigned short*)d_in[8];
  const unsigned short* b2 = (const unsigned short*)d_in[9];
  unsigned char* ws = (unsigned char*)d_ws;
  k_qproj<<<256, 64, 0, stream>>>(x, wq, ws);
  k_attn<<<256, 512, 0, stream>>>(kc, vc, ws);
  k_wo<<<256, 512, 0, stream>>>(wo, ws);
  k_final<<<256, 512, 0, stream>>>(x, w1, b1, w2, b2, ws, (unsigned short*)d_out);
}
```

```cpp
#include <hip/hip_runtime.h>
#include <hip/hip_bf16.h>
#include <stdint.h>

#define GAS __attribute__((address_space(1)))
#define LAS __attribute__((address_space(3)))
#define RLX_AGENT __ATOMIC_RELAXED, __HIP_MEMORY_SCOPE_AGENT
#define RLX_WG __ATOMIC_RELAXED, __HIP_MEMORY_SCOPE_WORKGROUP
typedef unsigned v4u __attribute__((ext_vector_type(4)));
typedef unsigned v2u __attribute__((ext_vector_type(2)));
typedef float f32x4 __attribute__((ext_vector_type(4)));
typedef float f32x2 __attribute__((ext_vector_type(2)));
typedef short bf16x8 __attribute__((ext_vector_type(8)));
typedef short v4s __attribute__((ext_vector_type(4)));
typedef __bf16 bf16x2_t __attribute__((ext_vector_type(2)));
typedef unsigned long long u64;
typedef GAS unsigned gu32;
typedef GAS u64 gu64;
typedef unsigned short us;

constexpr int DM = 512, HQ = 32, HKV = 8, SK = 4096, DH = 128, FF = 1792;
constexpr int KEYS_WG = 1024, KEYS_W = 128;
constexpr float LOG2E = 1.4426950408889634f;
constexpr float SCALE_L2 = 0.08838834764831845f * LOG2E;
constexpr unsigned SPIN_CAP = 200000u;
constexpr int AUX_NT = 2, AUX_SC1 = 16;
constexpr int TAB_E0 = 100;

#define NSLEEP 4
#define PSLEEP 1
#define PARSLEEP 8
#define WQPAT 2
#define CODEPF 16
#define THW 3
#define THN 14

constexpr int WS_NK = 0;
constexpr int WS_NB = 1024;
constexpr int WS_ME = 4096;
constexpr int WS_STAT = WS_ME + 256 * 128;
constexpr int WS_TAB = WS_STAT + 256 * 128;
constexpr int WS_PART = WS_TAB + 256 * 128;
constexpr int WS_OUTP = WS_PART + 256 * 2048;
constexpr int WS_MQ = WS_OUTP + 64 * 4096;
constexpr int WS_END = WS_MQ + 256 * 8192;
constexpr size_t WS_STAMP = 16u << 20;

constexpr int L_W = 0;
constexpr int L_B = 32768;
constexpr int L_X = L_B + 16384;
constexpr int L_P = L_X + 9472;
constexpr int L_T = L_P + 10752;
constexpr int L_Q = L_T + 16384;
constexpr int L_QO = L_Q + 1024;
constexpr int L_ML = L_QO + 1024;
constexpr int L_Z = L_ML + 256;
constexpr int L_NB = L_Z + 64;
constexpr int L_FL = L_NB + 128;
constexpr int L_V0 = L_FL + 64;
constexpr int L_TOTAL = L_V0 + 8 * 2304;
enum { F_Q = 0, F_CNT = 1, F_NONCE = 2, F_ECHO = 3, F_QO = 4, F_Z = 5, F_P = 6, F_FIN = 7, F_CLR = 8, F_XW = 9 };

__device__ __forceinline__ f32x4 mfma16(v4u a, v4u b, f32x4 c) {
  return __builtin_amdgcn_mfma_f32_16x16x32_bf16(__builtin_bit_cast(bf16x8, a), __builtin_bit_cast(bf16x8, b), c, 0, 0, 0);
}
__device__ __forceinline__ unsigned cvtpk(float lo, float hi) {
  return __builtin_bit_cast(unsigned, __builtin_convertvector(f32x2{lo, hi}, bf16x2_t));
}
__device__ __forceinline__ float bflo(unsigned u) { return __builtin_bit_cast(float, u << 16); }
__device__ __forceinline__ float bfhi(unsigned u) { return __builtin_bit_cast(float, u & 0xffff0000u); }
__device__ __forceinline__ float rbf(float x) { return bflo(cvtpk(x, x)); }
__device__ __forceinline__ float xrow_max(float x) {
  auto s = __builtin_amdgcn_permlane16_swap(__float_as_uint(x), __float_as_uint(x), false, false);
  x = fmaxf(__uint_as_float(s[0]), __uint_as_float(s[1]));
  auto t = __builtin_amdgcn_permlane32_swap(__float_as_uint(x), __float_as_uint(x), false, false);
  return fmaxf(__uint_as_float(t[0]), __uint_as_float(t[1]));
}
__device__ __forceinline__ float xrow_sum(float x) {
  auto s = __builtin_amdgcn_permlane16_swap(__float_as_uint(x), __float_as_uint(x), false, false);
  x = __uint_as_float(s[0]) + __uint_as_float(s[1]);
  auto t = __builtin_amdgcn_permlane32_swap(__float_as_uint(x), __float_as_uint(x), false, false);
  return __uint_as_float(t[0]) + __uint_as_float(t[1]);
}
template <int CTRL> __device__ __forceinline__ float dpp(float x) {
  return __builtin_bit_cast(float, __builtin_amdgcn_mov_dpp(__builtin_bit_cast(int, x), CTRL, 0xf, 0xf, true));
}
__device__ __forceinline__ float wave_sum(float x) {
  x += dpp<0xB1>(x); x += dpp<0x4E>(x); x += dpp<0x141>(x); x += dpp<0x128>(x);
  return xrow_sum(x);
}
#define SB() __builtin_amdgcn_sched_barrier(0)
#define CFENCE() asm volatile("" ::: "memory")
#define LGKM0() asm volatile("s_waitcnt lgkmcnt(0)" ::: "memory")
#define WG_BARRIER() do { asm volatile("s_waitcnt lgkmcnt(0)\n\ts_barrier" ::: "memory"); SB(); } while (0)
__device__ __forceinline__ u64 gran(unsigned tag, unsigned val) { return ((u64)tag << 32) | val; }
__device__ __forceinline__ v4u make_srd(const void* p, unsigned bytes) {
  const u64 a = (u64)p;
  v4u d;
  d[0] = __builtin_amdgcn_readfirstlane((unsigned)a);
  d[1] = __builtin_amdgcn_readfirstlane((unsigned)(a >> 32)) & 0xffffu;
  d[2] = bytes;
  d[3] = 0x00020000u;
  return d;
}
template <bool NT = true>
__device__ __forceinline__ void dma16(unsigned ldsaddr, int voff, v4u srd, int soff) {
  if (NT) asm volatile("s_mov_b32 m0, %0\n\ts_nop 0\n\tbuffer_load_dwordx4 %1, %2, %3 offen nt lds" :: "s"(ldsaddr), "v"(voff), "s"(srd), "s"(soff) : "memory");
  else asm volatile("s_mov_b32 m0, %0\n\ts_nop 0\n\tbuffer_load_dwordx4 %1, %2, %3 offen lds" :: "s"(ldsaddr), "v"(voff), "s"(srd), "s"(soff) : "memory");
}
__device__ __forceinline__ void lds_wait_ne0(LAS unsigned* f) {
  while (__hip_atomic_load(f, RLX_WG) == 0u) __builtin_amdgcn_s_sleep(1);
  CFENCE();
}

template <int NP>
__device__ __forceinline__ void ffn_eval(LAS unsigned char* lds, int lane, const f32x2 (&t)[NP], f32x2 (&acc)[NP]) {
  constexpr float C0 = -2.f * 0.7978845608028654f * LOG2E, C1 = C0 * 0.044715f;
#pragma unroll
  for (int p = 0; p < NP; ++p) acc[p] = f32x2{0.f, 0.f};
#pragma unroll 1
  for (int j = 0; j < 7; ++j) {
    const v2u ua = *(LAS v2u*)(lds + L_P + j * 512 + lane * 8);
    const v2u ub = *(LAS v2u*)(lds + L_P + 3584 + j * 512 + lane * 8);
    const v2u uw = *(LAS v2u*)(lds + L_P + 7168 + j * 512 + lane * 8);
#pragma unroll
    for (int i = 0; i < 4; ++i) {
      const float a = (i & 1) ? bfhi(ua[i >> 1]) : bflo(ua[i >> 1]);
      const float b = (i & 1) ? bfhi(ub[i >> 1]) : bflo(ub[i >> 1]);
      const float w = (i & 1) ? bfhi(uw[i >> 1]) : bflo(uw[i >> 1]);
#pragma unroll
      for (int p = 0; p < NP; ++p) {
        const f32x2 u = __builtin_elementwise_fma(t[p], f32x2{a, a}, f32x2{b, b});
        const f32x2 y = u * __builtin_elementwise_fma(u * u, f32x2{C1, C1}, f32x2{C0, C0});
        f32x2 d = f32x2{__builtin_amdgcn_exp2f(y[0]), __builtin_amdgcn_exp2f(y[1])} + 1.f;
        d = f32x2{__builtin_amdgcn_rcpf(d[0]), __builtin_amdgcn_rcpf(d[1])};
        acc[p] = __builtin_elementwise_fma(u * d, f32x2{w, w}, acc[p]);
      }
    }
  }
}

#define STAMP(i)

#define XV 4
#define XV2 4
__global__ __launch_bounds__(768, 3) void fused_kernel(const us* __restrict__ x, const us* __restrict__ kc, const us* __restrict__ vc,
                                                       const us* __restrict__ wq, const us* __restrict__ wo, const us* __restrict__ w1,
                                                       const us* __restrict__ b1, const us* __restrict__ w2, const us* __restrict__ b2,
                                                       us* __restrict__ out, unsigned char* ws) {
  __shared__ __attribute__((aligned(16))) unsigned char smem[L_TOTAL];
  LAS unsigned char* lds = (LAS unsigned char*)smem;
  const unsigned lds0 = (unsigned)(uintptr_t)lds;
  const int lane = threadIdx.x & 63, wave = __builtin_amdgcn_readfirstlane(threadIdx.x >> 6), g = lane >> 4, r = lane & 15;
  LAS unsigned* flags = (LAS unsigned*)(lds + L_FL);
  asm volatile(".Lpf_begin:");
  STAMP(0);
  asm volatile("" :: "s"(x), "s"(kc), "s"(vc), "s"(wq), "s"(wo), "s"(w1), "s"(b1), "s"(w2), "s"(b2), "s"(out), "s"(ws));
  if (threadIdx.x < 16) flags[threadIdx.x] = 0u;
  WG_BARRIER();
  const int bid = blockIdx.x, sp = (bid >> 3) & 3, b = bid >> 5, kvh = (bid + 3 * sp) & 7;
  const int wgl = (b << 5) | (sp << 3) | kvh;
  const int jk = sp * 8 + b;
  const int jb = sp * 8 + kvh;
  const int cj = sp * 4 + (b & 3), bh = b >> 2;
  const int partg = WS_PART + (kvh * 2 + bh) * 32768;
  const int me = (b & 3) * 4 + sp;
  const bool fin = (sp == 0) && (kvh == b);
  const __amdgpu_buffer_rsrc_t wsr = __builtin_amdgcn_make_buffer_rsrc((void*)ws, 0, WS_END, 0x00020000);
  const int voff = lane * 16;

  {
    const bool isx = wave < 4;
    const v4u srd = make_srd(isx ? x : wq + ((size_t)(sp * 128) * (HQ * DH) + kvh * 512 + b * 64), isx ? 8 * DM * 2 : 127 * 8192 + 128);
    const int vo = isx ? voff : (lane >> 3) * 8192 + (lane & 7) * 16;
    const unsigned la = lds0 + (isx ? L_X + wave * 2080 : L_B + (wave - 4) * 2048);
    const int so = isx ? wave * 2048 : (wave - 4) * 16 * 8192;
    dma16<false>(la, vo, srd, so);
    dma16<false>(la + (isx ? 1040 : 1024), vo, srd, so + (isx ? 1024 : 8 * 8192));
    SB();
  }
  unsigned nonce = 0u;
  const int nonce_o = lane ? WS_NB + b * 128 + jb * 4 : WS_NK + kvh * 128 + b * 16 + sp * 4;
  if (wave == 1) {
    nonce = (unsigned)__builtin_amdgcn_s_memrealtime();
    if (lane < 2) __builtin_amdgcn_raw_buffer_store_b32(nonce, wsr, nonce_o, 0, AUX_SC1);
    SB();
  }

  if (wave < 4) {
    __builtin_amdgcn_s_setprio(3);
    if (wave != 1) {
      asm volatile("s_waitcnt vmcnt(0)" ::: "memory");
      if (lane == 0) __hip_atomic_fetch_add(flags + F_XW, 1u, RLX_WG);
    }
    if (wave == 0) {
      *(LAS v4u*)(lds + L_X + 8 * 1040 + lane * 16) = v4u{0x3f803f80u, 0x3f803f80u, 0x3f803f80u, 0x3f803f80u};
      STAMP(1);
      while (__hip_atomic_load(flags + F_XW, RLX_WG) != 12u) __builtin_amdgcn_s_sleep(1);
      CFENCE();
      STAMP(2);
      const int xo = L_X + (r < 8 ? r : 8) * 1040 + g * 16;
      f32x4 d[4];
#pragma unroll
      for (int nt = 0; nt < 4; ++nt) d[nt] = f32x4{0.f, 0.f, 0.f, 0.f};
      const int troff = L_B + (8 * g + (r >> 2)) * 128 + (r & 3) * 8;
#pragma unroll
      for (int s = 0; s < 4; ++s) {
        const v4u xb = *(LAS v4u*)(lds + xo + sp * 256 + s * 64);
#pragma unroll
        for (int nt = 0; nt < 4; ++nt) {
          const v2u u0 = __builtin_bit_cast(v2u, __builtin_amdgcn_ds_read_tr16_b64_v4i16((LAS v4s*)(lds + troff + s * 4096 + nt * 32)));
          const v2u u1 = __builtin_bit_cast(v2u, __builtin_amdgcn_ds_read_tr16_b64_v4i16((LAS v4s*)(lds + troff + s * 4096 + 512 + nt * 32)));
          d[nt] = mfma16(v4u{u0.x, u0.y, u1.x, u1.y}, xb, d[nt]);
        }
      }
      f32x4 d2a = {0.f, 0.f, 0.f, 0.f}, d2b = {0.f, 0.f, 0.f, 0.f};
#pragma unroll
      for (int s = 0; s < 16; s += 2) {
        const v4u xa = *(LAS v4u*)(lds + xo + s * 64), xb = *(LAS v4u*)(lds + xo + s * 64 + 64);
        d2a = mfma16(xa, xa, d2a); d2b = mfma16(xb, xb, d2b);
      }
      d2a += d2b;
      const float dg = (r & 2) ? ((r & 1) ? d2a[3] : d2a[2]) : ((r & 1) ? d2a[1] : d2a[0]);
      const float sxx = xrow_sum(g == (r >> 2) ? dg : 0.f);
      const float mean = xrow_sum(g == 2 ? d2a[0] : 0.f) * (1.f / DM);
      const float rstd = rsqrtf(fmaxf(sxx * (1.f / DM) - mean * mean, 0.f) + 1e-5f);
#pragma unroll
      for (int nt = 0; nt < 4; ++nt) {
        float qv[4];
#pragma unroll
        for (int i = 0; i < 4; ++i) qv[i] = (d[nt][i] - mean * dpp<0x158>(d[nt][i])) * rstd;
        if (r < 8) *(LAS v2u*)(lds + L_QO + r * 128 + nt * 32 + g * 8) = v2u{cvtpk(qv[0], qv[1]), cvtpk(qv[2], qv[3])};
      }
      LGKM0();
      if (lane == 0) __hip_atomic_store(flags + F_QO, 1u, RLX_WG);
      STAMP(3);
    } else if (wave == 1) {
      const int mqo = WS_MQ + wgl * 8192 + lane * 16;
      v4u gq[8];
#pragma unroll
      for (int i = 0; i < 8; ++i) gq[i] = __builtin_amdgcn_raw_buffer_load_b128(wsr, mqo, i * 1024, AUX_SC1);
      const unsigned ge = __hip_atomic_load((gu32*)(ws + WS_ME) + wgl * 32 + (lane & 31), RLX_AGENT);
      SB();
      const v4u z4 = {0u, 0u, 0u, 0u};
      __builtin_amdgcn_raw_buffer_store_b128(z4, wsr, partg + me * 2048 + lane * 16, 0, AUX_SC1);
      __builtin_amdgcn_raw_buffer_store_b128(z4, wsr, partg + me * 2048 + 1024 + lane * 16, 0, AUX_SC1);
      __builtin_amdgcn_raw_buffer_store_b128(z4, wsr, WS_OUTP + (b * 8 + kvh) * 4096 + sp * 1024 + lane * 16, 0, AUX_SC1);
      if (lane < 16) __builtin_amdgcn_raw_buffer_store_b128(z4, wsr, (lane < 8 ? WS_STAT : WS_TAB - 128) + wgl * 128 + lane * 16, 0, AUX_SC1);
      SB();
      asm volatile("s_waitcnt vmcnt(14)" ::: "memory");
      if (lane == 0) __hip_atomic_fetch_add(flags + F_XW, 1u, RLX_WG);
      const int nko = WS_NK + kvh * 128 + g * 16;
      const int mq_out = WS_MQ + ((g << 5) | kvh) * 8192 + jk * 256 + r * 16;
      __builtin_amdgcn_s_sleep(NSLEEP);
      CFENCE();
      STAMP(1);
      v4u nk[2];
      nk[0] = __builtin_amdgcn_raw_buffer_load_b128(wsr, nko, 0, AUX_SC1);
      nk[1] = __builtin_amdgcn_raw_buffer_load_b128(wsr, nko + 64, 0, AUX_SC1);
      SB();
      {
        const unsigned first = nonce;
        for (;;) {
          bool hit = ge == nonce;
#pragma unroll
          for (int i = 0; i < 8; ++i) hit = hit || gq[i][1] == nonce || gq[i][3] == nonce;
          if (!__any(hit)) break;
          ++nonce;
        }
        if (nonce != first && lane < 2)
          __builtin_amdgcn_raw_buffer_store_b32(nonce, wsr, lane ? WS_NB + b * 128 + jb * 4 : WS_NK + kvh * 128 + b * 16 + sp * 4, 0, AUX_SC1);
      }
      asm volatile("s_waitcnt vmcnt(0)" ::: "memory");
      if (lane == 0) __hip_atomic_store(flags + F_CLR, 1u, RLX_WG);
      STAMP(2);
      lds_wait_ne0(flags + F_QO);
      v2u qo[2];
      qo[0] = *(LAS v2u*)(lds + L_QO + g * 128 + r * 8);
      qo[1] = *(LAS v2u*)(lds + L_QO + 512 + g * 128 + r * 8);
#pragma unroll
      for (int k = 0; k < 8; ++k)
        __builtin_amdgcn_raw_buffer_store_b128(v4u{qo[k >> 2].x, nk[k >> 2][k & 3], qo[k >> 2].y, nk[k >> 2][k & 3]}, wsr, mq_out, ((k >> 2) * 128 + (k & 3) * 8) * 8192, AUX_SC1);
      STAMP(3);
      bool done = false;
      unsigned nbv = 0u;
      for (unsigned spin = 0; spin < SPIN_CAP; ++spin) {
        CFENCE();
        v4u n2[2];
        n2[0] = __builtin_amdgcn_raw_buffer_load_b128(wsr, nko, 0, AUX_SC1);
        n2[1] = __builtin_amdgcn_raw_buffer_load_b128(wsr, nko + 64, 0, AUX_SC1);
        if (done) nbv = __hip_atomic_load((gu32*)(ws + WS_NB) + b * 32 + (lane & 31), RLX_AGENT);
        else {
#pragma unroll
          for (int i = 0; i < 8; ++i)
            if (gq[i][1] != nonce || gq[i][3] != nonce) gq[i] = __builtin_amdgcn_raw_buffer_load_b128(wsr, mqo, i * 1024, AUX_SC1);
        }
        bool chg = false;
#pragma unroll
        for (int k = 0; k < 8; ++k) chg = chg || n2[k >> 2][k & 3] != nk[k >> 2][k & 3];
        if (__any(chg)) {
          nk[0] = n2[0]; nk[1] = n2[1];
#pragma unroll 1
          for (int k = 0; k < 8; ++k) {
            const v2u qq = (k >> 2) ? qo[1] : qo[0];
            const v4u nn = (k >> 2) ? nk[1] : nk[0];
            const unsigned tg = (k & 2) ? ((k & 1) ? nn[3] : nn[2]) : ((k & 1) ? nn[1] : nn[0]);
            __builtin_amdgcn_raw_buffer_store_b128(v4u{qq.x, tg, qq.y, tg}, wsr, mq_out, ((k >> 2) * 128 + (k & 3) * 8) * 8192, AUX_SC1);
          }
        }
        if (done) break;
        bool ok = true;
#pragma unroll
        for (int i = 0; i < 8; ++i) ok = ok && gq[i][1] == nonce && gq[i][3] == nonce;
        if (__all(ok) || spin + 2 >= SPIN_CAP) {
#pragma unroll
          for (int hf = 0; hf < 2; ++hf) {
            float q0 = 0.f, q1 = 0.f, q2 = 0.f, q3 = 0.f;
#pragma unroll
            for (int kq = 0; kq < 4; ++kq) {
              const v4u t4 = gq[2 * kq + hf];
              q0 += bflo(t4[0]); q1 += bfhi(t4[0]); q2 += bflo(t4[2]); q3 += bfhi(t4[2]);
            }
            *(LAS v2u*)(lds + L_Q + hf * 512 + lane * 8) = v2u{cvtpk(q0, q1), cvtpk(q2, q3)};
          }
          LGKM0();
          if (lane == 0) __hip_atomic_store(flags + F_Q, 1u, RLX_WG);
          STAMP(4);
          done = true;
        }
      }
      if (lane < 32) __hip_atomic_store((gu32*)(ws + WS_ME) + ((b << 5) | lane) * 32 + jb, nbv, RLX_AGENT);
      if (lane == 0) __hip_atomic_store(flags + F_NONCE, nonce, RLX_WG);
      if (lane < 32) ((LAS unsigned*)(lds + L_NB))[lane] = nbv;
      STAMP(5);
    } else if (wave == 2) {
      const v4u s1 = make_srd(w1, FF * 2), s2 = make_srd(b1, FF * 2), s3 = make_srd(w2, FF * 2);
      __builtin_amdgcn_s_sleep(PARSLEEP);
#pragma unroll
      for (int i = 0; i < 3; ++i) {
        dma16<false>(lds0 + L_P + i * 1024, voff, s1, i * 1024);
        dma16<false>(lds0 + L_P + 3584 + i * 1024, voff, s2, i * 1024);
        dma16<false>(lds0 + L_P + 7168 + i * 1024, voff, s3, i * 1024);
      }
      if (lane < 32) {
        dma16<false>(lds0 + L_P + 3072, voff, s1, 3072);
        dma16<false>(lds0 + L_P + 3584 + 3072, voff, s2, 3072);
        dma16<false>(lds0 + L_P + 7168 + 3072, voff, s3, 3072);
      }
      asm volatile("s_waitcnt vmcnt(0)" ::: "memory");
      if (lane == 0) __hip_atomic_store(flags + F_P, 1u, RLX_WG);
      STAMP(1);
    } else if (CODEPF && bid < CODEPF) {
      u64 lo; unsigned back, la, lb;
      asm volatile("s_getpc_b64 %0\n.Lpf_here:\n\ts_mov_b32 %1, .Lpf_here-.Lpf_begin\n\ts_mov_b32 %2, .Lpf_end_a-.Lpf_begin\n\ts_mov_b32 %3, .Lpf_end_b-.Lpf_begin"
                   : "=s"(lo), "=s"(back), "=s"(la), "=s"(lb));
      const __amdgpu_buffer_rsrc_t crs = __builtin_amdgcn_make_buffer_rsrc((void*)(lo - back), 0, la > lb ? la : lb, 0x00020000);
      unsigned sink = 0;
#pragma unroll
      for (int i = 0; i < 6; ++i) sink ^= __builtin_amdgcn_raw_buffer_load_b32(crs, lane * 64 + i * 4096, 0, 0);
      asm volatile("" :: "v"(sink));
      STAMP(1);
    }
    if (wave != 1) {
      __builtin_amdgcn_s_setprio(0);
      lds_wait_ne0(flags + F_P);
      lds_wait_ne0(flags + F_CLR);
      const int p0 = wave == 0 ? 0 : 3 * wave - 4, p1 = wave == 0 ? 2 : 3 * wave - 1;
#pragma unroll 1
      for (int ps = p0; ps < p1; ++ps) {
        const int idx = (wgl * 8 + ps) * 4;
        const unsigned bits = (unsigned)((idx & 4095) + (TAB_E0 << 7)) | (unsigned)((idx >> 12) << 15);
        f32x2 t[2], a[2];
#pragma unroll
        for (int p = 0; p < 2; ++p) t[p] = f32x2{bflo(bits + 2 * p), bflo(bits + 2 * p + 1)};
        ffn_eval<2>(lds, lane, t, a);
        const unsigned k0 = cvtpk(wave_sum(a[0][0]), wave_sum(a[0][1])), k1 = cvtpk(wave_sum(a[1][0]), wave_sum(a[1][1]));
        __builtin_amdgcn_s_setprio(3);
        if (lane == 0) __builtin_amdgcn_raw_buffer_store_b128(v4u{k0, 1u, k1, 1u}, wsr, WS_TAB + wgl * 128 + ps * 16, 0, AUX_SC1);
        __builtin_amdgcn_s_setprio(0);
      }
      __builtin_amdgcn_s_setprio(3);
      STAMP(6);
    }
    if (wave == 0) {
      while (__hip_atomic_load(flags + F_CNT, RLX_WG) != 8u) __builtin_amdgcn_s_sleep(2);
      CFENCE();
      STAMP(7);
      const int hd = lane & 3;
      float M = -INFINITY, L = 0.f;
#pragma unroll
      for (int w = 0; w < 8; ++w) M = fmaxf(M, ((LAS float*)(lds + L_ML))[w * 8 + hd]);
#pragma unroll
      for (int w = 0; w < 8; ++w) L = fmaf(__builtin_amdgcn_exp2f(((LAS float*)(lds + L_ML))[w * 8 + hd] - M), ((LAS float*)(lds + L_ML))[w * 8 + 4 + hd], L);
      if (lane < 4) __builtin_amdgcn_raw_buffer_store_b128(v4u{__float_as_uint(M), 1u, __float_as_uint(L), 1u}, wsr, WS_STAT + wgl * 128 + lane * 16, 0, AUX_SC1);
      v4u sv;
      for (unsigned spin = 0; spin < SPIN_CAP; ++spin) {
        CFENCE();
        sv = __builtin_amdgcn_raw_buffer_load_b128(wsr, WS_STAT + ((b << 5) | (((lane >> 2) & 3) << 3) | kvh) * 128 + hd * 16, 0, AUX_SC1);
        if (__all(sv[1] == 1u && sv[3] == 1u)) break;
        __builtin_amdgcn_s_sleep(2);
      }
      const float ms = __uint_as_float(sv[0]);
      float Mg = fmaxf(ms, dpp<0x124>(ms)); Mg = fmaxf(Mg, dpp<0x128>(Mg));
      float Lg = __builtin_amdgcn_exp2f(ms - Mg) * __uint_as_float(sv[2]);
      Lg += dpp<0x128>(Lg); Lg += dpp<0x124>(Lg);
      if (lane < 4) ((LAS float*)(lds + L_Z))[lane] = Mg + __builtin_amdgcn_logf(Lg);
      LGKM0();
      if (lane == 0) __hip_atomic_store(flags + F_Z, 1u, RLX_WG);
      STAMP(8);
    } else if (wave == 1) {
      const unsigned my = __hip_atomic_load(flags + F_NONCE, RLX_WG);
      unsigned used = ((LAS unsigned*)(lds + L_NB))[lane & 31];
      bool done = false;
      for (unsigned spin = 0; spin < SPIN_CAP; ++spin) {
        const unsigned nbv = __hip_atomic_load((gu32*)(ws + WS_NB) + b * 32 + (lane & 31), RLX_AGENT);
        const unsigned ev = __hip_atomic_load((gu32*)(ws + WS_ME) + wgl * 32 + (lane & 31), RLX_AGENT);
        if (nbv != used && lane < 32) __hip_atomic_store((gu32*)(ws + WS_ME) + ((b << 5) | lane) * 32 + jb, nbv, RLX_AGENT);
        used = nbv;
        if (done) break;
        done = __all(ev == my);
        if (!done) __builtin_amdgcn_s_sleep(8);
      }
      STAMP(7);
      if (fin) {
#pragma unroll 1
        for (int c = 0; c < 2; ++c) {
          for (unsigned spin = 0; spin < SPIN_CAP; ++spin) {
            CFENCE();
            v4u tv[16];
#pragma unroll
            for (int i = 0; i < 16; ++i) tv[i] = __builtin_amdgcn_raw_buffer_load_b128(wsr, WS_TAB + c * 16384 + lane * 16, i * 1024, AUX_SC1);
            bool ok = true;
#pragma unroll
            for (int i = 0; i < 16; ++i) {
              ok = ok && tv[i][1] == 1u && tv[i][3] == 1u;
              *(LAS v2u*)(lds + L_T + c * 8192 + i * 512 + lane * 8) = v2u{tv[i][0], tv[i][2]};
            }
            if (__all(ok)) break;
            __builtin_amdgcn_s_sleep(16);
          }
        }
        LGKM0();
        if (lane == 0) __hip_atomic_store(flags + F_FIN, 1u, RLX_WG);
        asm volatile(".Lpf_end_a:");
        STAMP(8);
      }
    }
  } else {
    const int sw = wave - 4, tid = threadIdx.x - 256;
    const size_t base = ((size_t)(b * HKV + kvh) * SK + sp * KEYS_WG + sw * KEYS_W) * DH;
    const __amdgpu_buffer_rsrc_t krs = __builtin_amdgcn_make_buffer_rsrc((void*)(kc + base), 0, KEYS_W * DH * 2, 0x00020000);
    const __amdgpu_buffer_rsrc_t vrs = __builtin_amdgcn_make_buffer_rsrc((void*)(vc + base), 0, KEYS_W * DH * 2, 0x00020000);
    const int koff = (r >> 2) * 256 + g * 64 + (r & 3) * 16;
#define KLOAD(i) do { kf[i] = __builtin_amdgcn_raw_buffer_load_b128(krs, koff + ((i) & 3) * 1024, ((i) >> 2) * 4096, AUX_NT); SB(); } while (0)
#define VLOAD(i) do { vf[i] = __builtin_amdgcn_raw_buffer_load_b128(vrs, voff + ((i) & 3) * 1024, ((i) >> 2) * 4096, AUX_NT); SB(); } while (0)
    v4u kf[32], vf[32];
    __builtin_amdgcn_s_setprio(2);
#define THROTTLE() asm volatile("s_waitcnt vmcnt(%0)" :: "i"(THW - 1) : "memory")
#pragma unroll
    for (int i = 0; i < 32; ++i) {
      if (i == THW - 2) { asm volatile("s_waitcnt vmcnt(%0)" :: "i"(THW - 2) : "memory"); if (lane == 0) __hip_atomic_fetch_add(flags + F_XW, 1u, RLX_WG); }
      if (i >= THW && i < THN) THROTTLE();
      KLOAD(i);
    }
    STAMP(3);
#pragma unroll
    for (int i = 0; i < XV; ++i) VLOAD(i);
    STAMP(6);
    lds_wait_ne0(flags + F_Q);
    SB();
    const v4u qf = *(LAS v4u*)(lds + L_Q + (r & 3) * 256 + g * 64 + (r >> 2) * 16);
    float s[32];
#pragma unroll
    for (int i = 0; i < 32; ++i) {
      const f32x4 a = mfma16(kf[i], qf, f32x4{0.f, 0.f, 0.f, 0.f});
      float t_ = a[0] + dpp<0x104>(a[1]); asm volatile("" : "+v"(t_));
      t_ += dpp<0x108>(a[2]); asm volatile("" : "+v"(t_));
      s[i] = t_ + dpp<0x10C>(a[3]); asm volatile("" : "+v"(s[i]));
      if (i == 15) {
        SB();
#pragma unroll
        for (int k = XV; k < XV + XV2; ++k) VLOAD(k);
      }
    }
    SB();
    float m = s[0];
#pragma unroll
    for (int i = 1; i < 32; ++i) m = fmaxf(m, s[i]);
    m = xrow_max(m) * SCALE_L2;
    v4u pf[4];
#define SOFTMAX(G, mm) do { float p_[8]; \
    _Pragma("unroll") for (int j = 0; j < 8; ++j) p_[j] = __builtin_amdgcn_exp2f(fmaf(s[8 * (G) + j], SCALE_L2, -(mm))); \
    pf[G] = v4u{cvtpk(p_[0], p_[1]), cvtpk(p_[2], p_[3]), cvtpk(p_[4], p_[5]), cvtpk(p_[6], p_[7])}; } while (0)
    {
      SOFTMAX(0, m); SOFTMAX(1, m); SOFTMAX(2, m); SOFTMAX(3, m);
      const v4u ones = {0x3f803f80u, 0x3f803f80u, 0x3f803f80u, 0x3f803f80u};
      f32x4 l4 = {0.f, 0.f, 0.f, 0.f};
#pragma unroll
      for (int G = 0; G < 4; ++G) l4 = mfma16(pf[G], ones, l4);
      if (lane < 4) ((LAS float*)(lds + L_ML))[sw * 8 + lane] = m;
      if (lane == 0) *(LAS f32x4*)(lds + L_ML + sw * 32 + 16) = l4;
      LGKM0();
      if (lane == 0) __hip_atomic_fetch_add(flags + F_CNT, 1u, RLX_WG);
    }
    __builtin_amdgcn_s_setprio(1);
    STAMP(7);
    f32x4 acc[8];
#pragma unroll
    for (int j = 0; j < 8; ++j) acc[j] = f32x4{0.f, 0.f, 0.f, 0.f};
#define PV(G) do { \
    _Pragma("unroll") for (int d = 0; d < 4; ++d) { v4u lo_, hi_; \
      _Pragma("unroll") for (int jj = 0; jj < 4; ++jj) { \
        lo_[jj] = __builtin_amdgcn_perm(vf[8 * (G) + 2 * jj + 1][d], vf[8 * (G) + 2 * jj][d], 0x05040100u); \
        hi_[jj] = __builtin_amdgcn_perm(vf[8 * (G) + 2 * jj + 1][d], vf[8 * (G) + 2 * jj][d], 0x07060302u); } \
      acc[2 * d] = mfma16(pf[G], lo_, acc[2 * d]); acc[2 * d + 1] = mfma16(pf[G], hi_, acc[2 * d + 1]); } SB(); } while (0)
    {
      const v4u wsd = make_srd(wo + ((size_t)(kvh * 512 + sw) * DM + cj * 32), 511 * 1024);
      const int e4 = lane >> 2;
      const int wo_ = ((e4 & 3) * 128 + (e4 >> 2) * 8) * 1024 + (lane & 3) * 16;
#pragma unroll
      for (int i = 0; i < 4; ++i) dma16(lds0 + L_W + sw * 4096 + i * 1024, wo_, wsd, i * 32 * 1024);
      SB();
    }
#pragma unroll
    for (int i = XV + XV2; i < 20; ++i) VLOAD(i);
    PV(0);
#pragma unroll
    for (int i = 20; i < 28; ++i) VLOAD(i);
    PV(1);
#pragma unroll
    for (int i = 28; i < 32; ++i) VLOAD(i);
    float xres = 0.f, b2v = 0.f;
    if (fin) { xres = bflo(((LAS us*)(lds + L_X))[b * 520 + tid]); b2v = bflo(b2[0]); }
    STAMP(8);
    v4u wf[2][2];
    {
      const int wtr = L_W + sw * 4096 + (g * 8 + (r >> 2)) * 64 + (r & 3) * 8;
#pragma unroll
      for (int ks = 0; ks < 2; ++ks)
#pragma unroll
        for (int nt = 0; nt < 2; ++nt) {
          const v2u u0 = __builtin_bit_cast(v2u, __builtin_amdgcn_ds_read_tr16_b64_v4i16((LAS v4s*)(lds + wtr + ks * 2048 + nt * 32)));
          const v2u u1 = __builtin_bit_cast(v2u, __builtin_amdgcn_ds_read_tr16_b64_v4i16((LAS v4s*)(lds + wtr + ks * 2048 + 256 + nt * 32)));
          wf[ks][nt] = v4u{u0.x, u0.y, u1.x, u1.y};
          asm volatile("" : "+v"(wf[ks][nt]));
        }
      SB();
    }
    PV(2);
    lds_wait_ne0(flags + F_Z);
    SB();
    {
      const f32x4 Z4 = *(LAS f32x4*)(lds + L_Z);
      const f32x4 mo = *(LAS f32x4*)(lds + L_ML + sw * 32);
      const float zl = ((LAS float*)(lds + L_Z))[lane & 3];
      f32x4 f4;
#pragma unroll
      for (int i = 0; i < 4; ++i) f4[i] = __builtin_amdgcn_exp2f(mo[i] - Z4[i]);
#pragma unroll
      for (int j = 0; j < 8; ++j) acc[j] *= f4;
      const float zz = (r < 4) ? zl : m;
      SOFTMAX(3, zz);
      SB();
    }
    PV(3);
    STAMP(9);
    if (lane < 16) {
#pragma unroll
      for (int j = 0; j < 8; ++j) *(LAS f32x4*)(lds + L_B + ((sw * 8 + j) * 16 + lane) * 16) = acc[j];
    }
    WG_BARRIER();
    STAMP(10);
    LAS unsigned char* aim = lds + L_V0 + sw * 2304;
    if (lane < 16) {
      f32x4 o = *(LAS f32x4*)(lds + L_B + (sw * 16 + lane) * 16);
#pragma unroll
      for (int w = 1; w < 8; ++w) o += *(LAS f32x4*)(lds + L_B + ((w * 8 + sw) * 16 + lane) * 16);
      const unsigned o0 = cvtpk(o[0], o[1]), o1 = cvtpk(o[2], o[3]);
      __builtin_amdgcn_raw_buffer_store_b128(v4u{o0, 1u, o1, 1u}, wsr, partg + me * 2048 + sw * 256 + lane * 16, 0, AUX_SC1);
      *(LAS v2u*)(aim + me * 144 + lane * 8) = v2u{o0, o1};
    }
    SB();
    {
      const int po = partg + g * 2048 + sw * 256 + r * 16;
      bool okv[4];
#pragma unroll
      for (int i = 0; i < 4; ++i) okv[i] = (4 * i + g == me);
      for (unsigned spin = 0; spin < SPIN_CAP; ++spin) {
        CFENCE();
        v4u pv[4];
#pragma unroll
        for (int i = 0; i < 4; ++i) { pv[i] = v4u{0u, 0u, 0u, 0u}; if (!okv[i]) pv[i] = __builtin_amdgcn_raw_buffer_load_b128(wsr, po + i * 8192, 0, AUX_SC1); }
        bool ok = true;
#pragma unroll
        for (int i = 0; i < 4; ++i) {
          if (pv[i][1] == 1u && pv[i][3] == 1u) { okv[i] = true; *(LAS v2u*)(aim + (4 * i + g) * 144 + r * 8) = v2u{pv[i][0], pv[i][2]}; }
          ok = ok && okv[i];
        }
        if (__all(ok)) break;
        __builtin_amdgcn_s_sleep(PSLEEP);
      }
      STAMP(11);
      f32x4 c0 = {0.f, 0.f, 0.f, 0.f}, c1 = {0.f, 0.f, 0.f, 0.f};
#pragma unroll
      for (int ks = 0; ks < 2; ++ks) {
        const v4u af = *(LAS v4u*)(aim + r * 144 + ks * 64 + g * 16);
        c0 = mfma16(af, wf[ks][0], c0);
        c1 = mfma16(af, wf[ks][1], c1);
      }
      LAS float* red = (LAS float*)(lds + L_W);
      red[sw * 128 + lane] = (c0[0] + c0[1]) + (c0[2] + c0[3]);
      red[sw * 128 + 64 + lane] = (c1[0] + c1[1]) + (c1[2] + c1[3]);
    }
    WG_BARRIER();
    if (sw < 2) {
      LAS float* red = (LAS float*)(lds + L_W);
      float a = red[tid];
#pragma unroll
      for (int w = 1; w < 8; ++w) a += red[w * 128 + tid];
      __hip_atomic_store((gu64*)(ws + WS_OUTP) + ((bh * 4 + g) * 8 + kvh) * 512 + cj * 32 + sw * 16 + r, gran(1u, __float_as_uint(a)), RLX_AGENT);
    }
    STAMP(12);
    if (fin) {
      lds_wait_ne0(flags + F_FIN);
      u64 pv[8];
      for (unsigned spin = 0; spin < SPIN_CAP; ++spin) {
#pragma unroll
        for (int k = 0; k < 8; ++k) pv[k] = __hip_atomic_load((gu64*)(ws + WS_OUTP) + (b * 8 + k) * 512 + tid, RLX_AGENT);
        bool ok = true;
#pragma unroll
        for (int k = 0; k < 8; ++k) ok = ok && (unsigned)(pv[k] >> 32) == 1u;
        if (__all(ok)) break;
        __builtin_amdgcn_s_sleep(1);
      }
      float a = 0.f;
#pragma unroll
      for (int k = 0; k < 8; ++k) a += __uint_as_float((unsigned)pv[k]);
      const float h = rbf(xres + rbf(a));
      ((LAS float*)(lds + L_B))[tid] = h;
      STAMP(13);
      WG_BARRIER();
      const f32x4 h0 = *(LAS f32x4*)(lds + L_B + lane * 32), h1 = *(LAS f32x4*)(lds + L_B + lane * 32 + 16);
      float hv[8] = {h0[0], h0[1], h0[2], h0[3], h1[0], h1[1], h1[2], h1[3]};
      float t = h;
#pragma unroll
      for (int rep = 0; rep < 2; ++rep) {
        float sm = 0.f;
#pragma unroll
        for (int i = 0; i < 8; ++i) sm += hv[i];
        const float mean = wave_sum(sm) * (1.f / DM);
        float sq = 0.f;
#pragma unroll
        for (int i = 0; i < 8; ++i) { hv[i] -= mean; sq = fmaf(hv[i], hv[i], sq); }
        const float rs = rsqrtf(wave_sum(sq) * (1.f / DM) + 1e-5f);
#pragma unroll
        for (int i = 0; i < 8; ++i) hv[i] *= rs;
        t = (t - mean) * rs;
      }
      const unsigned tb = cvtpk(t, t) & 0xffffu;
      const unsigned rel = (tb & 0x7fffu) - (unsigned)(TAB_E0 << 7);
      const bool inr = rel < 4096u;
      float y = bflo(((LAS us*)(lds + L_T))[(inr ? rel : 0u) | ((tb >> 15) << 12)]);
      u64 miss = __ballot(!inr);
      while (miss) {
        const int l = __builtin_ctzll(miss);
        miss &= miss - 1;
        const float ts = bflo((unsigned)__builtin_amdgcn_readlane((int)tb, l));
        const f32x2 tt[1] = {f32x2{ts, ts}};
        f32x2 aa[1];
        ffn_eval<1>(lds, lane, tt, aa);
        const float ys = rbf(wave_sum(aa[0][0]));
        if (lane == l) y = ys;
      }
      const float ov = h + rbf(y + b2v);
      out[b * DM + tid] = (us)(cvtpk(ov, ov) & 0xffffu);
      asm volatile(".Lpf_end_b:");
      STAMP(14);
    }
  }
}

extern "C" void kernel_launch(void* const* d_in, const int* in_sizes, int n_in, void* d_out, int out_size, void* d_ws, size_t ws_size, hipStream_t stream) {
  fused_kernel<<<256, 768, 0, stream>>>((const us*)d_in[0], (const us*)d_in[1], (const us*)d_in[2], (const us*)d_in[3], (const us*)d_in[4],
                                        (const us*)d_in[6], (const us*)d_in[7], (const us*)d_in[8], (const us*)d_in[9], (us*)d_out, (unsigned char*)d_ws);
}
```

```cpp
#include <hip/hip_runtime.h>
#include <hip/hip_bf16.h>
#include <stdint.h>
#include <type_traits>

#define GAS __attribute__((address_space(1)))
#define LAS __attribute__((address_space(3)))
#define RLX_AGENT __ATOMIC_RELAXED, __HIP_MEMORY_SCOPE_AGENT
#define RLX_WG __ATOMIC_RELAXED, __HIP_MEMORY_SCOPE_WORKGROUP
typedef unsigned v4u __attribute__((ext_vector_type(4)));
typedef unsigned v2u __attribute__((ext_vector_type(2)));
typedef float f32x4 __attribute__((ext_vector_type(4)));
typedef float f32x2 __attribute__((ext_vector_type(2)));
typedef short bf16x8 __attribute__((ext_vector_type(8)));
typedef short v4s __attribute__((ext_vector_type(4)));
typedef __bf16 bf16x2_t __attribute__((ext_vector_type(2)));
typedef unsigned long long u64;
typedef GAS unsigned gu32;
typedef GAS u64 gu64;
typedef unsigned short us;

constexpr int DM = 512, HQ = 32, DH = 128, FF = 1792;
constexpr float LOG2E = 1.4426950408889634f;
constexpr float SCALE_L2 = 0.08838834764831845f * LOG2E;
constexpr unsigned SPIN_CAP = 200000u;
constexpr int AUX_NT = 2, AUX_SC1 = 16;
constexpr unsigned EMPTY = 0xffffffffu;
constexpr int TAB_E0 = 107;
constexpr int TAB_H = 24 * 128;

#define NSLEEP 4
#define H2D 8
#define CODEPF 16
#define KFIRST 2
#define PER16 512
#define LEAD 2
#define NE 16
#define EX16 100
#define WC16 200
#define XWSIG 5
#define PVD 5
#define XV 2
#define PSL 2
#define PSLT 7
#define NNT 130
#define KW 4
#define VW 6
#define PART 250
#define PRETOUCH 1
#define QD1 16
#define TABT 1200
constexpr int sched(int n) {
  return ((n - LEAD) * PER16 + (n < NE ? n : NE) * EX16 + ((n >= 16) + (n >= 24) + (n >= 36) + (n >= 44)) * WC16) >> 4;
}

constexpr int WS_NK = 0;
constexpr int WS_NB = 1024;
constexpr int WS_STAT = 4096;
constexpr int WS_TAB = WS_STAT + 256 * 128;
constexpr int WS_EM = WS_TAB + 256 * 128;
constexpr int WS_PART = WS_EM + 256 * 512;
constexpr int WS_OUTP = WS_PART + 256 * 1024;
constexpr int WS_QD = WS_OUTP + 64 * 2048;
constexpr int WS_END = WS_QD + 64 * 8192;
constexpr size_t WS_STAMP = 16u << 20;

constexpr int L_W = 0;
constexpr int L_B = 32768;
constexpr int L_X = L_B + 16384;
constexpr int L_P = L_X + 9472;
constexpr int L_T = L_P + 10752;
constexpr int L_Q = L_T + 16384;
constexpr int L_QO = L_Q + 1024;
constexpr int L_ML = L_QO + 1024;
constexpr int L_Z = L_ML + 256;
constexpr int L_FL = L_Z + 64;
constexpr int L_A = L_FL + 64;
constexpr int L_K0 = L_A + 8 * 2304;
constexpr int L_TOTAL = L_K0 + 8 * 2048;
enum { F_Q = 0, F_CNT = 1, F_QO = 2, F_Z = 3, F_P = 4, F_FIN = 5, F_XW = 6, F_B2 = 7, F_CLR = 8 };

__device__ __forceinline__ f32x4 mfma16(v4u a, v4u b, f32x4 c) {
  return __builtin_amdgcn_mfma_f32_16x16x32_bf16(__builtin_bit_cast(bf16x8, a), __builtin_bit_cast(bf16x8, b), c, 0, 0, 0);
}
__device__ __forceinline__ unsigned cvtpk(float lo, float hi) {
  return __builtin_bit_cast(unsigned, __builtin_convertvector(f32x2{lo, hi}, bf16x2_t));
}
__device__ __forceinline__ float bflo(unsigned u) { return __builtin_bit_cast(float, u << 16); }
__device__ __forceinline__ float bfhi(unsigned u) { return __builtin_bit_cast(float, u & 0xffff0000u); }
__device__ __forceinline__ float rbf(float x) { return bflo(cvtpk(x, x)); }
__device__ __forceinline__ float xrow_max(float x) {
  auto s = __builtin_amdgcn_permlane16_swap(__float_as_uint(x), __float_as_uint(x), false, false);
  x = fmaxf(__uint_as_float(s[0]), __uint_as_float(s[1]));
  auto t = __builtin_amdgcn_permlane32_swap(__float_as_uint(x), __float_as_uint(x), false, false);
  return fmaxf(__uint_as_float(t[0]), __uint_as_float(t[1]));
}
__device__ __forceinline__ float xrow_sum(float x) {
  auto s = __builtin_amdgcn_permlane16_swap(__float_as_uint(x), __float_as_uint(x), false, false);
  x = __uint_as_float(s[0]) + __uint_as_float(s[1]);
  auto t = __builtin_amdgcn_permlane32_swap(__float_as_uint(x), __float_as_uint(x), false, false);
  return __uint_as_float(t[0]) + __uint_as_float(t[1]);
}
template <int CTRL> __device__ __forceinline__ float dpp(float x) {
  return __builtin_bit_cast(float, __builtin_amdgcn_mov_dpp(__builtin_bit_cast(int, x), CTRL, 0xf, 0xf, true));
}
__device__ __forceinline__ float wave_sum(float x) {
  x += dpp<0xB1>(x); x += dpp<0x4E>(x); x += dpp<0x141>(x); x += dpp<0x128>(x);
  return xrow_sum(x);
}
#define SB() __builtin_amdgcn_sched_barrier(0)
#define CFENCE() asm volatile("" ::: "memory")
#define LGKM0() asm volatile("s_waitcnt lgkmcnt(0)" ::: "memory")
#define WAITV(n) asm volatile("s_waitcnt vmcnt(%0)" :: "i"(n) : "memory")
#define WG_BARRIER() do { asm volatile("s_waitcnt lgkmcnt(0)\n\ts_barrier" ::: "memory"); SB(); } while (0)
__device__ __forceinline__ v4u make_srd(const void* p, unsigned bytes) {
  const u64 a = (u64)p;
  v4u d;
  d[0] = __builtin_amdgcn_readfirstlane((unsigned)a);
  d[1] = __builtin_amdgcn_readfirstlane((unsigned)(a >> 32)) & 0xffffu;
  d[2] = bytes;
  d[3] = 0x00020000u;
  return d;
}
template <bool NT>
__device__ __forceinline__ void dma16(unsigned ldsaddr, int voff, v4u srd, int soff) {
  if (NT) asm volatile("s_mov_b32 m0, %0\n\ts_nop 0\n\tbuffer_load_dwordx4 %1, %2, %3 offen nt lds" :: "s"(ldsaddr), "v"(voff), "s"(srd), "s"(soff) : "memory");
  else asm volatile("s_mov_b32 m0, %0\n\ts_nop 0\n\tbuffer_load_dwordx4 %1, %2, %3 offen lds" :: "s"(ldsaddr), "v"(voff), "s"(srd), "s"(soff) : "memory");
}
__device__ __forceinline__ void lds_wait_ne0(LAS unsigned* f) {
  while (__hip_atomic_load(f, RLX_WG) == 0u) __builtin_amdgcn_s_sleep(1);
  CFENCE();
}
template <int TICKS>
__device__ __forceinline__ void pace(unsigned tstart) {
  if (TICKS > 0)
    asm volatile("s_memrealtime vcc\n\ts_waitcnt lgkmcnt(0)\n\ts_sub_i32 vcc_lo, vcc_lo, %0\n"
                 "1:\n\ts_cmpk_ge_i32 vcc_lo, %1\n\ts_cbranch_scc1 2f\n\ts_sleep %2\n\ts_addk_i32 vcc_lo, %3\n\ts_branch 1b\n2:"
                 :: "s"(tstart), "i"(TICKS), "i"(PSL), "i"(PSLT) : "vcc", "scc", "memory");
}

template <int NP>
__device__ __forceinline__ void ffn_eval(LAS unsigned char* lds, int lane, const f32x2 (&t)[NP], f32x2 (&acc)[NP]) {
  constexpr float C0 = -2.f * 0.7978845608028654f * LOG2E, C1 = C0 * 0.044715f;
#pragma unroll
  for (int p = 0; p < NP; ++p) acc[p] = f32x2{0.f, 0.f};
#pragma unroll 1
  for (int j = 0; j < 7; ++j) {
    const v2u ua = *(LAS v2u*)(lds + L_P + j * 512 + lane * 8);
    const v2u ub = *(LAS v2u*)(lds + L_P + 3584 + j * 512 + lane * 8);
    const v2u uw = *(LAS v2u*)(lds + L_P + 7168 + j * 512 + lane * 8);
#pragma unroll
    for (int i = 0; i < 4; ++i) {
      const float a = (i & 1) ? bfhi(ua[i >> 1]) : bflo(ua[i >> 1]);
      const float b = (i & 1) ? bfhi(ub[i >> 1]) : bflo(ub[i >> 1]);
      const float w = (i & 1) ? bfhi(uw[i >> 1]) : bflo(uw[i >> 1]);
#pragma unroll
      for (int p = 0; p < NP; ++p) {
        const f32x2 u = __builtin_elementwise_fma(t[p], f32x2{a, a}, f32x2{b, b});
        const f32x2 y = u * __builtin_elementwise_fma(u * u, f32x2{C1, C1}, f32x2{C0, C0});
        f32x2 d = f32x2{__builtin_amdgcn_exp2f(y[0]), __builtin_amdgcn_exp2f(y[1])} + 1.f;
        d = f32x2{__builtin_amdgcn_rcpf(d[0]), __builtin_amdgcn_rcpf(d[1])};
        acc[p] = __builtin_elementwise_fma(u * d, f32x2{w, w}, acc[p]);
      }
    }
  }
}

#define STAMP(i)

template <int I, int N, class F> __device__ __forceinline__ void static_for(F&& f) {
  if constexpr (I < N) { f(std::integral_constant<int, I>{}); static_for<I + 1, N>(f); }
}

__global__ __launch_bounds__(768, 3) void fused_kernel(const us* __restrict__ x, const us* __restrict__ kc, const us* __restrict__ vc,
                                                       const us* __restrict__ wq, const us* __restrict__ wo, const us* __restrict__ w1,
                                                       const us* __restrict__ b1, const us* __restrict__ w2, const us* __restrict__ b2,
                                                       us* __restrict__ out, unsigned char* ws) {
  __shared__ __attribute__((aligned(16))) unsigned char smem[L_TOTAL];
  LAS unsigned char* lds = (LAS unsigned char*)smem;
  const unsigned lds0 = (unsigned)(uintptr_t)lds;
  const int lane = threadIdx.x & 63, wave = __builtin_amdgcn_readfirstlane(threadIdx.x >> 6), g = lane >> 4, r = lane & 15;
  LAS unsigned* flags = (LAS unsigned*)(lds + L_FL);
  asm volatile(".Lpf_begin:");
  STAMP(0);
  const unsigned tstart = (unsigned)__builtin_amdgcn_s_memrealtime();
  asm volatile("" :: "s"(x), "s"(kc), "s"(vc), "s"(wq), "s"(ws));
  const int bid = blockIdx.x, sp = (bid >> 3) & 3, b = bid >> 5, kvh = (bid + 3 * sp) & 7;
  const int voff = lane * 16;
  const int kvo = (((b * 8 + kvh) * 4 + sp) << 18) + ((wave - 4) << 10);
  const int koff = kvo + (r >> 2) * 256 + g * 64 + (r & 3) * 16;
#define KLOAD(i) do { kf[i] = __builtin_amdgcn_raw_buffer_load_b128(krs, koff, (i) * 8192, AUX_NT); SB(); } while (0)
  const __amdgpu_buffer_rsrc_t wsr = __builtin_amdgcn_make_buffer_rsrc((void*)ws, 0, WS_END, 0x00020000);
  const int jk = sp * 8 + b;
  const int jb = sp * 8 + kvh;
  if (wave < 4) {
    const v4u srd = make_srd(x, 8 * DM * 2);
    dma16<false>(lds0 + L_X + wave * 2080, voff, srd, wave * 2048);
    dma16<false>(lds0 + L_X + wave * 2080 + 1040, voff, srd, wave * 2048 + 1024);
    if (wave == 1 && lane < 2)
      __builtin_amdgcn_raw_buffer_store_b32(tstart, wsr, lane ? WS_NB + b * 128 + jb * 4 : WS_NK + kvh * 128 + jk * 4, 0, AUX_SC1);
    if (CODEPF && bid < CODEPF && wave == 3) {
      u64 lo; unsigned back, la, lb;
      asm volatile("s_getpc_b64 %0\n.Lpf_here:\n\ts_mov_b32 %1, .Lpf_here-.Lpf_begin\n\ts_mov_b32 %2, .Lpf_end_a-.Lpf_begin\n\ts_mov_b32 %3, .Lpf_end_b-.Lpf_begin"
                   : "=s"(lo), "=s"(back), "=s"(la), "=s"(lb));
      const v4u cs = make_srd((const void*)(lo - back), la > lb ? la : lb);
      int co = lane * 128;
#pragma unroll 1
      for (int i = 0; i < 3; ++i) {
        asm volatile("s_mov_b32 m0, %0\n\ts_nop 0\n\tbuffer_load_dword %1, %2, 0 offen lds" :: "s"(lds0 + L_T), "v"(co), "s"(cs) : "memory");
        co += 8192;
      }
    }
    if (PRETOUCH && wave == 2) {
      const v4u vs = make_srd(vc, 64 << 20), os = make_srd(wo, HQ * DH * DM * 2);
      if (lane == 0) {
        asm volatile("s_mov_b32 m0, %0\n\ts_nop 0\n\tbuffer_load_dword %1, %2, 0 offen nt lds" :: "s"(lds0 + L_T + 512), "v"(kvo + 4096), "s"(vs) : "memory");
        asm volatile("s_mov_b32 m0, %0\n\ts_nop 0\n\tbuffer_load_dword %1, %2, 0 offen lds" :: "s"(lds0 + L_T + 512), "v"(kvh << 19), "s"(os) : "memory");
      }
    }
    if (threadIdx.x < 16) flags[threadIdx.x] = 0u;
    SB();
  } else {
    const v4u ksd = make_srd(kc, 64 << 20);
    dma16<true>(lds0 + L_K0 + (wave - 4) * 2048, koff, ksd, 0);
    dma16<true>(lds0 + L_K0 + (wave - 4) * 2048 + 1024, koff, ksd, 8192);
    STAMP(1);
    const v4u srd = make_srd(wq, DM * HQ * DH * 2);
    const int vo = (sp * 128 + (wave - 4) * 16 + (lane >> 3)) * 8192 + kvh * 1024 + b * 128 + (lane & 7) * 16;
    dma16<true>(lds0 + L_B + (wave - 4) * 2048, vo, srd, 0);
    dma16<true>(lds0 + L_B + (wave - 4) * 2048 + 1024, vo, srd, 8 * 8192);
    SB();
  }
  WG_BARRIER();
  const int wgl = (b << 5) | jb;
  const int cj = sp * 4 + (b & 3), bh = b >> 2;
  const int partg = WS_PART + (kvh * 2 + bh) * 16384;
  const int me = (b & 3) * 4 + sp;
  const bool fin = (sp == 0) && (kvh == b);

  if (wave < 4) {
    __builtin_amdgcn_s_setprio(3);
    if (wave != 1) {
      WAITV(0);
      if (lane == 0) __hip_atomic_fetch_add(flags + F_XW, 1u, RLX_WG);
    }
    if (wave == 0) {
      *(LAS v4u*)(lds + L_X + 8 * 1040 + lane * 16) = v4u{0x3f803f80u, 0x3f803f80u, 0x3f803f80u, 0x3f803f80u};
      while ((__hip_atomic_load(flags + F_XW, RLX_WG) & 255u) != 4u) __builtin_amdgcn_s_sleep(1);
      CFENCE();
      STAMP(1);
      const int xo = L_X + (r < 8 ? r : 8) * 1040 + g * 16;
      f32x4 d2a = {0.f, 0.f, 0.f, 0.f}, d2b = {0.f, 0.f, 0.f, 0.f};
#pragma unroll
      for (int s = 0; s < 16; s += 2) {
        const v4u xa = *(LAS v4u*)(lds + xo + s * 64), xb = *(LAS v4u*)(lds + xo + s * 64 + 64);
        d2a = mfma16(xa, xa, d2a); d2b = mfma16(xb, xb, d2b);
      }
      d2a += d2b;
      const float dg = (r & 2) ? ((r & 1) ? d2a[3] : d2a[2]) : ((r & 1) ? d2a[1] : d2a[0]);
      const float sxx = xrow_sum(g == (r >> 2) ? dg : 0.f);
      const float mean = xrow_sum(g == 2 ? d2a[0] : 0.f) * (1.f / DM);
      float rstd = rsqrtf(fmaxf(sxx * (1.f / DM) - mean * mean, 0.f) + 1e-5f);
      v4u xb[4];
#pragma unroll
      for (int s = 0; s < 4; ++s) { xb[s] = *(LAS v4u*)(lds + xo + sp * 256 + s * 64); asm volatile("" : "+v"(xb[s])); }
      asm volatile("" : "+v"(rstd));
      while (__hip_atomic_load(flags + F_XW, RLX_WG) != 4u + 8u * 256u) __builtin_amdgcn_s_sleep(1);
      CFENCE();
      STAMP(2);
      f32x4 d[4];
#pragma unroll
      for (int nt = 0; nt < 4; ++nt) d[nt] = f32x4{0.f, 0.f, 0.f, 0.f};
      const int troff = L_B + (8 * g + (r >> 2)) * 128 + (r & 3) * 8;
#pragma unroll
      for (int s = 0; s < 4; ++s)
#pragma unroll
        for (int nt = 0; nt < 4; ++nt) {
          const v2u u0 = __builtin_bit_cast(v2u, __builtin_amdgcn_ds_read_tr16_b64_v4i16((LAS v4s*)(lds + troff + s * 4096 + nt * 32)));
          const v2u u1 = __builtin_bit_cast(v2u, __builtin_amdgcn_ds_read_tr16_b64_v4i16((LAS v4s*)(lds + troff + s * 4096 + 512 + nt * 32)));
          d[nt] = mfma16(v4u{u0.x, u0.y, u1.x, u1.y}, xb[s], d[nt]);
        }
#pragma unroll
      for (int nt = 0; nt < 4; ++nt) {
        float qv[4];
#pragma unroll
        for (int i = 0; i < 4; ++i) qv[i] = (d[nt][i] - mean * dpp<0x158>(d[nt][i])) * rstd;
        if (r < 8) *(LAS v2u*)(lds + L_QO + r * 128 + nt * 32 + g * 8) = v2u{cvtpk(qv[0], qv[1]), cvtpk(qv[2], qv[3])};
      }
      LGKM0();
      if (lane == 0) __hip_atomic_store(flags + F_QO, 1u, RLX_WG);
      STAMP(3);
    } else if (wave == 1) {
      const int emo = WS_EM + wgl * 512 + lane * 8;
      const int qdo = WS_QD + (kvh * 8 + g) * 8192 + jk * 256 + r * 16;
      const int nno = lane < 32 ? WS_NK + kvh * 128 + lane * 4 : WS_NB + b * 128 + (lane - 32) * 4;
      const int eco = WS_EM + (lane < 32 ? ((((lane & 7) << 5) | (lane & 24) | kvh) * 64 + jk) : (((b << 5) | (lane - 32)) * 64 + 32 + jb)) * 8;
      const v4u e4 = {EMPTY, EMPTY, EMPTY, EMPTY};
      __builtin_amdgcn_raw_buffer_store_b128(e4, wsr, partg + me * 1024 + lane * 16, 0, AUX_SC1);
      if (lane < 42)
        __builtin_amdgcn_raw_buffer_store_b128(e4, wsr, (lane < 32 ? WS_OUTP + (b * 8 + kvh) * 2048 + sp * 512 : lane < 36 ? WS_STAT - 512 + wgl * 128 : WS_TAB - 576 + wgl * 96) + lane * 16, 0, AUX_SC1);
      SB();
      v2u em = __builtin_amdgcn_raw_buffer_load_b64(wsr, emo, 0, AUX_SC1);
      v4u s0 = __builtin_amdgcn_raw_buffer_load_b128(wsr, qdo, 0, AUX_SC1);
      v4u s1 = __builtin_amdgcn_raw_buffer_load_b128(wsr, qdo, 4 * 8192, AUX_SC1);
      SB();
      WAITV(6);
      if (lane == 0) __hip_atomic_fetch_add(flags + F_XW, 1u, RLX_WG);
      pace<NNT>(tstart);
      STAMP(1);
      unsigned nn = __builtin_amdgcn_raw_buffer_load_b32(wsr, nno, 0, AUX_SC1);
      asm volatile("s_waitcnt vmcnt(0)" : "+v"(em), "+v"(s0), "+v"(s1), "+v"(nn) :: "memory");
      unsigned nonce = tstart;
      while (__any((em[1] == nonce) | (s0[1] == nonce) | (s0[3] == nonce) | (s1[1] == nonce) | (s1[3] == nonce))) ++nonce;
      if (nonce != tstart) {
        if (lane < 2) __builtin_amdgcn_raw_buffer_store_b32(nonce, wsr, lane ? WS_NB + b * 128 + jb * 4 : WS_NK + kvh * 128 + jk * 4, 0, AUX_SC1);
        WAITV(0);
      }
      if (lane < 32) __builtin_amdgcn_raw_buffer_store_b64(v2u{nonce, nn}, wsr, eco, 0, AUX_SC1);
      if (lane == 0) __hip_atomic_store(flags + F_CLR, 1u, RLX_WG);
      STAMP(2);
      unsigned cand[8];
#pragma unroll
      for (int i = 0; i < 8; ++i) cand[i] = __builtin_amdgcn_ds_bpermute((4 * i + g) * 4, nn);
      lds_wait_ne0(flags + F_QO);
      {
        const v2u q0 = *(LAS v2u*)(lds + L_QO + lane * 8), q1 = *(LAS v2u*)(lds + L_QO + 512 + lane * 8);
        __builtin_amdgcn_raw_buffer_store_b128(v4u{q0.x, nonce, q0.y, nonce}, wsr, qdo, 0, AUX_SC1);
        __builtin_amdgcn_raw_buffer_store_b128(v4u{q1.x, nonce, q1.y, nonce}, wsr, qdo, 4 * 8192, AUX_SC1);
      }
      STAMP(3);
      const int qio = WS_QD + (kvh * 8 + b) * 8192 + lane * 16;
      v4u gq[8];
#pragma unroll
      for (int i = 0; i < 8; ++i) gq[i] = v4u{0u, ~cand[i], 0u, 0u};
      __builtin_amdgcn_s_sleep(QD1);
      for (unsigned spin = 0; spin < SPIN_CAP; ++spin) {
        CFENCE();
        const unsigned n2 = __builtin_amdgcn_raw_buffer_load_b32(wsr, nno, 0, AUX_SC1);
        em = __builtin_amdgcn_raw_buffer_load_b64(wsr, emo, 0, AUX_SC1);
#pragma unroll
        for (int i = 0; i < 8; ++i)
          if ((gq[i][1] != cand[i]) | (gq[i][3] != cand[i])) gq[i] = __builtin_amdgcn_raw_buffer_load_b128(wsr, qio, i * 1024, AUX_SC1);
        if (__any(n2 != nn)) {
          if (n2 != nn && lane < 32) __builtin_amdgcn_raw_buffer_store_b64(v2u{nonce, n2}, wsr, eco, 0, AUX_SC1);
          nn = n2;
#pragma unroll
          for (int i = 0; i < 8; ++i) cand[i] = __builtin_amdgcn_ds_bpermute((4 * i + g) * 4, nn);
        }
        bool ok = (lane >= 32) | ((em[1] == nonce) & (em[0] == nn));
#pragma unroll
        for (int i = 0; i < 8; ++i) ok = ok & (gq[i][1] == cand[i]) & (gq[i][3] == cand[i]);
        if (__all(ok)) break;
      }
#pragma unroll
      for (int hf = 0; hf < 2; ++hf) {
        float q0 = 0.f, q1 = 0.f, q2 = 0.f, q3 = 0.f;
#pragma unroll
        for (int kq = 0; kq < 4; ++kq) {
          const v4u t4 = gq[2 * kq + hf];
          q0 += bflo(t4[0]); q1 += bfhi(t4[0]); q2 += bflo(t4[2]); q3 += bfhi(t4[2]);
        }
        *(LAS v2u*)(lds + L_Q + hf * 512 + lane * 8) = v2u{cvtpk(q0, q1), cvtpk(q2, q3)};
      }
      LGKM0();
      if (lane == 0) __hip_atomic_store(flags + F_Q, 1u, RLX_WG);
      STAMP(4);
      if (lane >= 32) __builtin_amdgcn_raw_buffer_store_b64(v2u{nonce, nn}, wsr, eco, 0, AUX_SC1);
      for (unsigned spin = 0; spin < SPIN_CAP; ++spin) {
        __builtin_amdgcn_s_sleep(32);
        CFENCE();
        const unsigned n2 = __builtin_amdgcn_raw_buffer_load_b32(wsr, nno, 0, AUX_SC1);
        em = __builtin_amdgcn_raw_buffer_load_b64(wsr, emo, 0, AUX_SC1);
        if (n2 != nn && lane >= 32) __builtin_amdgcn_raw_buffer_store_b64(v2u{nonce, n2}, wsr, eco, 0, AUX_SC1);
        nn = n2;
        if (__all((lane < 32) | ((em[1] == nonce) & (em[0] == nn)))) break;
      }
      STAMP(7);
      if (fin) {
        pace<TABT>(tstart);
#pragma unroll 1
        for (int c = 0; c < 3; ++c) {
          for (unsigned spin = 0; spin < SPIN_CAP; ++spin) {
            CFENCE();
            v4u tv[8];
#pragma unroll
            for (int i = 0; i < 8; ++i) tv[i] = __builtin_amdgcn_raw_buffer_load_b128(wsr, WS_TAB + c * 8192 + lane * 16, i * 1024, AUX_SC1);
            bool ok = true;
#pragma unroll
            for (int i = 0; i < 8; ++i) {
              ok = ok & (tv[i][1] == 1u) & (tv[i][3] == 1u);
              *(LAS v2u*)(lds + L_T + c * 4096 + i * 512 + lane * 8) = v2u{tv[i][0], tv[i][2]};
            }
            if (__all(ok)) break;
            __builtin_amdgcn_s_sleep(16);
          }
        }
        LGKM0();
        if (lane == 0) __hip_atomic_store(flags + F_FIN, 1u, RLX_WG);
        asm volatile(".Lpf_end_a:");
        STAMP(8);
      }
    } else if (wave == 2) {
      pace<PART>(tstart);
      const v4u s1 = make_srd(w1, FF * 2), s2 = make_srd(b1, FF * 2), s3 = make_srd(w2, FF * 2);
#pragma unroll
      for (int i = 0; i < 3; ++i) {
        dma16<false>(lds0 + L_P + i * 1024, voff, s1, i * 1024);
        dma16<false>(lds0 + L_P + 3584 + i * 1024, voff, s2, i * 1024);
        dma16<false>(lds0 + L_P + 7168 + i * 1024, voff, s3, i * 1024);
      }
      if (lane < 32) {
        dma16<false>(lds0 + L_P + 3072, voff, s1, 3072);
        dma16<false>(lds0 + L_P + 3584 + 3072, voff, s2, 3072);
        dma16<false>(lds0 + L_P + 7168 + 3072, voff, s3, 3072);
      }
      const unsigned b2u = b2[0];
      if (lane == 0) flags[F_B2] = b2u;
      asm volatile("s_waitcnt vmcnt(0) lgkmcnt(0)" ::: "memory");
      if (lane == 0) __hip_atomic_store(flags + F_P, 1u, RLX_WG);
      STAMP(1);
    }
    if (wave != 1) {
      __builtin_amdgcn_s_setprio(0);
      lds_wait_ne0(flags + F_P);
      lds_wait_ne0(flags + F_CLR);
      const int p0 = wave == 0 ? 0 : 2 * wave - 2;
#pragma unroll 1
      for (int ps = p0; ps < p0 + 2; ++ps) {
        const unsigned bits = (unsigned)((wgl & 127) * 24 + ps * 4 + (TAB_E0 << 7)) | (unsigned)((wgl >> 7) << 15);
        f32x2 t[2], a[2];
#pragma unroll
        for (int p = 0; p < 2; ++p) t[p] = f32x2{bflo(bits + 2 * p), bflo(bits + 2 * p + 1)};
        ffn_eval<2>(lds, lane, t, a);
        const unsigned k0 = cvtpk(wave_sum(a[0][0]), wave_sum(a[0][1])), k1 = cvtpk(wave_sum(a[1][0]), wave_sum(a[1][1]));
        __builtin_amdgcn_s_setprio(3);
        if (lane == 0) __builtin_amdgcn_raw_buffer_store_b128(v4u{k0, 1u, k1, 1u}, wsr, WS_TAB + wgl * 96 + ps * 16, 0, AUX_SC1);
        __builtin_amdgcn_s_setprio(0);
      }
      __builtin_amdgcn_s_setprio(3);
      STAMP(6);
    }
    if (wave == 0) {
      while (__hip_atomic_load(flags + F_CNT, RLX_WG) != 8u) __builtin_amdgcn_s_sleep(2);
      CFENCE();
      STAMP(7);
      const int hd = lane & 3;
      float M = -INFINITY, L = 0.f;
#pragma unroll
      for (int w = 0; w < 8; ++w) M = fmaxf(M, ((LAS float*)(lds + L_ML))[w * 8 + hd]);
#pragma unroll
      for (int w = 0; w < 8; ++w) L = fmaf(__builtin_amdgcn_exp2f(((LAS float*)(lds + L_ML))[w * 8 + hd] - M), ((LAS float*)(lds + L_ML))[w * 8 + 4 + hd], L);
      if (lane < 4) __builtin_amdgcn_raw_buffer_store_b128(v4u{__float_as_uint(M), 1u, __float_as_uint(L), 1u}, wsr, WS_STAT + wgl * 128 + lane * 16, 0, AUX_SC1);
      v4u sv = {0u, 1u, 0u, 1u};
      for (unsigned spin = 0; spin < SPIN_CAP; ++spin) {
        __builtin_amdgcn_s_sleep(8);
        CFENCE();
        if (lane < 16) sv = __builtin_amdgcn_raw_buffer_load_b128(wsr, WS_STAT + ((b << 5) | ((lane >> 2) << 3) | kvh) * 128 + hd * 16, 0, AUX_SC1);
        if (__all((sv[1] == 1u) & (sv[3] == 1u))) break;
      }
      const float ms = __uint_as_float(sv[0]);
      float Mg = fmaxf(ms, dpp<0x124>(ms)); Mg = fmaxf(Mg, dpp<0x128>(Mg));
      float Lg = __builtin_amdgcn_exp2f(ms - Mg) * __uint_as_float(sv[2]);
      Lg += dpp<0x128>(Lg); Lg += dpp<0x124>(Lg);
      if (lane < 4) ((LAS float*)(lds + L_Z))[lane] = Mg + __builtin_amdgcn_logf(Lg);
      LGKM0();
      if (lane == 0) __hip_atomic_store(flags + F_Z, 1u, RLX_WG);
      STAMP(8);
    }
  } else {
    const int sw = wave - 4, tid = threadIdx.x - 256;
    const __amdgpu_buffer_rsrc_t vrs = __builtin_amdgcn_make_buffer_rsrc((void*)vc, 0, 64 << 20, 0x00020000);
    const int vvo = kvo + voff;
    const v4u wsd = make_srd(wo, HQ * DH * DM * 2);
    const int wo_ = (kvh * 512 + sw + ((lane >> 2) & 3) * 128 + (lane >> 4) * 8) * 1024 + cj * 64 + (lane & 3) * 16;
#define WPIECE(k) do { dma16<false>(lds0 + L_W + sw * 4096 + (k) * 1024, wo_, wsd, (k) * 32 * 1024); SB(); } while (0)
#define VLOAD(i) do { WAITV(VW - 1); pace<sched(32 + (i))>(tstart); vf[i] = __builtin_amdgcn_raw_buffer_load_b128(vrs, vvo, (i) * 8192, AUX_NT); SB(); \
    if ((i) == 3) WPIECE(2); if ((i) == 11) WPIECE(3); } while (0)
    const __amdgpu_buffer_rsrc_t krs = __builtin_amdgcn_make_buffer_rsrc((void*)kc, 0, 64 << 20, 0x00020000);
    v4u kf[32], vf[32];
    __builtin_amdgcn_s_setprio(2);
    static_for<KFIRST, 32>([&](auto ic) {
      constexpr int i = ic.value;
      WAITV(KW - 1);
      if (i == KFIRST + KW - 1) { if (lane == 0) __hip_atomic_fetch_add(flags + F_XW, 256u, RLX_WG); }
      pace<sched(i)>(tstart);
      KLOAD(i);
      if (i == 15) WPIECE(0);
      if (i == 23) WPIECE(1);
    });
    STAMP(3);
    static_for<0, XV>([&](auto ic) { VLOAD(ic.value); });
    STAMP(6);
    lds_wait_ne0(flags + F_Q);
    SB();
    const v4u qf = *(LAS v4u*)(lds + L_Q + (r & 3) * 256 + g * 64 + (r >> 2) * 16);
    kf[0] = *(LAS v4u*)(lds + L_K0 + sw * 2048 + voff);
    kf[1] = *(LAS v4u*)(lds + L_K0 + sw * 2048 + 1024 + voff);
    float s[32];
    static_for<0, 32>([&](auto ic) {
      constexpr int i = ic.value;
      const f32x4 a = mfma16(kf[i], qf, f32x4{0.f, 0.f, 0.f, 0.f});
      float t_ = a[0] + dpp<0x104>(a[1]); asm volatile("" : "+v"(t_));
      t_ += dpp<0x108>(a[2]); asm volatile("" : "+v"(t_));
      s[i] = t_ + dpp<0x10C>(a[3]); asm volatile("" : "+v"(s[i]));
      if constexpr ((i & 7) == 7) { SB(); VLOAD(XV + (i >> 3)); }
    });
    SB();
    float m = s[0];
#pragma unroll
    for (int i = 1; i < 32; ++i) m = fmaxf(m, s[i]);
    m = xrow_max(m) * SCALE_L2;
    v4u pf[4];
#define SOFTMAX(G, mm) do { float p_[8]; \
    _Pragma("unroll") for (int j = 0; j < 8; ++j) p_[j] = __builtin_amdgcn_exp2f(fmaf(s[8 * (G) + j], SCALE_L2, -(mm))); \
    pf[G] = v4u{cvtpk(p_[0], p_[1]), cvtpk(p_[2], p_[3]), cvtpk(p_[4], p_[5]), cvtpk(p_[6], p_[7])}; } while (0)
    {
      SOFTMAX(0, m); SOFTMAX(1, m); SOFTMAX(2, m); SOFTMAX(3, m);
      const v4u ones = {0x3f803f80u, 0x3f803f80u, 0x3f803f80u, 0x3f803f80u};
      f32x4 l4 = {0.f, 0.f, 0.f, 0.f};
#pragma unroll
      for (int G = 0; G < 4; ++G) l4 = mfma16(pf[G], ones, l4);
      if (lane < 4) ((LAS float*)(lds + L_ML))[sw * 8 + lane] = m;
      if (lane == 0) *(LAS f32x4*)(lds + L_ML + sw * 32 + 16) = l4;
      LGKM0();
      if (lane == 0) __hip_atomic_fetch_add(flags + F_CNT, 1u, RLX_WG);
    }
    STAMP(7);
    f32x4 acc[8];
#pragma unroll
    for (int j = 0; j < 8; ++j) acc[j] = f32x4{0.f, 0.f, 0.f, 0.f};
#define PV(G) do { \
    _Pragma("unroll") for (int d = 0; d < 4; ++d) { v4u lo_, hi_; \
      _Pragma("unroll") for (int jj = 0; jj < 4; ++jj) { \
        lo_[jj] = __builtin_amdgcn_perm(vf[8 * (G) + 2 * jj + 1][d], vf[8 * (G) + 2 * jj][d], 0x05040100u); \
        hi_[jj] = __builtin_amdgcn_perm(vf[8 * (G) + 2 * jj + 1][d], vf[8 * (G) + 2 * jj][d], 0x07060302u); } \
      acc[2 * d] = mfma16(pf[G], lo_, acc[2 * d]); acc[2 * d + 1] = mfma16(pf[G], hi_, acc[2 * d + 1]); } SB(); } while (0)
    static_for<XV + 4, 32>([&](auto ic) {
      constexpr int i = ic.value;
      VLOAD(i);
      if constexpr (i == 7 + PVD) PV(0);
      if constexpr (i == 15 + PVD) PV(1);
      if constexpr (i == 23 + PVD) PV(2);
    });
    STAMP(8);
    v4u wf[2][2];
    {
      const int wtr = L_W + sw * 4096 + (g * 8 + (r >> 2)) * 64 + (r & 3) * 8;
#pragma unroll
      for (int ks = 0; ks < 2; ++ks)
#pragma unroll
        for (int nt = 0; nt < 2; ++nt) {
          const v2u u0 = __builtin_bit_cast(v2u, __builtin_amdgcn_ds_read_tr16_b64_v4i16((LAS v4s*)(lds + wtr + ks * 2048 + nt * 32)));
          const v2u u1 = __builtin_bit_cast(v2u, __builtin_amdgcn_ds_read_tr16_b64_v4i16((LAS v4s*)(lds + wtr + ks * 2048 + 256 + nt * 32)));
          wf[ks][nt] = v4u{u0.x, u0.y, u1.x, u1.y};
          asm volatile("" : "+v"(wf[ks][nt]));
        }
      SB();
    }
    lds_wait_ne0(flags + F_Z);
    SB();
    {
      const f32x4 Z4 = *(LAS f32x4*)(lds + L_Z);
      const f32x4 mo = *(LAS f32x4*)(lds + L_ML + sw * 32);
      const float zl = ((LAS float*)(lds + L_Z))[lane & 3];
      f32x4 f4;
#pragma unroll
      for (int i = 0; i < 4; ++i) f4[i] = __builtin_amdgcn_exp2f(mo[i] - Z4[i]);
#pragma unroll
      for (int j = 0; j < 8; ++j) acc[j] *= f4;
      const float zz = (r < 4) ? zl : m;
      SOFTMAX(3, zz);
      SB();
    }
    {
      v4u lo_[4], hi_[4];
#pragma unroll
      for (int jj = 0; jj < 4; ++jj) {
#pragma unroll
        for (int d = 0; d < 4; ++d) {
          lo_[d][jj] = __builtin_amdgcn_perm(vf[24 + 2 * jj + 1][d], vf[24 + 2 * jj][d], 0x05040100u);
          hi_[d][jj] = __builtin_amdgcn_perm(vf[24 + 2 * jj + 1][d], vf[24 + 2 * jj][d], 0x07060302u);
        }
        SB();
      }
#pragma unroll
      for (int d = 0; d < 4; ++d) { acc[2 * d] = mfma16(pf[3], lo_[d], acc[2 * d]); acc[2 * d + 1] = mfma16(pf[3], hi_[d], acc[2 * d + 1]); }
      SB();
    }
    STAMP(9);
    if (lane < 16) {
#pragma unroll
      for (int j = 0; j < 8; ++j) *(LAS f32x4*)(lds + L_B + ((sw * 8 + j) * 16 + lane) * 16) = acc[j];
    }
    WG_BARRIER();
    STAMP(10);
    LAS unsigned char* aim = lds + L_A + sw * 2304;
    if (lane < 16) {
      f32x4 o = *(LAS f32x4*)(lds + L_B + (sw * 16 + lane) * 16);
#pragma unroll
      for (int w = 1; w < 8; ++w) o += *(LAS f32x4*)(lds + L_B + ((w * 8 + sw) * 16 + lane) * 16);
      const v2u op = {cvtpk(o[0], o[1]), min(cvtpk(o[2], o[3]), EMPTY - 1u)};
      __builtin_amdgcn_raw_buffer_store_b64(op, wsr, partg + me * 1024 + sw * 128 + lane * 8, 0, AUX_SC1);
      *(LAS v2u*)(aim + me * 144 + lane * 8) = op;
    }
    SB();
    {
      const int po = partg + (lane >> 3) * 1024 + sw * 128 + (lane & 7) * 16;
      bool ok0 = (lane >> 3) == me, ok1 = (lane >> 3) + 8 == me;
      __builtin_amdgcn_s_sleep(H2D);
      for (unsigned spin = 0; spin < SPIN_CAP; ++spin) {
        CFENCE();
        v4u p0 = {EMPTY, EMPTY, EMPTY, EMPTY}, p1 = {EMPTY, EMPTY, EMPTY, EMPTY};
        if (!ok0) p0 = __builtin_amdgcn_raw_buffer_load_b128(wsr, po, 0, AUX_SC1);
        if (!ok1) p1 = __builtin_amdgcn_raw_buffer_load_b128(wsr, po, 8192, AUX_SC1);
        if ((p0[1] != EMPTY) & (p0[3] != EMPTY)) { ok0 = true; *(LAS v4u*)(aim + (lane >> 3) * 144 + (lane & 7) * 16) = p0; }
        if ((p1[1] != EMPTY) & (p1[3] != EMPTY)) { ok1 = true; *(LAS v4u*)(aim + (lane >> 3) * 144 + 1152 + (lane & 7) * 16) = p1; }
        if (__all(ok0 & ok1)) break;
      }
      STAMP(11);
      f32x4 c0 = {0.f, 0.f, 0.f, 0.f}, c1 = {0.f, 0.f, 0.f, 0.f};
#pragma unroll
      for (int ks = 0; ks < 2; ++ks) {
        const v4u af = *(LAS v4u*)(aim + r * 144 + ks * 64 + g * 16);
        c0 = mfma16(af, wf[ks][0], c0);
        c1 = mfma16(af, wf[ks][1], c1);
      }
      LAS float* red = (LAS float*)(lds + L_W);
      red[sw * 128 + lane] = (c0[0] + c0[1]) + (c0[2] + c0[3]);
      red[sw * 128 + 64 + lane] = (c1[0] + c1[1]) + (c1[2] + c1[3]);
    }
    WG_BARRIER();
    if (sw < 2) {
      LAS float* red = (LAS float*)(lds + L_W);
      float a = red[tid];
#pragma unroll
      for (int w = 1; w < 8; ++w) a += red[w * 128 + tid];
      __builtin_amdgcn_raw_buffer_store_b32(min(__float_as_uint(a), EMPTY - 1u), wsr, WS_OUTP + (((bh * 4 + g) * 8 + kvh) * 512 + cj * 32 + sw * 16 + r) * 4, 0, AUX_SC1);
    }
    STAMP(12);
    if (fin) {
      lds_wait_ne0(flags + F_FIN);
      const float xres = bflo(((LAS us*)(lds + L_X))[b * 520 + tid]), b2v = bflo(flags[F_B2]);
      unsigned pv[8];
#pragma unroll
      for (int k = 0; k < 8; ++k) pv[k] = EMPTY;
      for (unsigned spin = 0; spin < SPIN_CAP; ++spin) {
        CFENCE();
#pragma unroll
        for (int k = 0; k < 8; ++k)
          if (pv[k] == EMPTY) pv[k] = __builtin_amdgcn_raw_buffer_load_b32(wsr, WS_OUTP + b * 16384 + tid * 4, k * 2048, AUX_SC1);
        bool ok = true;
#pragma unroll
        for (int k = 0; k < 8; ++k) ok = ok & (pv[k] != EMPTY);
        if (__all(ok)) break;
      }
      STAMP(13);
      const float a = ((__uint_as_float(pv[0]) + __uint_as_float(pv[1])) + (__uint_as_float(pv[2]) + __uint_as_float(pv[3]))) +
                      ((__uint_as_float(pv[4]) + __uint_as_float(pv[5])) + (__uint_as_float(pv[6]) + __uint_as_float(pv[7])));
      const float h = rbf(xres + rbf(a));
      const float s1 = wave_sum(h), s2 = wave_sum(h * h);
      if (lane == 0) *(LAS f32x2*)(lds + L_B + sw * 8) = f32x2{s1, s2};
      WG_BARRIER();
      const f32x4 q0 = *(LAS f32x4*)(lds + L_B), q1 = *(LAS f32x4*)(lds + L_B + 16), q2 = *(LAS f32x4*)(lds + L_B + 32), q3 = *(LAS f32x4*)(lds + L_B + 48);
      const f32x4 qs = (q0 + q1) + (q2 + q3);
      const float mean = (qs[0] + qs[2]) * (1.f / DM);
      const float var = fmaxf((qs[1] + qs[3]) * (1.f / DM) - mean * mean, 0.f);
      const float r1 = __builtin_amdgcn_rsqf(var + 1e-5f);
      const float r2 = __builtin_amdgcn_rsqf(fmaf(var * r1, r1, 1e-5f));
      const float t = ((h - mean) * r1) * r2;
      const unsigned tb = cvtpk(t, t) & 0xffffu;
      const unsigned rel = (tb & 0x7fffu) - (unsigned)(TAB_E0 << 7);
      const bool inr = rel < (unsigned)TAB_H;
      float y = bflo(((LAS us*)(lds + L_T))[(inr ? rel : 0u) + (tb >> 15) * TAB_H]);
      u64 miss = __ballot(!inr);
      while (miss) {
        const int l = __builtin_ctzll(miss);
        miss &= miss - 1;
        const float ts = bflo((unsigned)__builtin_amdgcn_readlane((int)tb, l));
        const f32x2 tt[1] = {f32x2{ts, ts}};
        f32x2 aa[1];
        ffn_eval<1>(lds, lane, tt, aa);
        const float ys = rbf(wave_sum(aa[0][0]));
        if (lane == l) y = ys;
      }
      const float ov = h + rbf(y + b2v);
      out[b * DM + tid] = (us)(cvtpk(ov, ov) & 0xffffu);
      STAMP(14);
    }
  }
  asm volatile(".Lpf_end_b:");
}

extern "C" void kernel_launch(void* const* d_in, const int* in_sizes, int n_in, void* d_out, int out_size, void* d_ws, size_t ws_size, hipStream_t stream) {
  fused_kernel<<<256, 768, 0, stream>>>((const us*)d_in[0], (const us*)d_in[1], (const us*)d_in[2], (const us*)d_in[3], (const us*)d_in[4],
                                        (const us*)d_in[6], (const us*)d_in[7], (const us*)d_in[8], (const us*)d_in[9], (us*)d_out, (unsigned char*)d_ws);
}
```

```cpp
#include <hip/hip_runtime.h>
#include <hip/hip_bf16.h>
#include <stdint.h>
#include <type_traits>

#define GAS __attribute__((address_space(1)))
#define LAS __attribute__((address_space(3)))
#define RLX_AGENT __ATOMIC_RELAXED, __HIP_MEMORY_SCOPE_AGENT
#define RLX_WG __ATOMIC_RELAXED, __HIP_MEMORY_SCOPE_WORKGROUP
typedef unsigned v4u __attribute__((ext_vector_type(4)));
typedef unsigned v2u __attribute__((ext_vector_type(2)));
typedef float f32x4 __attribute__((ext_vector_type(4)));
typedef float f32x2 __attribute__((ext_vector_type(2)));
typedef short bf16x8 __attribute__((ext_vector_type(8)));
typedef short v4s __attribute__((ext_vector_type(4)));
typedef __bf16 bf16x2_t __attribute__((ext_vector_type(2)));
typedef unsigned long long u64;
typedef GAS unsigned gu32;
typedef GAS u64 gu64;
typedef unsigned short us;

constexpr int DM = 512, HQ = 32, DH = 128, FF = 1792;
constexpr float LOG2E = 1.4426950408889634f;
constexpr float SCALE_L2 = 0.08838834764831845f * LOG2E;
constexpr unsigned SPIN_CAP = 200000u;
constexpr int AUX_NT = 2, AUX_SC1 = 16;
constexpr unsigned EMPTY = 0xffffffffu;
constexpr int TAB_E0 = 107;
constexpr int TAB_H = 24 * 128;

#define NSLEEP 4
#define H2D 8
#define CODEPF 16
#define KFIRST 2
#define PER16 480
#define LEAD 2
#define NE 24
#define EX16 120
#define WC16 200
#define XWSIG 5
#define PVD 5
#define XV 2
#define PSL 2
#define PSLT 7
#define NNT 130
#define KW 4
#define VW 6
#define PART 250
#define PRETOUCH 1
#define KW1 4
#define KSW 16
#define MAPB 1
#define QT1 480
#define QSL 8
#define NS 48
#define SP16 0
#define QD1 16
#define TABT 1200
constexpr int sched(int n) {
  return ((n - LEAD) * PER16 + (n < NE ? n : NE) * EX16 - (n > NS ? n - NS : 0) * SP16 + ((n >= 16) + (n >= 24) + (n >= 36) + (n >= 44)) * WC16) >> 4;
}

constexpr int WS_NK = 0;
constexpr int WS_NB = 1024;
constexpr int WS_STAT = 4096;
constexpr int WS_TAB = WS_STAT + 256 * 128;
constexpr int WS_EM = WS_TAB + 256 * 128;
constexpr int WS_PART = WS_EM + 256 * 512;
constexpr int WS_OUTP = WS_PART + 256 * 1024;
constexpr int WS_QD = WS_OUTP + 64 * 2048;
constexpr int WS_END = WS_QD + 64 * 8192;
constexpr size_t WS_STAMP = 16u << 20;

constexpr int L_W = 0;
constexpr int L_B = 32768;
constexpr int L_X = L_B + 16384;
constexpr int L_P = L_X + 9472;
constexpr int L_T = L_P + 10752;
constexpr int L_Q = L_T + 16384;
constexpr int L_QO = L_Q + 1024;
constexpr int L_ML = L_QO + 1024;
constexpr int L_Z = L_ML + 256;
constexpr int L_FL = L_Z + 64;
constexpr int L_A = L_FL + 64;
constexpr int L_K0 = L_A + 8 * 2304;
constexpr int L_TOTAL = L_K0 + 8 * 2048;
enum { F_Q = 0, F_CNT = 1, F_QO = 2, F_Z = 3, F_P = 4, F_FIN = 5, F_XW = 6, F_B2 = 7, F_CLR = 8 };

__device__ __forceinline__ f32x4 mfma16(v4u a, v4u b, f32x4 c) {
  return __builtin_amdgcn_mfma_f32_16x16x32_bf16(__builtin_bit_cast(bf16x8, a), __builtin_bit_cast(bf16x8, b), c, 0, 0, 0);
}
__device__ __forceinline__ unsigned cvtpk(float lo, float hi) {
  return __builtin_bit_cast(unsigned, __builtin_convertvector(f32x2{lo, hi}, bf16x2_t));
}
__device__ __forceinline__ float bflo(unsigned u) { return __builtin_bit_cast(float, u << 16); }
__device__ __forceinline__ float bfhi(unsigned u) { return __builtin_bit_cast(float, u & 0xffff0000u); }
__device__ __forceinline__ float rbf(float x) { return bflo(cvtpk(x, x)); }
__device__ __forceinline__ float xrow_max(float x) {
  auto s = __builtin_amdgcn_permlane16_swap(__float_as_uint(x), __float_as_uint(x), false, false);
  x = fmaxf(__uint_as_float(s[0]), __uint_as_float(s[1]));
  auto t = __builtin_amdgcn_permlane32_swap(__float_as_uint(x), __float_as_uint(x), false, false);
  return fmaxf(__uint_as_float(t[0]), __uint_as_float(t[1]));
}
__device__ __forceinline__ float xrow_sum(float x) {
  auto s = __builtin_amdgcn_permlane16_swap(__float_as_uint(x), __float_as_uint(x), false, false);
  x = __uint_as_float(s[0]) + __uint_as_float(s[1]);
  auto t = __builtin_amdgcn_permlane32_swap(__float_as_uint(x), __float_as_uint(x), false, false);
  return __uint_as_float(t[0]) + __uint_as_float(t[1]);
}
template <int CTRL> __device__ __forceinline__ float dpp(float x) {
  return __builtin_bit_cast(float, __builtin_amdgcn_mov_dpp(__builtin_bit_cast(int, x), CTRL, 0xf, 0xf, true));
}
__device__ __forceinline__ float wave_sum(float x) {
  x += dpp<0xB1>(x); x += dpp<0x4E>(x); x += dpp<0x141>(x); x += dpp<0x128>(x);
  return xrow_sum(x);
}
#define SB() __builtin_amdgcn_sched_barrier(0)
#define CFENCE() asm volatile("" ::: "memory")
#define LGKM0() asm volatile("s_waitcnt lgkmcnt(0)" ::: "memory")
#define WAITV(n) asm volatile("s_waitcnt vmcnt(%0)" :: "i"(n) : "memory")
#define WG_BARRIER() do { asm volatile("s_waitcnt lgkmcnt(0)\n\ts_barrier" ::: "memory"); SB(); } while (0)
__device__ __forceinline__ v4u make_srd(const void* p, unsigned bytes) {
  const u64 a = (u64)p;
  v4u d;
  d[0] = __builtin_amdgcn_readfirstlane((unsigned)a);
  d[1] = __builtin_amdgcn_readfirstlane((unsigned)(a >> 32)) & 0xffffu;
  d[2] = bytes;
  d[3] = 0x00020000u;
  return d;
}
template <bool NT>
__device__ __forceinline__ void dma16(unsigned ldsaddr, int voff, v4u srd, int soff) {
  if (NT) asm volatile("s_mov_b32 m0, %0\n\ts_nop 0\n\tbuffer_load_dwordx4 %1, %2, %3 offen nt lds" :: "s"(ldsaddr), "v"(voff), "s"(srd), "s"(soff) : "memory");
  else asm volatile("s_mov_b32 m0, %0\n\ts_nop 0\n\tbuffer_load_dwordx4 %1, %2, %3 offen lds" :: "s"(ldsaddr), "v"(voff), "s"(srd), "s"(soff) : "memory");
}
__device__ __forceinline__ void lds_wait_ne0(LAS unsigned* f) {
  while (__hip_atomic_load(f, RLX_WG) == 0u) __builtin_amdgcn_s_sleep(1);
  CFENCE();
}
template <int TICKS>
__device__ __forceinline__ void pace(unsigned tstart) {
  if (TICKS > 0)
    asm volatile("s_memrealtime vcc\n\ts_waitcnt lgkmcnt(0)\n\ts_sub_i32 vcc_lo, vcc_lo, %0\n"
                 "1:\n\ts_cmpk_ge_i32 vcc_lo, %1\n\ts_cbranch_scc1 2f\n\ts_sleep %2\n\ts_addk_i32 vcc_lo, %3\n\ts_branch 1b\n2:"
                 :: "s"(tstart), "i"(TICKS), "i"(PSL), "i"(PSLT) : "vcc", "scc", "memory");
}

template <int NP>
__device__ __forceinline__ void ffn_eval(LAS unsigned char* lds, int lane, const f32x2 (&t)[NP], f32x2 (&acc)[NP]) {
  constexpr float C0 = -2.f * 0.7978845608028654f * LOG2E, C1 = C0 * 0.044715f;
#pragma unroll
  for (int p = 0; p < NP; ++p) acc[p] = f32x2{0.f, 0.f};
#pragma unroll 1
  for (int j = 0; j < 7; ++j) {
    const v2u ua = *(LAS v2u*)(lds + L_P + j * 512 + lane * 8);
    const v2u ub = *(LAS v2u*)(lds + L_P + 3584 + j * 512 + lane * 8);
    const v2u uw = *(LAS v2u*)(lds + L_P + 7168 + j * 512 + lane * 8);
#pragma unroll
    for (int i = 0; i < 4; ++i) {
      const float a = (i & 1) ? bfhi(ua[i >> 1]) : bflo(ua[i >> 1]);
      const float b = (i & 1) ? bfhi(ub[i >> 1]) : bflo(ub[i >> 1]);
      const float w = (i & 1) ? bfhi(uw[i >> 1]) : bflo(uw[i >> 1]);
#pragma unroll
      for (int p = 0; p < NP; ++p) {
        const f32x2 u = __builtin_elementwise_fma(t[p], f32x2{a, a}, f32x2{b, b});
        const f32x2 y = u * __builtin_elementwise_fma(u * u, f32x2{C1, C1}, f32x2{C0, C0});
        f32x2 d = f32x2{__builtin_amdgcn_exp2f(y[0]), __builtin_amdgcn_exp2f(y[1])} + 1.f;
        d = f32x2{__builtin_amdgcn_rcpf(d[0]), __builtin_amdgcn_rcpf(d[1])};
        acc[p] = __builtin_elementwise_fma(u * d, f32x2{w, w}, acc[p]);
      }
    }
  }
}

#define STAMP(i)

template <int I, int N, class F> __device__ __forceinline__ void static_for(F&& f) {
  if constexpr (I < N) { f(std::integral_constant<int, I>{}); static_for<I + 1, N>(f); }
}

__global__ __launch_bounds__(768, 3) void fused_kernel(const us* __restrict__ x, const us* __restrict__ kc, const us* __restrict__ vc,
                                                       const us* __restrict__ wq, const us* __restrict__ wo, const us* __restrict__ w1,
                                                       const us* __restrict__ b1, const us* __restrict__ w2, const us* __restrict__ b2,
                                                       us* __restrict__ out, unsigned char* ws) {
  __shared__ __attribute__((aligned(16))) unsigned char smem[L_TOTAL];
  LAS unsigned char* lds = (LAS unsigned char*)smem;
  const unsigned lds0 = (unsigned)(uintptr_t)lds;
  const int lane = threadIdx.x & 63, wave = __builtin_amdgcn_readfirstlane(threadIdx.x >> 6), g = lane >> 4, r = lane & 15;
  LAS unsigned* flags = (LAS unsigned*)(lds + L_FL);
  asm volatile(".Lpf_begin:");
  STAMP(0);
  const unsigned tstart = (unsigned)__builtin_amdgcn_s_memrealtime();
  asm volatile("" :: "s"(x), "s"(kc), "s"(vc), "s"(wq), "s"(ws));
  const int bid = blockIdx.x, sp = (bid >> 3) & 3, b = ((bid >> 3) & 4) | (bid >> 6), kvh = (bid + 3 * (bid >> 6)) & 7;
  const int voff = lane * 16;
  const int kvo = (((b * 8 + kvh) * 4 + sp) << 18) + ((wave - 4) << 10);
  const int koff = kvo + (r >> 2) * 256 + g * 64 + (r & 3) * 16;
#define KLOAD(i) do { kf[i] = __builtin_amdgcn_raw_buffer_load_b128(krs, koff, (i) * 8192, AUX_NT); SB(); } while (0)
  const __amdgpu_buffer_rsrc_t wsr = __builtin_amdgcn_make_buffer_rsrc((void*)ws, 0, WS_END, 0x00020000);
  const int jk = sp * 8 + b;
  const int jb = sp * 8 + kvh;
  if (wave < 4) {
    const v4u srd = make_srd(x, 8 * DM * 2);
    dma16<false>(lds0 + L_X + wave * 2080, voff, srd, wave * 2048);
    dma16<false>(lds0 + L_X + wave * 2080 + 1040, voff, srd, wave * 2048 + 1024);
    if (wave == 1 && lane < 2)
      __builtin_amdgcn_raw_buffer_store_b32(tstart, wsr, lane ? WS_NB + b * 128 + jb * 4 : WS_NK + kvh * 128 + jk * 4, 0, AUX_SC1);
    if (CODEPF && bid < CODEPF && wave == 3) {
      u64 lo; unsigned back, la, lb;
      asm volatile("s_getpc_b64 %0\n.Lpf_here:\n\ts_mov_b32 %1, .Lpf_here-.Lpf_begin\n\ts_mov_b32 %2, .Lpf_end_a-.Lpf_begin\n\ts_mov_b32 %3, .Lpf_end_b-.Lpf_begin"
                   : "=s"(lo), "=s"(back), "=s"(la), "=s"(lb));
      const v4u cs = make_srd((const void*)(lo - back), la > lb ? la : lb);
      int co = lane * 128;
#pragma unroll 1
      for (int i = 0; i < 3; ++i) {
        asm volatile("s_mov_b32 m0, %0\n\ts_nop 0\n\tbuffer_load_dword %1, %2, 0 offen lds" :: "s"(lds0 + L_T), "v"(co), "s"(cs) : "memory");
        co += 8192;
      }
    }
    if (PRETOUCH && wave == 2) {
      const v4u vs = make_srd(vc, 64 << 20), os = make_srd(wo, HQ * DH * DM * 2);
      if (lane == 0) {
        asm volatile("s_mov_b32 m0, %0\n\ts_nop 0\n\tbuffer_load_dword %1, %2, 0 offen nt lds" :: "s"(lds0 + L_T + 512), "v"(kvo + 4096), "s"(vs) : "memory");
        asm volatile("s_mov_b32 m0, %0\n\ts_nop 0\n\tbuffer_load_dword %1, %2, 0 offen lds" :: "s"(lds0 + L_T + 512), "v"(kvh << 19), "s"(os) : "memory");
      }
    }
    if (threadIdx.x < 16) flags[threadIdx.x] = 0u;
    SB();
  } else {
    const v4u ksd = make_srd(kc, 64 << 20);
    dma16<true>(lds0 + L_K0 + (wave - 4) * 2048, koff, ksd, 0);
    dma16<true>(lds0 + L_K0 + (wave - 4) * 2048 + 1024, koff, ksd, 8192);
    STAMP(1);
    const v4u srd = make_srd(wq, DM * HQ * DH * 2);
    const int vo = (sp * 128 + (wave - 4) * 16 + (lane >> 3)) * 8192 + kvh * 1024 + b * 128 + (lane & 7) * 16;
    dma16<true>(lds0 + L_B + (wave - 4) * 2048, vo, srd, 0);
    dma16<true>(lds0 + L_B + (wave - 4) * 2048 + 1024, vo, srd, 8 * 8192);
    SB();
  }
  WG_BARRIER();
  const int wgl = (b << 5) | jb;
  const int cj = (b & 3) * 4 + sp, bh = b >> 2;
  const int partg = WS_PART + (kvh * 2 + bh) * 16384;
  const int me = (b & 3) * 4 + sp;
  const int fjb = (b + 3 * (b & 3)) & 7;
  const bool fin = jb == fjb;

  if (wave < 4) {
    __builtin_amdgcn_s_setprio(3);
    if (wave != 1) {
      WAITV(0);
      if (lane == 0) __hip_atomic_fetch_add(flags + F_XW, 1u, RLX_WG);
    }
    if (wave == 0) {
      *(LAS v4u*)(lds + L_X + 8 * 1040 + lane * 16) = v4u{0x3f803f80u, 0x3f803f80u, 0x3f803f80u, 0x3f803f80u};
      while ((__hip_atomic_load(flags + F_XW, RLX_WG) & 255u) != 4u) __builtin_amdgcn_s_sleep(1);
      CFENCE();
      STAMP(1);
      const int xo = L_X + (r < 8 ? r : 8) * 1040 + g * 16;
      f32x4 d2a = {0.f, 0.f, 0.f, 0.f}, d2b = {0.f, 0.f, 0.f, 0.f};
#pragma unroll
      for (int s = 0; s < 16; s += 2) {
        const v4u xa = *(LAS v4u*)(lds + xo + s * 64), xb = *(LAS v4u*)(lds + xo + s * 64 + 64);
        d2a = mfma16(xa, xa, d2a); d2b = mfma16(xb, xb, d2b);
      }
      d2a += d2b;
      const float dg = (r & 2) ? ((r & 1) ? d2a[3] : d2a[2]) : ((r & 1) ? d2a[1] : d2a[0]);
      const float sxx = xrow_sum(g == (r >> 2) ? dg : 0.f);
      const float mean = xrow_sum(g == 2 ? d2a[0] : 0.f) * (1.f / DM);
      float rstd = rsqrtf(fmaxf(sxx * (1.f / DM) - mean * mean, 0.f) + 1e-5f);
      v4u xb[4];
#pragma unroll
      for (int s = 0; s < 4; ++s) { xb[s] = *(LAS v4u*)(lds + xo + sp * 256 + s * 64); asm volatile("" : "+v"(xb[s])); }
      asm volatile("" : "+v"(rstd));
      while (__hip_atomic_load(flags + F_XW, RLX_WG) != 4u + 8u * 256u) __builtin_amdgcn_s_sleep(1);
      CFENCE();
      STAMP(2);
      f32x4 d[4];
#pragma unroll
      for (int nt = 0; nt < 4; ++nt) d[nt] = f32x4{0.f, 0.f, 0.f, 0.f};
      const int troff = L_B + (8 * g + (r >> 2)) * 128 + (r & 3) * 8;
#pragma unroll
      for (int s = 0; s < 4; ++s)
#pragma unroll
        for (int nt = 0; nt < 4; ++nt) {
          const v2u u0 = __builtin_bit_cast(v2u, __builtin_amdgcn_ds_read_tr16_b64_v4i16((LAS v4s*)(lds + troff + s * 4096 + nt * 32)));
          const v2u u1 = __builtin_bit_cast(v2u, __builtin_amdgcn_ds_read_tr16_b64_v4i16((LAS v4s*)(lds + troff + s * 4096 + 512 + nt * 32)));
          d[nt] = mfma16(v4u{u0.x, u0.y, u1.x, u1.y}, xb[s], d[nt]);
        }
#pragma unroll
      for (int nt = 0; nt < 4; ++nt) {
        float qv[4];
#pragma unroll
        for (int i = 0; i < 4; ++i) qv[i] = (d[nt][i] - mean * dpp<0x158>(d[nt][i])) * rstd;
        if (r < 8) *(LAS v2u*)(lds + L_QO + r * 128 + nt * 32 + g * 8) = v2u{cvtpk(qv[0], qv[1]), cvtpk(qv[2], qv[3])};
      }
      LGKM0();
      if (lane == 0) __hip_atomic_store(flags + F_QO, 1u, RLX_WG);
      STAMP(3);
    } else if (wave == 1) {
      const int emo = WS_EM + wgl * 512 + lane * 8;
      const int qdo = WS_QD + (kvh * 8 + g) * 8192 + jk * 256 + r * 16;
      const int nno = lane < 32 ? WS_NK + kvh * 128 + lane * 4 : WS_NB + b * 128 + (lane - 32) * 4;
      const int eco = WS_EM + (lane < 32 ? ((((lane & 7) << 5) | (lane & 24) | kvh) * 64 + jk) : (((b << 5) | (lane - 32)) * 64 + 32 + jb)) * 8;
      const v4u e4 = {EMPTY, EMPTY, EMPTY, EMPTY};
      __builtin_amdgcn_raw_buffer_store_b128(e4, wsr, partg + me * 1024 + lane * 16, 0, AUX_SC1);
      if (lane < 42)
        __builtin_amdgcn_raw_buffer_store_b128(e4, wsr, (lane < 32 ? WS_OUTP + (b * 8 + kvh) * 2048 + sp * 512 : lane < 36 ? WS_STAT - 512 + wgl * 128 : WS_TAB - 576 + wgl * 96) + lane * 16, 0, AUX_SC1);
      SB();
      v2u em = __builtin_amdgcn_raw_buffer_load_b64(wsr, emo, 0, AUX_SC1);
      v4u s0 = __builtin_amdgcn_raw_buffer_load_b128(wsr, qdo, 0, AUX_SC1);
      v4u s1 = __builtin_amdgcn_raw_buffer_load_b128(wsr, qdo, 4 * 8192, AUX_SC1);
      SB();
      WAITV(6);
      if (lane == 0) __hip_atomic_fetch_add(flags + F_XW, 1u, RLX_WG);
      pace<NNT>(tstart);
      STAMP(1);
      unsigned nn = __builtin_amdgcn_raw_buffer_load_b32(wsr, nno, 0, AUX_SC1);
      asm volatile("s_waitcnt vmcnt(0)" : "+v"(em), "+v"(s0), "+v"(s1), "+v"(nn) :: "memory");
      unsigned nonce = tstart;
      while (__any((em[1] == nonce) | (s0[1] == nonce) | (s0[3] == nonce) | (s1[1] == nonce) | (s1[3] == nonce))) ++nonce;
      if (nonce != tstart) {
        if (lane < 2) __builtin_amdgcn_raw_buffer_store_b32(nonce, wsr, lane ? WS_NB + b * 128 + jb * 4 : WS_NK + kvh * 128 + jk * 4, 0, AUX_SC1);
        WAITV(0);
      }
      if (lane < 32) __builtin_amdgcn_raw_buffer_store_b64(v2u{nonce, nn}, wsr, eco, 0, AUX_SC1);
      if (lane == 0) __hip_atomic_store(flags + F_CLR, 1u, RLX_WG);
      STAMP(2);
      unsigned cand[8];
#pragma unroll
      for (int i = 0; i < 8; ++i) cand[i] = __builtin_amdgcn_ds_bpermute((4 * i + g) * 4, nn);
      lds_wait_ne0(flags + F_QO);
      {
        const v2u q0 = *(LAS v2u*)(lds + L_QO + lane * 8), q1 = *(LAS v2u*)(lds + L_QO + 512 + lane * 8);
        __builtin_amdgcn_raw_buffer_store_b128(v4u{q0.x, nonce, q0.y, nonce}, wsr, qdo, 0, AUX_SC1);
        __builtin_amdgcn_raw_buffer_store_b128(v4u{q1.x, nonce, q1.y, nonce}, wsr, qdo, 4 * 8192, AUX_SC1);
      }
      STAMP(3);
      const int qio = WS_QD + (kvh * 8 + b) * 8192 + lane * 16;
      v4u gq[8];
#pragma unroll
      for (int i = 0; i < 8; ++i) gq[i] = v4u{0u, ~cand[i], 0u, 0u};
      pace<QT1>(tstart);
      for (unsigned spin = 0; spin < SPIN_CAP; ++spin) {
        CFENCE();
        const unsigned n2 = __builtin_amdgcn_raw_buffer_load_b32(wsr, nno, 0, AUX_SC1);
        if (lane < 32) em = __builtin_amdgcn_raw_buffer_load_b64(wsr, emo, 0, AUX_SC1);
#pragma unroll
        for (int i = 0; i < 8; ++i)
          if ((gq[i][1] != cand[i]) | (gq[i][3] != cand[i])) gq[i] = __builtin_amdgcn_raw_buffer_load_b128(wsr, qio, i * 1024, AUX_SC1);
        if (__any(n2 != nn)) {
          if (n2 != nn && lane < 32) __builtin_amdgcn_raw_buffer_store_b64(v2u{nonce, n2}, wsr, eco, 0, AUX_SC1);
          nn = n2;
#pragma unroll
          for (int i = 0; i < 8; ++i) cand[i] = __builtin_amdgcn_ds_bpermute((4 * i + g) * 4, nn);
        }
        bool ok = (lane >= 32) | ((em[1] == nonce) & (em[0] == nn));
#pragma unroll
        for (int i = 0; i < 8; ++i) ok = ok & (gq[i][1] == cand[i]) & (gq[i][3] == cand[i]);
        if (__all(ok)) break;
        __builtin_amdgcn_s_sleep(QSL);
      }
#pragma unroll
      for (int hf = 0; hf < 2; ++hf) {
        float q0 = 0.f, q1 = 0.f, q2 = 0.f, q3 = 0.f;
#pragma unroll
        for (int kq = 0; kq < 4; ++kq) {
          const v4u t4 = gq[2 * kq + hf];
          q0 += bflo(t4[0]); q1 += bfhi(t4[0]); q2 += bflo(t4[2]); q3 += bfhi(t4[2]);
        }
        *(LAS v2u*)(lds + L_Q + hf * 512 + lane * 8) = v2u{cvtpk(q0, q1), cvtpk(q2, q3)};
      }
      LGKM0();
      if (lane == 0) __hip_atomic_store(flags + F_Q, 1u, RLX_WG);
      STAMP(4);
      const bool r2 = lane >= 32 && (fin || lane - 32 == fjb);
      if (r2) __builtin_amdgcn_raw_buffer_store_b64(v2u{nonce, nn}, wsr, eco, 0, AUX_SC1);
      for (unsigned spin = 0; spin < SPIN_CAP; ++spin) {
        __builtin_amdgcn_s_sleep(48);
        CFENCE();
        unsigned n2 = nn;
        if (r2) {
          n2 = __builtin_amdgcn_raw_buffer_load_b32(wsr, nno, 0, AUX_SC1);
          em = __builtin_amdgcn_raw_buffer_load_b64(wsr, emo, 0, AUX_SC1);
          if (n2 != nn) __builtin_amdgcn_raw_buffer_store_b64(v2u{nonce, n2}, wsr, eco, 0, AUX_SC1);
        }
        nn = n2;
        if (__all(!r2 | ((em[1] == nonce) & (em[0] == nn)))) break;
      }
      STAMP(7);
      if (fin) {
        pace<TABT>(tstart);
#pragma unroll 1
        for (int c = 0; c < 3; ++c) {
          for (unsigned spin = 0; spin < SPIN_CAP; ++spin) {
            CFENCE();
            v4u tv[8];
#pragma unroll
            for (int i = 0; i < 8; ++i) tv[i] = __builtin_amdgcn_raw_buffer_load_b128(wsr, WS_TAB + c * 8192 + lane * 16, i * 1024, AUX_SC1);
            bool ok = true;
#pragma unroll
            for (int i = 0; i < 8; ++i) {
              ok = ok & (tv[i][1] == 1u) & (tv[i][3] == 1u);
              *(LAS v2u*)(lds + L_T + c * 4096 + i * 512 + lane * 8) = v2u{tv[i][0], tv[i][2]};
            }
            if (__all(ok)) break;
            __builtin_amdgcn_s_sleep(16);
          }
        }
        LGKM0();
        if (lane == 0) __hip_atomic_store(flags + F_FIN, 1u, RLX_WG);
        asm volatile(".Lpf_end_a:");
        STAMP(8);
      }
    } else if (wave == 2) {
      pace<PART>(tstart);
      const v4u s1 = make_srd(w1, FF * 2), s2 = make_srd(b1, FF * 2), s3 = make_srd(w2, FF * 2);
#pragma unroll
      for (int i = 0; i < 3; ++i) {
        dma16<false>(lds0 + L_P + i * 1024, voff, s1, i * 1024);
        dma16<false>(lds0 + L_P + 3584 + i * 1024, voff, s2, i * 1024);
        dma16<false>(lds0 + L_P + 7168 + i * 1024, voff, s3, i * 1024);
      }
      if (lane < 32) {
        dma16<false>(lds0 + L_P + 3072, voff, s1, 3072);
        dma16<false>(lds0 + L_P + 3584 + 3072, voff, s2, 3072);
        dma16<false>(lds0 + L_P + 7168 + 3072, voff, s3, 3072);
      }
      const unsigned b2u = b2[0];
      if (lane == 0) flags[F_B2] = b2u;
      asm volatile("s_waitcnt vmcnt(0) lgkmcnt(0)" ::: "memory");
      if (lane == 0) __hip_atomic_store(flags + F_P, 1u, RLX_WG);
      STAMP(1);
    }
    if (wave != 1) {
      __builtin_amdgcn_s_setprio(0);
      lds_wait_ne0(flags + F_P);
      lds_wait_ne0(flags + F_CLR);
      const int p0 = wave == 0 ? 0 : 2 * wave - 2;
#pragma unroll 1
      for (int ps = p0; ps < p0 + 2; ++ps) {
        const unsigned bits = (unsigned)((wgl & 127) * 24 + ps * 4 + (TAB_E0 << 7)) | (unsigned)((wgl >> 7) << 15);
        f32x2 t[2], a[2];
#pragma unroll
        for (int p = 0; p < 2; ++p) t[p] = f32x2{bflo(bits + 2 * p), bflo(bits + 2 * p + 1)};
        ffn_eval<2>(lds, lane, t, a);
        const unsigned k0 = cvtpk(wave_sum(a[0][0]), wave_sum(a[0][1])), k1 = cvtpk(wave_sum(a[1][0]), wave_sum(a[1][1]));
        __builtin_amdgcn_s_setprio(3);
        if (lane == 0) __builtin_amdgcn_raw_buffer_store_b128(v4u{k0, 1u, k1, 1u}, wsr, WS_TAB + wgl * 96 + ps * 16, 0, AUX_SC1);
        __builtin_amdgcn_s_setprio(0);
      }
      __builtin_amdgcn_s_setprio(3);
      STAMP(6);
    }
    if (wave == 0) {
      while (__hip_atomic_load(flags + F_CNT, RLX_WG) != 8u) __builtin_amdgcn_s_sleep(2);
      CFENCE();
      STAMP(7);
      const int hd = lane & 3;
      float M = -INFINITY, L = 0.f;
#pragma unroll
      for (int w = 0; w < 8; ++w) M = fmaxf(M, ((LAS float*)(lds + L_ML))[w * 8 + hd]);
#pragma unroll
      for (int w = 0; w < 8; ++w) L = fmaf(__builtin_amdgcn_exp2f(((LAS float*)(lds + L_ML))[w * 8 + hd] - M), ((LAS float*)(lds + L_ML))[w * 8 + 4 + hd], L);
      if (lane < 4) __builtin_amdgcn_raw_buffer_store_b128(v4u{__float_as_uint(M), 1u, __float_as_uint(L), 1u}, wsr, WS_STAT + wgl * 128 + lane * 16, 0, AUX_SC1);
      v4u sv = {0u, 1u, 0u, 1u};
      for (unsigned spin = 0; spin < SPIN_CAP; ++spin) {
        __builtin_amdgcn_s_sleep(8);
        CFENCE();
        if (lane < 16) sv = __builtin_amdgcn_raw_buffer_load_b128(wsr, WS_STAT + ((b << 5) | ((lane >> 2) << 3) | kvh) * 128 + hd * 16, 0, AUX_SC1);
        if (__all((sv[1] == 1u) & (sv[3] == 1u))) break;
      }
      const float ms = __uint_as_float(sv[0]);
      float Mg = fmaxf(ms, dpp<0x124>(ms)); Mg = fmaxf(Mg, dpp<0x128>(Mg));
      float Lg = __builtin_amdgcn_exp2f(ms - Mg) * __uint_as_float(sv[2]);
      Lg += dpp<0x128>(Lg); Lg += dpp<0x124>(Lg);
      if (lane < 4) ((LAS float*)(lds + L_Z))[lane] = Mg + __builtin_amdgcn_logf(Lg);
      LGKM0();
      if (lane == 0) __hip_atomic_store(flags + F_Z, 1u, RLX_WG);
      STAMP(8);
    }
  } else {
    const int sw = wave - 4, tid = threadIdx.x - 256;
    const __amdgpu_buffer_rsrc_t vrs = __builtin_amdgcn_make_buffer_rsrc((void*)vc, 0, 64 << 20, 0x00020000);
    const int vvo = kvo + voff;
    const v4u wsd = make_srd(wo, HQ * DH * DM * 2);
    const int wo_ = (kvh * 512 + sw + ((lane >> 2) & 3) * 128 + (lane >> 4) * 8) * 1024 + cj * 64 + (lane & 3) * 16;
#define WPIECE(k) do { dma16<false>(lds0 + L_W + sw * 4096 + (k) * 1024, wo_, wsd, (k) * 32 * 1024); SB(); } while (0)
#define VLOAD(i) do { WAITV(VW - 1); pace<sched(32 + (i))>(tstart); vf[i] = __builtin_amdgcn_raw_buffer_load_b128(vrs, vvo, (i) * 8192, AUX_NT); SB(); \
    if ((i) == 3) WPIECE(2); if ((i) == 11) WPIECE(3); } while (0)
    const __amdgpu_buffer_rsrc_t krs = __builtin_amdgcn_make_buffer_rsrc((void*)kc, 0, 64 << 20, 0x00020000);
    v4u kf[32], vf[32];
    __builtin_amdgcn_s_setprio(2);
    static_for<KFIRST, 32>([&](auto ic) {
      constexpr int i = ic.value;
      WAITV((i < KSW ? KW1 : KW) - 1);
      pace<sched(i)>(tstart);
      if (i == XWSIG) { WAITV(XWSIG - KFIRST); if (lane == 0) __hip_atomic_fetch_add(flags + F_XW, 256u, RLX_WG); }
      KLOAD(i);
      if (i == 15) WPIECE(0);
      if (i == 23) WPIECE(1);
    });
    STAMP(3);
    static_for<0, XV>([&](auto ic) { VLOAD(ic.value); });
    STAMP(6);
    lds_wait_ne0(flags + F_Q);
    SB();
    const v4u qf = *(LAS v4u*)(lds + L_Q + (r & 3) * 256 + g * 64 + (r >> 2) * 16);
    kf[0] = *(LAS v4u*)(lds + L_K0 + sw * 2048 + voff);
    kf[1] = *(LAS v4u*)(lds + L_K0 + sw * 2048 + 1024 + voff);
    float s[32];
    static_for<0, 32>([&](auto ic) {
      constexpr int i = ic.value;
      const f32x4 a = mfma16(kf[i], qf, f32x4{0.f, 0.f, 0.f, 0.f});
      float t_ = a[0] + dpp<0x104>(a[1]); asm volatile("" : "+v"(t_));
      t_ += dpp<0x108>(a[2]); asm volatile("" : "+v"(t_));
      s[i] = t_ + dpp<0x10C>(a[3]); asm volatile("" : "+v"(s[i]));
      if constexpr ((i & 7) == 7) { SB(); VLOAD(XV + (i >> 3)); }
    });
    SB();
    float m = s[0];
#pragma unroll
    for (int i = 1; i < 32; ++i) m = fmaxf(m, s[i]);
    m = xrow_max(m) * SCALE_L2;
    v4u pf[4];
#define SOFTMAX(G, mm) do { float p_[8]; \
    _Pragma("unroll") for (int j = 0; j < 8; ++j) p_[j] = __builtin_amdgcn_exp2f(fmaf(s[8 * (G) + j], SCALE_L2, -(mm))); \
    pf[G] = v4u{cvtpk(p_[0], p_[1]), cvtpk(p_[2], p_[3]), cvtpk(p_[4], p_[5]), cvtpk(p_[6], p_[7])}; } while (0)
    {
      SOFTMAX(0, m); SOFTMAX(1, m); SOFTMAX(2, m); SOFTMAX(3, m);
      const v4u ones = {0x3f803f80u, 0x3f803f80u, 0x3f803f80u, 0x3f803f80u};
      f32x4 l4 = {0.f, 0.f, 0.f, 0.f};
#pragma unroll
      for (int G = 0; G < 4; ++G) l4 = mfma16(pf[G], ones, l4);
      if (lane < 4) ((LAS float*)(lds + L_ML))[sw * 8 + lane] = m;
      if (lane == 0) *(LAS f32x4*)(lds + L_ML + sw * 32 + 16) = l4;
      LGKM0();
      if (lane == 0) __hip_atomic_fetch_add(flags + F_CNT, 1u, RLX_WG);
    }
    STAMP(7);
    f32x4 acc[8];
#pragma unroll
    for (int j = 0; j < 8; ++j) acc[j] = f32x4{0.f, 0.f, 0.f, 0.f};
#define PV(G) do { \
    _Pragma("unroll") for (int d = 0; d < 4; ++d) { v4u lo_, hi_; \
      _Pragma("unroll") for (int jj = 0; jj < 4; ++jj) { \
        lo_[jj] = __builtin_amdgcn_perm(vf[8 * (G) + 2 * jj + 1][d], vf[8 * (G) + 2 * jj][d], 0x05040100u); \
        hi_[jj] = __builtin_amdgcn_perm(vf[8 * (G) + 2 * jj + 1][d], vf[8 * (G) + 2 * jj][d], 0x07060302u); } \
      acc[2 * d] = mfma16(pf[G], lo_, acc[2 * d]); acc[2 * d + 1] = mfma16(pf[G], hi_, acc[2 * d + 1]); } SB(); } while (0)
    static_for<XV + 4, 32>([&](auto ic) {
      constexpr int i = ic.value;
      VLOAD(i);
      if constexpr (i == 7 + PVD) PV(0);
      if constexpr (i == 15 + PVD) PV(1);
      if constexpr (i == 23 + PVD) PV(2);
    });
    STAMP(8);
    v4u wf[2][2];
    {
      const int wtr = L_W + sw * 4096 + (g * 8 + (r >> 2)) * 64 + (r & 3) * 8;
#pragma unroll
      for (int ks = 0; ks < 2; ++ks)
#pragma unroll
        for (int nt = 0; nt < 2; ++nt) {
          const v2u u0 = __builtin_bit_cast(v2u, __builtin_amdgcn_ds_read_tr16_b64_v4i16((LAS v4s*)(lds + wtr + ks * 2048 + nt * 32)));
          const v2u u1 = __builtin_bit_cast(v2u, __builtin_amdgcn_ds_read_tr16_b64_v4i16((LAS v4s*)(lds + wtr + ks * 2048 + 256 + nt * 32)));
          wf[ks][nt] = v4u{u0.x, u0.y, u1.x, u1.y};
          asm volatile("" : "+v"(wf[ks][nt]));
        }
      SB();
    }
    lds_wait_ne0(flags + F_Z);
    SB();
    {
      const f32x4 Z4 = *(LAS f32x4*)(lds + L_Z);
      const f32x4 mo = *(LAS f32x4*)(lds + L_ML + sw * 32);
      const float zl = ((LAS float*)(lds + L_Z))[lane & 3];
      f32x4 f4;
#pragma unroll
      for (int i = 0; i < 4; ++i) f4[i] = __builtin_amdgcn_exp2f(mo[i] - Z4[i]);
#pragma unroll
      for (int j = 0; j < 8; ++j) acc[j] *= f4;
      const float zz = (r < 4) ? zl : m;
      SOFTMAX(3, zz);
      SB();
    }
    {
      v4u lo_[4], hi_[4];
#pragma unroll
      for (int jj = 0; jj < 4; ++jj) {
#pragma unroll
        for (int d = 0; d < 4; ++d) {
          lo_[d][jj] = __builtin_amdgcn_perm(vf[24 + 2 * jj + 1][d], vf[24 + 2 * jj][d], 0x05040100u);
          hi_[d][jj] = __builtin_amdgcn_perm(vf[24 + 2 * jj + 1][d], vf[24 + 2 * jj][d], 0x07060302u);
        }
        SB();
      }
#pragma unroll
      for (int d = 0; d < 4; ++d) { acc[2 * d] = mfma16(pf[3], lo_[d], acc[2 * d]); acc[2 * d + 1] = mfma16(pf[3], hi_[d], acc[2 * d + 1]); }
      SB();
    }
    STAMP(9);
    if (lane < 16) {
#pragma unroll
      for (int j = 0; j < 8; ++j) *(LAS f32x4*)(lds + L_B + ((sw * 8 + j) * 16 + lane) * 16) = acc[j];
    }
    WG_BARRIER();
    STAMP(10);
    LAS unsigned char* aim = lds + L_A + sw * 2304;
    if (lane < 16) {
      f32x4 o = *(LAS f32x4*)(lds + L_B + (sw * 16 + lane) * 16);
#pragma unroll
      for (int w = 1; w < 8; ++w) o += *(LAS f32x4*)(lds + L_B + ((w * 8 + sw) * 16 + lane) * 16);
      const v2u op = {cvtpk(o[0], o[1]), min(cvtpk(o[2], o[3]), EMPTY - 1u)};
      __builtin_amdgcn_raw_buffer_store_b64(op, wsr, partg + me * 1024 + sw * 128 + lane * 8, 0, AUX_SC1);
      *(LAS v2u*)(aim + me * 144 + lane * 8) = op;
    }
    SB();
    {
      const int po = partg + (lane >> 3) * 1024 + sw * 128 + (lane & 7) * 16;
      bool ok0 = (lane >> 3) == me, ok1 = (lane >> 3) + 8 == me;
      __builtin_amdgcn_s_sleep(H2D);
      for (unsigned spin = 0; spin < SPIN_CAP; ++spin) {
        CFENCE();
        v4u p0 = {EMPTY, EMPTY, EMPTY, EMPTY}, p1 = {EMPTY, EMPTY, EMPTY, EMPTY};
        if (!ok0) p0 = __builtin_amdgcn_raw_buffer_load_b128(wsr, po, 0, AUX_SC1);
        if (!ok1) p1 = __builtin_amdgcn_raw_buffer_load_b128(wsr, po, 8192, AUX_SC1);
        if ((p0[1] != EMPTY) & (p0[3] != EMPTY)) { ok0 = true; *(LAS v4u*)(aim + (lane >> 3) * 144 + (lane & 7) * 16) = p0; }
        if ((p1[1] != EMPTY) & (p1[3] != EMPTY)) { ok1 = true; *(LAS v4u*)(aim + (lane >> 3) * 144 + 1152 + (lane & 7) * 16) = p1; }
        if (__all(ok0 & ok1)) break;
      }
      STAMP(11);
      f32x4 c0 = {0.f, 0.f, 0.f, 0.f}, c1 = {0.f, 0.f, 0.f, 0.f};
#pragma unroll
      for (int ks = 0; ks < 2; ++ks) {
        const v4u af = *(LAS v4u*)(aim + r * 144 + ks * 64 + g * 16);
        c0 = mfma16(af, wf[ks][0], c0);
        c1 = mfma16(af, wf[ks][1], c1);
      }
      LAS float* red = (LAS float*)(lds + L_W);
      red[sw * 128 + lane] = (c0[0] + c0[1]) + (c0[2] + c0[3]);
      red[sw * 128 + 64 + lane] = (c1[0] + c1[1]) + (c1[2] + c1[3]);
    }
    WG_BARRIER();
    if (sw < 2) {
      LAS float* red = (LAS float*)(lds + L_W);
      float a = red[tid];
#pragma unroll
      for (int w = 1; w < 8; ++w) a += red[w * 128 + tid];
      __builtin_amdgcn_raw_buffer_store_b32(min(__float_as_uint(a), EMPTY - 1u), wsr, WS_OUTP + (((bh * 4 + g) * 8 + kvh) * 512 + cj * 32 + sw * 16 + r) * 4, 0, AUX_SC1);
    }
    STAMP(12);
    if (fin) {
      lds_wait_ne0(flags + F_FIN);
      const float xres = bflo(((LAS us*)(lds + L_X))[b * 520 + tid]), b2v = bflo(flags[F_B2]);
      unsigned pv[8];
#pragma unroll
      for (int k = 0; k < 8; ++k) pv[k] = EMPTY;
      for (unsigned spin = 0; spin < SPIN_CAP; ++spin) {
        CFENCE();
#pragma unroll
        for (int k = 0; k < 8; ++k)
          if (pv[k] == EMPTY) pv[k] = __builtin_amdgcn_raw_buffer_load_b32(wsr, WS_OUTP + b * 16384 + tid * 4, k * 2048, AUX_SC1);
        bool ok = true;
#pragma unroll
        for (int k = 0; k < 8; ++k) ok = ok & (pv[k] != EMPTY);
        if (__all(ok)) break;
      }
      STAMP(13);
      const float a = ((__uint_as_float(pv[0]) + __uint_as_float(pv[1])) + (__uint_as_float(pv[2]) + __uint_as_float(pv[3]))) +
                      ((__uint_as_float(pv[4]) + __uint_as_float(pv[5])) + (__uint_as_float(pv[6]) + __uint_as_float(pv[7])));
      const float h = rbf(xres + rbf(a));
      const float s1 = wave_sum(h), s2 = wave_sum(h * h);
      if (lane == 0) *(LAS f32x2*)(lds + L_B + sw * 8) = f32x2{s1, s2};
      WG_BARRIER();
      const f32x4 q0 = *(LAS f32x4*)(lds + L_B), q1 = *(LAS f32x4*)(lds + L_B + 16), q2 = *(LAS f32x4*)(lds + L_B + 32), q3 = *(LAS f32x4*)(lds + L_B + 48);
      const f32x4 qs = (q0 + q1) + (q2 + q3);
      const float mean = (qs[0] + qs[2]) * (1.f / DM);
      const float var = fmaxf((qs[1] + qs[3]) * (1.f / DM) - mean * mean, 0.f);
      const float r1 = __builtin_amdgcn_rsqf(var + 1e-5f);
      const float r2 = __builtin_amdgcn_rsqf(fmaf(var * r1, r1, 1e-5f));
      const float t = ((h - mean) * r1) * r2;
      const unsigned tb = cvtpk(t, t) & 0xffffu;
      const unsigned rel = (tb & 0x7fffu) - (unsigned)(TAB_E0 << 7);
      const bool inr = rel < (unsigned)TAB_H;
      float y = bflo(((LAS us*)(lds + L_T))[(inr ? rel : 0u) + (tb >> 15) * TAB_H]);
      u64 miss = __ballot(!inr);
      while (miss) {
        const int l = __builtin_ctzll(miss);
        miss &= miss - 1;
        const float ts = bflo((unsigned)__builtin_amdgcn_readlane((int)tb, l));
        const f32x2 tt[1] = {f32x2{ts, ts}};
        f32x2 aa[1];
        ffn_eval<1>(lds, lane, tt, aa);
        const float ys = rbf(wave_sum(aa[0][0]));
        if (lane == l) y = ys;
      }
      const float ov = h + rbf(y + b2v);
      out[b * DM + tid] = (us)(cvtpk(ov, ov) & 0xffffu);
      STAMP(14);
    }
  }
  asm volatile(".Lpf_end_b:");
}

extern "C" void kernel_launch(void* const* d_in, const int* in_sizes, int n_in, void* d_out, int out_size, void* d_ws, size_t ws_size, hipStream_t stream) {
  fused_kernel<<<256, 768, 0, stream>>>((const us*)d_in[0], (const us*)d_in[1], (const us*)d_in[2], (const us*)d_in[3], (const us*)d_in[4],
                                        (const us*)d_in[6], (const us*)d_in[7], (const us*)d_in[8], (const us*)d_in[9], (us*)d_out, (unsigned char*)d_ws);
}
```

```cpp
#include <hip/hip_runtime.h>
#include <hip/hip_bf16.h>
#include <stdint.h>
#include <type_traits>

#define GAS __attribute__((address_space(1)))
#define LAS __attribute__((address_space(3)))
#define RLX_AGENT __ATOMIC_RELAXED, __HIP_MEMORY_SCOPE_AGENT
#define RLX_WG __ATOMIC_RELAXED, __HIP_MEMORY_SCOPE_WORKGROUP
typedef unsigned v4u __attribute__((ext_vector_type(4)));
typedef unsigned v2u __attribute__((ext_vector_type(2)));
typedef float f32x4 __attribute__((ext_vector_type(4)));
typedef float f32x2 __attribute__((ext_vector_type(2)));
typedef short bf16x8 __attribute__((ext_vector_type(8)));
typedef short v4s __attribute__((ext_vector_type(4)));
typedef __bf16 bf16x2_t __attribute__((ext_vector_type(2)));
typedef unsigned long long u64;
typedef GAS unsigned gu32;
typedef GAS u64 gu64;
typedef unsigned short us;

constexpr int DM = 512, HQ = 32, DH = 128, FF = 1792;
constexpr float LOG2E = 1.4426950408889634f;
constexpr float SCALE_L2 = 0.08838834764831845f * LOG2E;
constexpr unsigned SPIN_CAP = 200000u;
constexpr int AUX_NT = 2, AUX_SC1 = 16;
constexpr unsigned EMPTY = 0xffffffffu;
constexpr int TAB_E0 = 107;
constexpr int TAB_H = 24 * 128;

#define NSLEEP 4
#define H2D 11
#define CODEPF 16
#define KFIRST 2
#define PER16 472
#define LEAD 2
#define NE 24
#define EX16 120
#define WC16 200
#define XWSIG 5
#define PVD 5
#define XV 2
#define PSL 2
#define PSLT 7
#define NNT 130
#define KW 4
#define VW 6
#define PART 250
#define PRETOUCH 0
#define KW1 4
#define KSW 16
#define MAPB 1
#define QT1 480
#define QSL 8
#define NS 34
#define SP16 8
#define TBASE 1
#define TBO -7
#define STG 30
#define WP0 15
#define WP1 23
#define WP2 35
#define WP3 43
#define PFSPLIT 0
#define QD1 16
#define TABT 1200
constexpr int sched(int n) {
  return ((n - LEAD) * PER16 + (n < NE ? n : NE) * EX16 - (n > NS ? n - NS : 0) * SP16 + ((n > WP0) + (n > WP1) + (n > WP2) + (n > WP3)) * WC16) >> 4;
}

constexpr int WS_NK = 0;
constexpr int WS_NB = 1024;
constexpr int WS_STAT = 4096;
constexpr int WS_TAB = WS_STAT + 256 * 128;
constexpr int WS_EM = WS_TAB + 256 * 128;
constexpr int WS_PART = WS_EM + 256 * 512;
constexpr int WS_OUTP = WS_PART + 256 * 1024;
constexpr int WS_QD = WS_OUTP + 64 * 2048;
constexpr int WS_END = WS_QD + 64 * 8192;
constexpr size_t WS_STAMP = 16u << 20;

constexpr int L_W = 0;
constexpr int L_B = 32768;
constexpr int L_X = L_B + 16384;
constexpr int L_P = L_X + 9472;
constexpr int L_T = L_P + 10752;
constexpr int L_Q = L_T + 16384;
constexpr int L_QO = L_Q + 1024;
constexpr int L_ML = L_QO + 1024;
constexpr int L_Z = L_ML + 256;
constexpr int L_FL = L_Z + 64;
constexpr int L_A = L_FL + 64;
constexpr int L_K0 = L_A + 8 * 2304;
constexpr int L_TOTAL = L_K0 + 8 * 2048;
enum { F_Q = 0, F_CNT = 1, F_QO = 2, F_Z = 3, F_P = 4, F_FIN = 5, F_XW = 6, F_B2 = 7, F_CLR = 8, F_TB = 9 };

__device__ __forceinline__ f32x4 mfma16(v4u a, v4u b, f32x4 c) {
  return __builtin_amdgcn_mfma_f32_16x16x32_bf16(__builtin_bit_cast(bf16x8, a), __builtin_bit_cast(bf16x8, b), c, 0, 0, 0);
}
__device__ __forceinline__ unsigned cvtpk(float lo, float hi) {
  return __builtin_bit_cast(unsigned, __builtin_convertvector(f32x2{lo, hi}, bf16x2_t));
}
__device__ __forceinline__ float bflo(unsigned u) { return __builtin_bit_cast(float, u << 16); }
__device__ __forceinline__ float bfhi(unsigned u) { return __builtin_bit_cast(float, u & 0xffff0000u); }
__device__ __forceinline__ float rbf(float x) { return bflo(cvtpk(x, x)); }
__device__ __forceinline__ float xrow_max(float x) {
  auto s = __builtin_amdgcn_permlane16_swap(__float_as_uint(x), __float_as_uint(x), false, false);
  x = fmaxf(__uint_as_float(s[0]), __uint_as_float(s[1]));
  auto t = __builtin_amdgcn_permlane32_swap(__float_as_uint(x), __float_as_uint(x), false, false);
  return fmaxf(__uint_as_float(t[0]), __uint_as_float(t[1]));
}
__device__ __forceinline__ float xrow_sum(float x) {
  auto s = __builtin_amdgcn_permlane16_swap(__float_as_uint(x), __float_as_uint(x), false, false);
  x = __uint_as_float(s[0]) + __uint_as_float(s[1]);
  auto t = __builtin_amdgcn_permlane32_swap(__float_as_uint(x), __float_as_uint(x), false, false);
  return __uint_as_float(t[0]) + __uint_as_float(t[1]);
}
template <int CTRL> __device__ __forceinline__ float dpp(float x) {
  return __builtin_bit_cast(float, __builtin_amdgcn_mov_dpp(__builtin_bit_cast(int, x), CTRL, 0xf, 0xf, true));
}
__device__ __forceinline__ float wave_sum(float x) {
  x += dpp<0xB1>(x); x += dpp<0x4E>(x); x += dpp<0x141>(x); x += dpp<0x128>(x);
  return xrow_sum(x);
}
#define SB() __builtin_amdgcn_sched_barrier(0)
#define CFENCE() asm volatile("" ::: "memory")
#define LGKM0() asm volatile("s_waitcnt lgkmcnt(0)" ::: "memory")
#define WAITV(n) asm volatile("s_waitcnt vmcnt(%0)" :: "i"(n) : "memory")
#define WG_BARRIER() do { asm volatile("s_waitcnt lgkmcnt(0)\n\ts_barrier" ::: "memory"); SB(); } while (0)
__device__ __forceinline__ v4u make_srd(const void* p, unsigned bytes) {
  const u64 a = (u64)p;
  v4u d;
  d[0] = __builtin_amdgcn_readfirstlane((unsigned)a);
  d[1] = __builtin_amdgcn_readfirstlane((unsigned)(a >> 32)) & 0xffffu;
  d[2] = bytes;
  d[3] = 0x00020000u;
  return d;
}
template <bool NT>
__device__ __forceinline__ void dma16(unsigned ldsaddr, int voff, v4u srd, int soff) {
  if (NT) asm volatile("s_mov_b32 m0, %0\n\ts_nop 0\n\tbuffer_load_dwordx4 %1, %2, %3 offen nt lds" :: "s"(ldsaddr), "v"(voff), "s"(srd), "s"(soff) : "memory");
  else asm volatile("s_mov_b32 m0, %0\n\ts_nop 0\n\tbuffer_load_dwordx4 %1, %2, %3 offen lds" :: "s"(ldsaddr), "v"(voff), "s"(srd), "s"(soff) : "memory");
}
__device__ __forceinline__ void lds_wait_ne0(LAS unsigned* f) {
  while (__hip_atomic_load(f, RLX_WG) == 0u) __builtin_amdgcn_s_sleep(1);
  CFENCE();
}
template <int TICKS>
__device__ __forceinline__ void pace(unsigned tstart) {
  if (TICKS > 0)
    asm volatile("s_memrealtime vcc\n\ts_waitcnt lgkmcnt(0)\n\ts_sub_i32 vcc_lo, vcc_lo, %0\n"
                 "1:\n\ts_cmpk_ge_i32 vcc_lo, %1\n\ts_cbranch_scc1 2f\n\ts_sleep %2\n\ts_addk_i32 vcc_lo, %3\n\ts_branch 1b\n2:"
                 :: "s"(tstart), "i"(TICKS), "i"(PSL), "i"(PSLT) : "vcc", "scc", "memory");
}

template <int NP>
__device__ __forceinline__ void ffn_eval(LAS unsigned char* lds, int lane, const f32x2 (&t)[NP], f32x2 (&acc)[NP]) {
  constexpr float C0 = -2.f * 0.7978845608028654f * LOG2E, C1 = C0 * 0.044715f;
#pragma unroll
  for (int p = 0; p < NP; ++p) acc[p] = f32x2{0.f, 0.f};
#pragma unroll 1
  for (int j = 0; j < 7; ++j) {
    const v2u ua = *(LAS v2u*)(lds + L_P + j * 512 + lane * 8);
    const v2u ub = *(LAS v2u*)(lds + L_P + 3584 + j * 512 + lane * 8);
    const v2u uw = *(LAS v2u*)(lds + L_P + 7168 + j * 512 + lane * 8);
#pragma unroll
    for (int i = 0; i < 4; ++i) {
      const float a = (i & 1) ? bfhi(ua[i >> 1]) : bflo(ua[i >> 1]);
      const float b = (i & 1) ? bfhi(ub[i >> 1]) : bflo(ub[i >> 1]);
      const float w = (i & 1) ? bfhi(uw[i >> 1]) : bflo(uw[i >> 1]);
#pragma unroll
      for (int p = 0; p < NP; ++p) {
        const f32x2 u = __builtin_elementwise_fma(t[p], f32x2{a, a}, f32x2{b, b});
        const f32x2 y = u * __builtin_elementwise_fma(u * u, f32x2{C1, C1}, f32x2{C0, C0});
        f32x2 d = f32x2{__builtin_amdgcn_exp2f(y[0]), __builtin_amdgcn_exp2f(y[1])} + 1.f;
        d = f32x2{__builtin_amdgcn_rcpf(d[0]), __builtin_amdgcn_rcpf(d[1])};
        acc[p] = __builtin_elementwise_fma(u * d, f32x2{w, w}, acc[p]);
      }
    }
  }
}

#define STAMP(i)

template <int I, int N, class F> __device__ __forceinline__ void static_for(F&& f) {
  if constexpr (I < N) { f(std::integral_constant<int, I>{}); static_for<I + 1, N>(f); }
}

__global__ __launch_bounds__(768, 3) void fused_kernel(const us* __restrict__ x, const us* __restrict__ kc, const us* __restrict__ vc,
                                                       const us* __restrict__ wq, const us* __restrict__ wo, const us* __restrict__ w1,
                                                       const us* __restrict__ b1, const us* __restrict__ w2, const us* __restrict__ b2,
                                                       us* __restrict__ out, unsigned char* ws) {
  __shared__ __attribute__((aligned(16))) unsigned char smem[L_TOTAL];
  LAS unsigned char* lds = (LAS unsigned char*)smem;
  const unsigned lds0 = (unsigned)(uintptr_t)lds;
  const int lane = threadIdx.x & 63, wave = __builtin_amdgcn_readfirstlane(threadIdx.x >> 6), g = lane >> 4, r = lane & 15;
  LAS unsigned* flags = (LAS unsigned*)(lds + L_FL);
  asm volatile(".Lpf_begin:");
  STAMP(0);
  const unsigned tstart = (unsigned)__builtin_amdgcn_s_memrealtime();
  asm volatile("" :: "s"(x), "s"(kc), "s"(vc), "s"(wq), "s"(ws));
  const int bid = blockIdx.x, sp = (bid >> 3) & 3, b = ((bid >> 3) & 4) | (bid >> 6), kvh = (bid + 3 * (bid >> 6)) & 7;
  const int voff = lane * 16;
  const int kvo = (((b * 8 + kvh) * 4 + sp) << 18) + ((wave - 4) << 10);
  const int koff = kvo + (r >> 2) * 256 + g * 64 + (r & 3) * 16;
#define KLOAD(i) do { kf[i] = __builtin_amdgcn_raw_buffer_load_b128(krs, koff, (i) * 8192, AUX_NT); SB(); } while (0)
  const __amdgpu_buffer_rsrc_t wsr = __builtin_amdgcn_make_buffer_rsrc((void*)ws, 0, WS_END, 0x00020000);
  const int jk = sp * 8 + b;
  const int jb = sp * 8 + kvh;
  if (wave < 4) {
    const v4u srd = make_srd(x, 8 * DM * 2);
    dma16<false>(lds0 + L_X + wave * 2080, voff, srd, wave * 2048);
    dma16<false>(lds0 + L_X + wave * 2080 + 1040, voff, srd, wave * 2048 + 1024);
    if (wave == 1 && lane < 2)
      __builtin_amdgcn_raw_buffer_store_b32(tstart, wsr, lane ? WS_NB + b * 128 + jb * 4 : WS_NK + kvh * 128 + jk * 4, 0, AUX_SC1);
    if (CODEPF && bid < CODEPF && wave == 3) {
      u64 lo; unsigned back, la, lb;
      asm volatile("s_getpc_b64 %0\n.Lpf_here:\n\ts_mov_b32 %1, .Lpf_here-.Lpf_begin\n\ts_mov_b32 %2, .Lpf_end_a-.Lpf_begin\n\ts_mov_b32 %3, .Lpf_end_b-.Lpf_begin"
                   : "=s"(lo), "=s"(back), "=s"(la), "=s"(lb));
      const v4u cs = make_srd((const void*)(lo - back), la > lb ? la : lb);
      int co = lane * 128;
#pragma unroll 1
      for (int i = 0; i < 3; ++i) {
        asm volatile("s_mov_b32 m0, %0\n\ts_nop 0\n\tbuffer_load_dword %1, %2, 0 offen lds" :: "s"(lds0 + L_T), "v"(co), "s"(cs) : "memory");
        co += 8192;
      }
    }
    if (PRETOUCH && wave == 2) {
      const v4u vs = make_srd(vc, 64 << 20), os = make_srd(wo, HQ * DH * DM * 2);
      if (lane == 0) {
        asm volatile("s_mov_b32 m0, %0\n\ts_nop 0\n\tbuffer_load_dword %1, %2, 0 offen nt lds" :: "s"(lds0 + L_T + 512), "v"(kvo + 4096), "s"(vs) : "memory");
        asm volatile("s_mov_b32 m0, %0\n\ts_nop 0\n\tbuffer_load_dword %1, %2, 0 offen lds" :: "s"(lds0 + L_T + 512), "v"(kvh << 19), "s"(os) : "memory");
      }
    }
    if (threadIdx.x < 16) flags[threadIdx.x] = 0u;
    SB();
  } else {
    const v4u ksd = make_srd(kc, 64 << 20);
    dma16<true>(lds0 + L_K0 + (wave - 4) * 2048, koff, ksd, 0);
    dma16<true>(lds0 + L_K0 + (wave - 4) * 2048 + 1024, koff, ksd, 8192);
    STAMP(1);
    const v4u srd = make_srd(wq, DM * HQ * DH * 2);
    const int vo = (sp * 128 + (wave - 4) * 16 + (lane >> 3)) * 8192 + kvh * 1024 + b * 128 + (lane & 7) * 16;
    dma16<true>(lds0 + L_B + (wave - 4) * 2048, vo, srd, 0);
    dma16<true>(lds0 + L_B + (wave - 4) * 2048 + 1024, vo, srd, 8 * 8192);
    SB();
  }
  WG_BARRIER();
  const int wgl = (b << 5) | jb;
  const int cj = (b & 3) * 4 + sp, bh = b >> 2;
  const int partg = WS_PART + (kvh * 2 + bh) * 16384;
  const int me = (b & 3) * 4 + sp;
  const int fjb = (b + 3 * (b & 3)) & 7;
  const bool fin = jb == fjb;

  if (wave < 4) {
    __builtin_amdgcn_s_setprio(3);
    if (wave != 1) {
      WAITV(0);
      if (lane == 0) __hip_atomic_fetch_add(flags + F_XW, 1u, RLX_WG);
    }
    if (wave == 0) {
      *(LAS v4u*)(lds + L_X + 8 * 1040 + lane * 16) = v4u{0x3f803f80u, 0x3f803f80u, 0x3f803f80u, 0x3f803f80u};
      while ((__hip_atomic_load(flags + F_XW, RLX_WG) & 255u) != 4u) __builtin_amdgcn_s_sleep(1);
      CFENCE();
      STAMP(1);
      const int xo = L_X + (r < 8 ? r : 8) * 1040 + g * 16;
      f32x4 d2a = {0.f, 0.f, 0.f, 0.f}, d2b = {0.f, 0.f, 0.f, 0.f};
#pragma unroll
      for (int s = 0; s < 16; s += 2) {
        const v4u xa = *(LAS v4u*)(lds + xo + s * 64), xb = *(LAS v4u*)(lds + xo + s * 64 + 64);
        d2a = mfma16(xa, xa, d2a); d2b = mfma16(xb, xb, d2b);
      }
      d2a += d2b;
      const float dg = (r & 2) ? ((r & 1) ? d2a[3] : d2a[2]) : ((r & 1) ? d2a[1] : d2a[0]);
      const float sxx = xrow_sum(g == (r >> 2) ? dg : 0.f);
      const float mean = xrow_sum(g == 2 ? d2a[0] : 0.f) * (1.f / DM);
      float rstd = rsqrtf(fmaxf(sxx * (1.f / DM) - mean * mean, 0.f) + 1e-5f);
      v4u xb[4];
#pragma unroll
      for (int s = 0; s < 4; ++s) { xb[s] = *(LAS v4u*)(lds + xo + sp * 256 + s * 64); asm volatile("" : "+v"(xb[s])); }
      asm volatile("" : "+v"(rstd));
      while (__hip_atomic_load(flags + F_XW, RLX_WG) != 4u + 8u * 256u) __builtin_amdgcn_s_sleep(1);
      CFENCE();
      STAMP(2);
      f32x4 d[4];
#pragma unroll
      for (int nt = 0; nt < 4; ++nt) d[nt] = f32x4{0.f, 0.f, 0.f, 0.f};
      const int troff = L_B + (8 * g + (r >> 2)) * 128 + (r & 3) * 8;
#pragma unroll
      for (int s = 0; s < 4; ++s)
#pragma unroll
        for (int nt = 0; nt < 4; ++nt) {
          const v2u u0 = __builtin_bit_cast(v2u, __builtin_amdgcn_ds_read_tr16_b64_v4i16((LAS v4s*)(lds + troff + s * 4096 + nt * 32)));
          const v2u u1 = __builtin_bit_cast(v2u, __builtin_amdgcn_ds_read_tr16_b64_v4i16((LAS v4s*)(lds + troff + s * 4096 + 512 + nt * 32)));
          d[nt] = mfma16(v4u{u0.x, u0.y, u1.x, u1.y}, xb[s], d[nt]);
        }
#pragma unroll
      for (int nt = 0; nt < 4; ++nt) {
        float qv[4];
#pragma unroll
        for (int i = 0; i < 4; ++i) qv[i] = (d[nt][i] - mean * dpp<0x158>(d[nt][i])) * rstd;
        if (r < 8) *(LAS v2u*)(lds + L_QO + r * 128 + nt * 32 + g * 8) = v2u{cvtpk(qv[0], qv[1]), cvtpk(qv[2], qv[3])};
      }
      LGKM0();
      if (lane == 0) __hip_atomic_store(flags + F_QO, 1u, RLX_WG);
      STAMP(3);
    } else if (wave == 1) {
      const int emo = WS_EM + wgl * 512 + lane * 8;
      const int qdo = WS_QD + (kvh * 8 + g) * 8192 + jk * 256 + r * 16;
      const int nno = lane < 32 ? WS_NK + kvh * 128 + lane * 4 : WS_NB + b * 128 + (lane - 32) * 4;
      const int eco = WS_EM + (lane < 32 ? ((((lane & 7) << 5) | (lane & 24) | kvh) * 64 + jk) : (((b << 5) | (lane - 32)) * 64 + 32 + jb)) * 8;
      const v4u e4 = {EMPTY, EMPTY, EMPTY, EMPTY};
      __builtin_amdgcn_raw_buffer_store_b128(e4, wsr, partg + me * 1024 + lane * 16, 0, AUX_SC1);
      if (lane < 42)
        __builtin_amdgcn_raw_buffer_store_b128(e4, wsr, (lane < 32 ? WS_OUTP + (b * 8 + kvh) * 2048 + sp * 512 : lane < 36 ? WS_STAT - 512 + wgl * 128 : WS_TAB - 576 + wgl * 96) + lane * 16, 0, AUX_SC1);
      SB();
      v2u em = __builtin_amdgcn_raw_buffer_load_b64(wsr, emo, 0, AUX_SC1);
      v4u s0 = __builtin_amdgcn_raw_buffer_load_b128(wsr, qdo, 0, AUX_SC1);
      v4u s1 = __builtin_amdgcn_raw_buffer_load_b128(wsr, qdo, 4 * 8192, AUX_SC1);
      SB();
      WAITV(6);
      if (lane == 0) __hip_atomic_fetch_add(flags + F_XW, 1u, RLX_WG);
      pace<NNT>(tstart);
      STAMP(1);
      unsigned nn = __builtin_amdgcn_raw_buffer_load_b32(wsr, nno, 0, AUX_SC1);
      asm volatile("s_waitcnt vmcnt(0)" : "+v"(em), "+v"(s0), "+v"(s1), "+v"(nn) :: "memory");
      unsigned nonce = tstart;
      while (__any((em[1] == nonce) | (s0[1] == nonce) | (s0[3] == nonce) | (s1[1] == nonce) | (s1[3] == nonce))) ++nonce;
      if (nonce != tstart) {
        if (lane < 2) __builtin_amdgcn_raw_buffer_store_b32(nonce, wsr, lane ? WS_NB + b * 128 + jb * 4 : WS_NK + kvh * 128 + jk * 4, 0, AUX_SC1);
        WAITV(0);
      }
      if (lane < 32) __builtin_amdgcn_raw_buffer_store_b64(v2u{nonce, nn}, wsr, eco, 0, AUX_SC1);
      if (lane == 0) __hip_atomic_store(flags + F_CLR, 1u, RLX_WG);
      STAMP(2);
      unsigned cand[8];
#pragma unroll
      for (int i = 0; i < 8; ++i) cand[i] = __builtin_amdgcn_ds_bpermute((4 * i + g) * 4, nn);
      lds_wait_ne0(flags + F_QO);
      {
        const v2u q0 = *(LAS v2u*)(lds + L_QO + lane * 8), q1 = *(LAS v2u*)(lds + L_QO + 512 + lane * 8);
        __builtin_amdgcn_raw_buffer_store_b128(v4u{q0.x, nonce, q0.y, nonce}, wsr, qdo, 0, AUX_SC1);
        __builtin_amdgcn_raw_buffer_store_b128(v4u{q1.x, nonce, q1.y, nonce}, wsr, qdo, 4 * 8192, AUX_SC1);
      }
      STAMP(3);
      const int qio = WS_QD + (kvh * 8 + b) * 8192 + lane * 16;
      v4u gq[8];
#pragma unroll
      for (int i = 0; i < 8; ++i) gq[i] = v4u{0u, ~cand[i], 0u, 0u};
      pace<QT1>(tstart);
      for (unsigned spin = 0; spin < SPIN_CAP; ++spin) {
        CFENCE();
        const unsigned n2 = __builtin_amdgcn_raw_buffer_load_b32(wsr, nno, 0, AUX_SC1);
        if (lane < 32) em = __builtin_amdgcn_raw_buffer_load_b64(wsr, emo, 0, AUX_SC1);
#pragma unroll
        for (int i = 0; i < 8; ++i)
          if ((gq[i][1] != cand[i]) | (gq[i][3] != cand[i])) gq[i] = __builtin_amdgcn_raw_buffer_load_b128(wsr, qio, i * 1024, AUX_SC1);
        if (__any(n2 != nn)) {
          if (n2 != nn && lane < 32) __builtin_amdgcn_raw_buffer_store_b64(v2u{nonce, n2}, wsr, eco, 0, AUX_SC1);
          nn = n2;
#pragma unroll
          for (int i = 0; i < 8; ++i) cand[i] = __builtin_amdgcn_ds_bpermute((4 * i + g) * 4, nn);
        }
        bool ok = (lane >= 32) | ((em[1] == nonce) & (em[0] == nn));
#pragma unroll
        for (int i = 0; i < 8; ++i) ok = ok & (gq[i][1] == cand[i]) & (gq[i][3] == cand[i]);
        if (__all(ok)) break;
        __builtin_amdgcn_s_sleep(QSL);
      }
#pragma unroll
      for (int hf = 0; hf < 2; ++hf) {
        float q0 = 0.f, q1 = 0.f, q2 = 0.f, q3 = 0.f;
#pragma unroll
        for (int kq = 0; kq < 4; ++kq) {
          const v4u t4 = gq[2 * kq + hf];
          q0 += bflo(t4[0]); q1 += bfhi(t4[0]); q2 += bflo(t4[2]); q3 += bfhi(t4[2]);
        }
        *(LAS v2u*)(lds + L_Q + hf * 512 + lane * 8) = v2u{cvtpk(q0, q1), cvtpk(q2, q3)};
      }
      {
        const int df = (int)(nn - tstart);
        const bool use = lane >= 32 && df > -256 && df < 256;
        const float sd = wave_sum(use ? (float)df : 0.f), cn = wave_sum(use ? 1.f : 0.f);
        if (lane == 0) flags[F_TB] = tstart + (unsigned)(int)(sd * __builtin_amdgcn_rcpf(fmaxf(cn, 1.f)));
      }
      LGKM0();
      if (lane == 0) __hip_atomic_store(flags + F_Q, 1u, RLX_WG);
      STAMP(4);
      const bool r2 = lane >= 32 && (fin || lane - 32 == fjb);
      if (r2) __builtin_amdgcn_raw_buffer_store_b64(v2u{nonce, nn}, wsr, eco, 0, AUX_SC1);
      for (unsigned spin = 0; spin < SPIN_CAP; ++spin) {
        __builtin_amdgcn_s_sleep(48);
        CFENCE();
        unsigned n2 = nn;
        if (r2) {
          n2 = __builtin_amdgcn_raw_buffer_load_b32(wsr, nno, 0, AUX_SC1);
          em = __builtin_amdgcn_raw_buffer_load_b64(wsr, emo, 0, AUX_SC1);
          if (n2 != nn) __builtin_amdgcn_raw_buffer_store_b64(v2u{nonce, n2}, wsr, eco, 0, AUX_SC1);
        }
        nn = n2;
        if (__all(!r2 | ((em[1] == nonce) & (em[0] == nn)))) break;
      }
      STAMP(7);
      if (fin) {
        pace<TABT>(tstart);
#pragma unroll 1
        for (int c = 0; c < 3; ++c) {
          for (unsigned spin = 0; spin < SPIN_CAP; ++spin) {
            CFENCE();
            v4u tv[8];
#pragma unroll
            for (int i = 0; i < 8; ++i) tv[i] = __builtin_amdgcn_raw_buffer_load_b128(wsr, WS_TAB + c * 8192 + lane * 16, i * 1024, AUX_SC1);
            bool ok = true;
#pragma unroll
            for (int i = 0; i < 8; ++i) {
              ok = ok & (tv[i][1] == 1u) & (tv[i][3] == 1u);
              *(LAS v2u*)(lds + L_T + c * 4096 + i * 512 + lane * 8) = v2u{tv[i][0], tv[i][2]};
            }
            if (__all(ok)) break;
            __builtin_amdgcn_s_sleep(16);
          }
        }
        LGKM0();
        if (lane == 0) __hip_atomic_store(flags + F_FIN, 1u, RLX_WG);
        asm volatile(".Lpf_end_a:");
        STAMP(8);
      }
    } else if (wave == 2) {
      pace<PART>(tstart);
      const v4u s1 = make_srd(w1, FF * 2), s2 = make_srd(b1, FF * 2), s3 = make_srd(w2, FF * 2);
#pragma unroll
      for (int i = 0; i < 3; ++i) {
        dma16<false>(lds0 + L_P + i * 1024, voff, s1, i * 1024);
        dma16<false>(lds0 + L_P + 3584 + i * 1024, voff, s2, i * 1024);
        dma16<false>(lds0 + L_P + 7168 + i * 1024, voff, s3, i * 1024);
      }
      if (lane < 32) {
        dma16<false>(lds0 + L_P + 3072, voff, s1, 3072);
        dma16<false>(lds0 + L_P + 3584 + 3072, voff, s2, 3072);
        dma16<false>(lds0 + L_P + 7168 + 3072, voff, s3, 3072);
      }
      const unsigned b2u = b2[0];
      if (lane == 0) flags[F_B2] = b2u;
      asm volatile("s_waitcnt vmcnt(0) lgkmcnt(0)" ::: "memory");
      if (lane == 0) __hip_atomic_store(flags + F_P, 1u, RLX_WG);
      STAMP(1);
    }
    if (wave != 1) {
      __builtin_amdgcn_s_setprio(0);
      lds_wait_ne0(flags + F_P);
      lds_wait_ne0(flags + F_CLR);
      const int p0 = wave == 0 ? 0 : 2 * wave - 2;
#pragma unroll 1
      for (int ps = p0; ps < p0 + 2; ++ps) {
        const unsigned bits = (unsigned)((wgl & 127) * 24 + ps * 4 + (TAB_E0 << 7)) | (unsigned)((wgl >> 7) << 15);
        f32x2 t[2], a[2];
#pragma unroll
        for (int p = 0; p < 2; ++p) t[p] = f32x2{bflo(bits + 2 * p), bflo(bits + 2 * p + 1)};
        ffn_eval<2>(lds, lane, t, a);
        const unsigned k0 = cvtpk(wave_sum(a[0][0]), wave_sum(a[0][1])), k1 = cvtpk(wave_sum(a[1][0]), wave_sum(a[1][1]));
        __builtin_amdgcn_s_setprio(3);
        if (lane == 0) __builtin_amdgcn_raw_buffer_store_b128(v4u{k0, 1u, k1, 1u}, wsr, WS_TAB + wgl * 96 + ps * 16, 0, AUX_SC1);
        __builtin_amdgcn_s_setprio(0);
      }
      __builtin_amdgcn_s_setprio(3);
      STAMP(6);
    }
    if (wave == 0) {
      while (__hip_atomic_load(flags + F_CNT, RLX_WG) != 8u) __builtin_amdgcn_s_sleep(2);
      CFENCE();
      STAMP(7);
      const int hd = lane & 3;
      float M = -INFINITY, L = 0.f;
#pragma unroll
      for (int w = 0; w < 8; ++w) M = fmaxf(M, ((LAS float*)(lds + L_ML))[w * 8 + hd]);
#pragma unroll
      for (int w = 0; w < 8; ++w) L = fmaf(__builtin_amdgcn_exp2f(((LAS float*)(lds + L_ML))[w * 8 + hd] - M), ((LAS float*)(lds + L_ML))[w * 8 + 4 + hd], L);
      if (lane < 4) __builtin_amdgcn_raw_buffer_store_b128(v4u{__float_as_uint(M), 1u, __float_as_uint(L), 1u}, wsr, WS_STAT + wgl * 128 + lane * 16, 0, AUX_SC1);
      v4u sv = {0u, 1u, 0u, 1u};
      for (unsigned spin = 0; spin < SPIN_CAP; ++spin) {
        __builtin_amdgcn_s_sleep(8);
        CFENCE();
        if (lane < 16) sv = __builtin_amdgcn_raw_buffer_load_b128(wsr, WS_STAT + ((b << 5) | ((lane >> 2) << 3) | kvh) * 128 + hd * 16, 0, AUX_SC1);
        if (__all((sv[1] == 1u) & (sv[3] == 1u))) break;
      }
      const float ms = __uint_as_float(sv[0]);
      float Mg = fmaxf(ms, dpp<0x124>(ms)); Mg = fmaxf(Mg, dpp<0x128>(Mg));
      float Lg = __builtin_amdgcn_exp2f(ms - Mg) * __uint_as_float(sv[2]);
      Lg += dpp<0x128>(Lg); Lg += dpp<0x124>(Lg);
      if (lane < 4) ((LAS float*)(lds + L_Z))[lane] = Mg + __builtin_amdgcn_logf(Lg);
      LGKM0();
      if (lane == 0) __hip_atomic_store(flags + F_Z, 1u, RLX_WG);
      STAMP(8);
    }
  } else {
    const int sw = wave - 4, tid = threadIdx.x - 256;
    const __amdgpu_buffer_rsrc_t vrs = __builtin_amdgcn_make_buffer_rsrc((void*)vc, 0, 64 << 20, 0x00020000);
    const int vvo = kvo + voff;
    const v4u wsd = make_srd(wo, HQ * DH * DM * 2);
    const int wo_ = (kvh * 512 + sw + ((lane >> 2) & 3) * 128 + (lane >> 4) * 8) * 1024 + cj * 64 + (lane & 3) * 16;
#define WPIECE(k) do { dma16<false>(lds0 + L_W + sw * 4096 + (k) * 1024, wo_, wsd, (k) * 32 * 1024); SB(); } while (0)
#define VLOAD(i) do { WAITV(VW - 1); pace<sched(32 + (i))>(tb); vf[i] = __builtin_amdgcn_raw_buffer_load_b128(vrs, vvo, (i) * 8192, AUX_NT); SB(); \
    if (32 + (i) == WP0) WPIECE(0); if (32 + (i) == WP1) WPIECE(1); if (32 + (i) == WP2) WPIECE(2); if (32 + (i) == WP3) WPIECE(3); } while (0)
    const __amdgpu_buffer_rsrc_t krs = __builtin_amdgcn_make_buffer_rsrc((void*)kc, 0, 64 << 20, 0x00020000);
    v4u kf[32], vf[32];
    __builtin_amdgcn_s_setprio(2);
    unsigned tb = tstart + (STG * sw >> 4);
    static_for<KFIRST, 32>([&](auto ic) {
      constexpr int i = ic.value;
      WAITV((i < KSW ? KW1 : KW) - 1);
      pace<sched(i)>(tb);
      if (i == XWSIG) { WAITV(XWSIG - KFIRST); if (lane == 0) __hip_atomic_fetch_add(flags + F_XW, 256u, RLX_WG); }
      KLOAD(i);
      if (i == WP0) WPIECE(0);
      if (i == WP1) WPIECE(1);
    });
    STAMP(3);
    static_for<0, XV>([&](auto ic) { VLOAD(ic.value); });
    STAMP(6);
    lds_wait_ne0(flags + F_Q);
    if (TBASE) tb = __builtin_amdgcn_readfirstlane(flags[F_TB]) + TBO + (STG * sw >> 4);
    SB();
    const v4u qf = *(LAS v4u*)(lds + L_Q + (r & 3) * 256 + g * 64 + (r >> 2) * 16);
    kf[0] = *(LAS v4u*)(lds + L_K0 + sw * 2048 + voff);
    kf[1] = *(LAS v4u*)(lds + L_K0 + sw * 2048 + 1024 + voff);
    float s[32];
    static_for<0, 32>([&](auto ic) {
      constexpr int i = ic.value;
      const f32x4 a = mfma16(kf[i], qf, f32x4{0.f, 0.f, 0.f, 0.f});
      float t_ = a[0] + dpp<0x104>(a[1]); asm volatile("" : "+v"(t_));
      t_ += dpp<0x108>(a[2]); asm volatile("" : "+v"(t_));
      s[i] = t_ + dpp<0x10C>(a[3]); asm volatile("" : "+v"(s[i]));
      if constexpr ((i & 3) == 3) { SB(); VLOAD(XV + (i >> 2)); }
    });
    SB();
    float m = s[0];
#pragma unroll
    for (int i = 1; i < 32; ++i) m = fmaxf(m, s[i]);
    m = xrow_max(m) * SCALE_L2;
    asm volatile("" : "+v"(m));
    SB(); VLOAD(XV + 8);
    v4u pf[4];
#define SOFTMAX(G, mm) do { float p_[8]; \
    _Pragma("unroll") for (int j = 0; j < 8; ++j) p_[j] = __builtin_amdgcn_exp2f(fmaf(s[8 * (G) + j], SCALE_L2, -(mm))); \
    pf[G] = v4u{cvtpk(p_[0], p_[1]), cvtpk(p_[2], p_[3]), cvtpk(p_[4], p_[5]), cvtpk(p_[6], p_[7])}; } while (0)
    {
      SOFTMAX(0, m); SOFTMAX(1, m);
      asm volatile("" : "+v"(pf[0]), "+v"(pf[1]));
      SB(); VLOAD(XV + 9);
      SOFTMAX(2, m); SOFTMAX(3, m);
      asm volatile("" : "+v"(pf[2]), "+v"(pf[3]));
      SB(); VLOAD(XV + 10);
      const v4u ones = {0x3f803f80u, 0x3f803f80u, 0x3f803f80u, 0x3f803f80u};
      f32x4 l4 = {0.f, 0.f, 0.f, 0.f};
#pragma unroll
      for (int G = 0; G < 4; ++G) l4 = mfma16(pf[G], ones, l4);
      if (lane < 4) ((LAS float*)(lds + L_ML))[sw * 8 + lane] = m;
      if (lane == 0) *(LAS f32x4*)(lds + L_ML + sw * 32 + 16) = l4;
      LGKM0();
      if (lane == 0) __hip_atomic_fetch_add(flags + F_CNT, 1u, RLX_WG);
    }
    STAMP(7);
    f32x4 acc[8];
#pragma unroll
    for (int j = 0; j < 8; ++j) acc[j] = f32x4{0.f, 0.f, 0.f, 0.f};
#define PV(G) do { \
    _Pragma("unroll") for (int d = 0; d < 4; ++d) { v4u lo_, hi_; \
      _Pragma("unroll") for (int jj = 0; jj < 4; ++jj) { \
        lo_[jj] = __builtin_amdgcn_perm(vf[8 * (G) + 2 * jj + 1][d], vf[8 * (G) + 2 * jj][d], 0x05040100u); \
        hi_[jj] = __builtin_amdgcn_perm(vf[8 * (G) + 2 * jj + 1][d], vf[8 * (G) + 2 * jj][d], 0x07060302u); } \
      acc[2 * d] = mfma16(pf[G], lo_, acc[2 * d]); acc[2 * d + 1] = mfma16(pf[G], hi_, acc[2 * d + 1]); } SB(); } while (0)
    static_for<XV + 11, 32>([&](auto ic) {
      constexpr int i = ic.value;
      VLOAD(i);
      if constexpr (i == XV + 11) PV(0);
      if constexpr (i == 15 + PVD) PV(1);
      if constexpr (i == 23 + PVD) PV(2);
    });
    STAMP(8);
    v4u wf[2][2];
    {
      const int wtr = L_W + sw * 4096 + (g * 8 + (r >> 2)) * 64 + (r & 3) * 8;
#pragma unroll
      for (int ks = 0; ks < 2; ++ks)
#pragma unroll
        for (int nt = 0; nt < 2; ++nt) {
          const v2u u0 = __builtin_bit_cast(v2u, __builtin_amdgcn_ds_read_tr16_b64_v4i16((LAS v4s*)(lds + wtr + ks * 2048 + nt * 32)));
          const v2u u1 = __builtin_bit_cast(v2u, __builtin_amdgcn_ds_read_tr16_b64_v4i16((LAS v4s*)(lds + wtr + ks * 2048 + 256 + nt * 32)));
          wf[ks][nt] = v4u{u0.x, u0.y, u1.x, u1.y};
          asm volatile("" : "+v"(wf[ks][nt]));
        }
      SB();
    }
    lds_wait_ne0(flags + F_Z);
    SB();
    {
      const f32x4 Z4 = *(LAS f32x4*)(lds + L_Z);
      const f32x4 mo = *(LAS f32x4*)(lds + L_ML + sw * 32);
      const float zl = ((LAS float*)(lds + L_Z))[lane & 3];
      f32x4 f4;
#pragma unroll
      for (int i = 0; i < 4; ++i) f4[i] = __builtin_amdgcn_exp2f(mo[i] - Z4[i]);
#pragma unroll
      for (int j = 0; j < 8; ++j) acc[j] *= f4;
      const float zz = (r < 4) ? zl : m;
      SOFTMAX(3, zz);
      SB();
    }
    {
      v4u lo_[4], hi_[4];
#pragma unroll
      for (int jj = 0; jj < 4; ++jj) {
#pragma unroll
        for (int d = 0; d < 4; ++d) {
          lo_[d][jj] = __builtin_amdgcn_perm(vf[24 + 2 * jj + 1][d], vf[24 + 2 * jj][d], 0x05040100u);
          hi_[d][jj] = __builtin_amdgcn_perm(vf[24 + 2 * jj + 1][d], vf[24 + 2 * jj][d], 0x07060302u);
        }
        SB();
      }
#pragma unroll
      for (int d = 0; d < 4; ++d) { acc[2 * d] = mfma16(pf[3], lo_[d], acc[2 * d]); acc[2 * d + 1] = mfma16(pf[3], hi_[d], acc[2 * d + 1]); }
      SB();
    }
    STAMP(9);
    if (lane < 16) {
#pragma unroll
      for (int j = 0; j < 8; ++j) *(LAS f32x4*)(lds + L_B + ((sw * 8 + j) * 16 + lane) * 16) = acc[j];
    }
    WG_BARRIER();
    STAMP(10);
    LAS unsigned char* aim = lds + L_A + sw * 2304;
    if (lane < 16) {
      f32x4 o = *(LAS f32x4*)(lds + L_B + (sw * 16 + lane) * 16);
#pragma unroll
      for (int w = 1; w < 8; ++w) o += *(LAS f32x4*)(lds + L_B + ((w * 8 + sw) * 16 + lane) * 16);
      const v2u op = {cvtpk(o[0], o[1]), min(cvtpk(o[2], o[3]), EMPTY - 1u)};
      __builtin_amdgcn_raw_buffer_store_b64(op, wsr, partg + me * 1024 + sw * 128 + lane * 8, 0, AUX_SC1);
      *(LAS v2u*)(aim + me * 144 + lane * 8) = op;
    }
    SB();
    {
      const int po = partg + (lane >> 3) * 1024 + sw * 128 + (lane & 7) * 16;
      bool ok0 = (lane >> 3) == me, ok1 = (lane >> 3) + 8 == me;
      __builtin_amdgcn_s_sleep(H2D);
      for (unsigned spin = 0; spin < SPIN_CAP; ++spin) {
        CFENCE();
        v4u p0 = {EMPTY, EMPTY, EMPTY, EMPTY}, p1 = {EMPTY, EMPTY, EMPTY, EMPTY};
        if (!ok0) p0 = __builtin_amdgcn_raw_buffer_load_b128(wsr, po, 0, AUX_SC1);
        if (!ok1) p1 = __builtin_amdgcn_raw_buffer_load_b128(wsr, po, 8192, AUX_SC1);
        if ((p0[1] != EMPTY) & (p0[3] != EMPTY)) { ok0 = true; *(LAS v4u*)(aim + (lane >> 3) * 144 + (lane & 7) * 16) = p0; }
        if ((p1[1] != EMPTY) & (p1[3] != EMPTY)) { ok1 = true; *(LAS v4u*)(aim + (lane >> 3) * 144 + 1152 + (lane & 7) * 16) = p1; }
        if (__all(ok0 & ok1)) break;
      }
      STAMP(11);
      f32x4 c0 = {0.f, 0.f, 0.f, 0.f}, c1 = {0.f, 0.f, 0.f, 0.f};
#pragma unroll
      for (int ks = 0; ks < 2; ++ks) {
        const v4u af = *(LAS v4u*)(aim + r * 144 + ks * 64 + g * 16);
        c0 = mfma16(af, wf[ks][0], c0);
        c1 = mfma16(af, wf[ks][1], c1);
      }
      LAS float* red = (LAS float*)(lds + L_W);
      red[sw * 128 + lane] = (c0[0] + c0[1]) + (c0[2] + c0[3]);
      red[sw * 128 + 64 + lane] = (c1[0] + c1[1]) + (c1[2] + c1[3]);
    }
    WG_BARRIER();
    if (sw < 2) {
      LAS float* red = (LAS float*)(lds + L_W) + (g & 1) * 64 + (2 * sw + (g >> 1)) * 16 + r;
      float a = red[0];
#pragma unroll
      for (int w = 1; w < 8; ++w) a += red[w * 128];
      __builtin_amdgcn_raw_buffer_store_b32(min(__float_as_uint(a), EMPTY - 1u), wsr, WS_OUTP + (((bh * 4 + 2 * sw + (g >> 1)) * 8 + kvh) * 512 + cj * 32 + (lane & 31)) * 4, 0, AUX_SC1);
    }
    STAMP(12);
    if (fin) {
      lds_wait_ne0(flags + F_FIN);
      const float xres = bflo(((LAS us*)(lds + L_X))[b * 520 + tid]), b2v = bflo(flags[F_B2]);
      unsigned pv[8];
#pragma unroll
      for (int k = 0; k < 8; ++k) pv[k] = EMPTY;
      for (unsigned spin = 0; spin < SPIN_CAP; ++spin) {
        CFENCE();
#pragma unroll
        for (int k = 0; k < 8; ++k)
          if (pv[k] == EMPTY) pv[k] = __builtin_amdgcn_raw_buffer_load_b32(wsr, WS_OUTP + b * 16384 + tid * 4, k * 2048, AUX_SC1);
        bool ok = true;
#pragma unroll
        for (int k = 0; k < 8; ++k) ok = ok & (pv[k] != EMPTY);
        if (__all(ok)) break;
      }
      STAMP(13);
      const float a = ((__uint_as_float(pv[0]) + __uint_as_float(pv[1])) + (__uint_as_float(pv[2]) + __uint_as_float(pv[3]))) +
                      ((__uint_as_float(pv[4]) + __uint_as_float(pv[5])) + (__uint_as_float(pv[6]) + __uint_as_float(pv[7])));
      const float h = rbf(xres + rbf(a));
      const float s1 = wave_sum(h), s2 = wave_sum(h * h);
      if (lane == 0) *(LAS f32x2*)(lds + L_B + sw * 8) = f32x2{s1, s2};
      WG_BARRIER();
      const f32x4 q0 = *(LAS f32x4*)(lds + L_B), q1 = *(LAS f32x4*)(lds + L_B + 16), q2 = *(LAS f32x4*)(lds + L_B + 32), q3 = *(LAS f32x4*)(lds + L_B + 48);
      const f32x4 qs = (q0 + q1) + (q2 + q3);
      const float mean = (qs[0] + qs[2]) * (1.f / DM);
      const float var = fmaxf((qs[1] + qs[3]) * (1.f / DM) - mean * mean, 0.f);
      const float r1 = __builtin_amdgcn_rsqf(var + 1e-5f);
      const float r2 = __builtin_amdgcn_rsqf(fmaf(var * r1, r1, 1e-5f));
      const float t = ((h - mean) * r1) * r2;
      const unsigned tb = cvtpk(t, t) & 0xffffu;
      const unsigned rel = (tb & 0x7fffu) - (unsigned)(TAB_E0 << 7);
      const bool inr = rel < (unsigned)TAB_H;
      float y = bflo(((LAS us*)(lds + L_T))[(inr ? rel : 0u) + (tb >> 15) * TAB_H]);
      u64 miss = __ballot(!inr);
      while (miss) {
        const int l = __builtin_ctzll(miss);
        miss &= miss - 1;
        const float ts = bflo((unsigned)__builtin_amdgcn_readlane((int)tb, l));
        const f32x2 tt[1] = {f32x2{ts, ts}};
        f32x2 aa[1];
        ffn_eval<1>(lds, lane, tt, aa);
        const float ys = rbf(wave_sum(aa[0][0]));
        if (lane == l) y = ys;
      }
      const float ov = h + rbf(y + b2v);
      out[b * DM + tid] = (us)(cvtpk(ov, ov) & 0xffffu);
      STAMP(14);
    }
  }
  asm volatile(".Lpf_end_b:");
}

extern "C" void kernel_launch(void* const* d_in, const int* in_sizes, int n_in, void* d_out, int out_size, void* d_ws, size_t ws_size, hipStream_t stream) {
  fused_kernel<<<256, 768, 0, stream>>>((const us*)d_in[0], (const us*)d_in[1], (const us*)d_in[2], (const us*)d_in[3], (const us*)d_in[4],
                                        (const us*)d_in[6], (const us*)d_in[7], (const us*)d_in[8], (const us*)d_in[9], (us*)d_out, (unsigned char*)d_ws);
}
```

```cpp
#include <hip/hip_runtime.h>
#include <hip/hip_bf16.h>
#include <stdint.h>
#include <type_traits>

#define GAS __attribute__((address_space(1)))
#define LAS __attribute__((address_space(3)))
#define RLX_AGENT __ATOMIC_RELAXED, __HIP_MEMORY_SCOPE_AGENT
#define RLX_WG __ATOMIC_RELAXED, __HIP_MEMORY_SCOPE_WORKGROUP
typedef unsigned v4u __attribute__((ext_vector_type(4)));
typedef unsigned v2u __attribute__((ext_vector_type(2)));
typedef float f32x4 __attribute__((ext_vector_type(4)));
typedef float f32x2 __attribute__((ext_vector_type(2)));
typedef short bf16x8 __attribute__((ext_vector_type(8)));
typedef short v4s __attribute__((ext_vector_type(4)));
typedef __bf16 bf16x2_t __attribute__((ext_vector_type(2)));
typedef unsigned long long u64;
typedef GAS unsigned gu32;
typedef GAS u64 gu64;
typedef unsigned short us;

constexpr int DM = 512, HQ = 32, DH = 128, FF = 1792;
constexpr float LOG2E = 1.4426950408889634f;
constexpr float SCALE_L2 = 0.08838834764831845f * LOG2E;
constexpr unsigned SPIN_CAP = 200000u;
constexpr int AUX_NT = 2, AUX_SC1 = 16;
constexpr unsigned EMPTY = 0xffffffffu;
constexpr int TAB_E0 = 107;
constexpr int TAB_H = 24 * 128;

#define NSLEEP 4
#define H2D 11
#define CODEPF 16
#define KFIRST 2
#define PER16 476
#define LEAD 2
#define NE 24
#define EX16 120
#define WC16 260
#define XWSIG 5
#define PVD 5
#define XV 2
#define PSL 2
#define PSLT 7
#define NNT 130
#define KW 4
#define VW 6
#define PART 250
#define PRETOUCH 0
#define KW1 4
#define KSW 16
#define MAPB 1
#define QT1 480
#define QSL 8
#define NS 34
#define SP16 0
#define TBASE 1
#define TBO -7
#define STG 30
#define WP0 15
#define WP1 23
#define WP2 35
#define WP3 43
#define PFSPLIT 0
#define WNT true
#define XNT false
#define PNT false
#define QD1 16
#define TABT 1200
constexpr int sched(int n) {
  return ((n - LEAD) * PER16 + (n < NE ? n : NE) * EX16 - (n > NS ? n - NS : 0) * SP16 + ((n > WP0) + (n > WP1) + (n > WP2) + (n > WP3)) * WC16) >> 4;
}

constexpr int WS_NK = 0;
constexpr int WS_NB = 1024;
constexpr int WS_STAT = 4096;
constexpr int WS_TAB = WS_STAT + 256 * 128;
constexpr int WS_EM = WS_TAB + 256 * 128;
constexpr int WS_PART = WS_EM + 256 * 512;
constexpr int WS_OUTP = WS_PART + 256 * 1024;
constexpr int WS_QD = WS_OUTP + 64 * 2048;
constexpr int WS_END = WS_QD + 64 * 8192;
constexpr size_t WS_STAMP = 16u << 20;

constexpr int L_W = 0;
constexpr int L_B = 32768;
constexpr int L_X = L_B + 16384;
constexpr int L_P = L_X + 9472;
constexpr int L_T = L_P + 10752;
constexpr int L_Q = L_T + 16384;
constexpr int L_QO = L_Q + 1024;
constexpr int L_ML = L_QO + 1024;
constexpr int L_Z = L_ML + 256;
constexpr int L_FL = L_Z + 64;
constexpr int L_A = L_FL + 64;
constexpr int L_K0 = L_A + 8 * 2304;
constexpr int L_TOTAL = L_K0 + 8 * 2048;
enum { F_Q = 0, F_CNT = 1, F_QO = 2, F_Z = 3, F_P = 4, F_FIN = 5, F_XW = 6, F_B2 = 7, F_CLR = 8, F_TB = 9 };

__device__ __forceinline__ f32x4 mfma16(v4u a, v4u b, f32x4 c) {
  return __builtin_amdgcn_mfma_f32_16x16x32_bf16(__builtin_bit_cast(bf16x8, a), __builtin_bit_cast(bf16x8, b), c, 0, 0, 0);
}
__device__ __forceinline__ unsigned cvtpk(float lo, float hi) {
  return __builtin_bit_cast(unsigned, __builtin_convertvector(f32x2{lo, hi}, bf16x2_t));
}
__device__ __forceinline__ float bflo(unsigned u) { return __builtin_bit_cast(float, u << 16); }
__device__ __forceinline__ float bfhi(unsigned u) { return __builtin_bit_cast(float, u & 0xffff0000u); }
__device__ __forceinline__ float rbf(float x) { return bflo(cvtpk(x, x)); }
__device__ __forceinline__ float xrow_max(float x) {
  auto s = __builtin_amdgcn_permlane16_swap(__float_as_uint(x), __float_as_uint(x), false, false);
  x = fmaxf(__uint_as_float(s[0]), __uint_as_float(s[1]));
  auto t = __builtin_amdgcn_permlane32_swap(__float_as_uint(x), __float_as_uint(x), false, false);
  return fmaxf(__uint_as_float(t[0]), __uint_as_float(t[1]));
}
__device__ __forceinline__ float xrow_sum(float x) {
  auto s = __builtin_amdgcn_permlane16_swap(__float_as_uint(x), __float_as_uint(x), false, false);
  x = __uint_as_float(s[0]) + __uint_as_float(s[1]);
  auto t = __builtin_amdgcn_permlane32_swap(__float_as_uint(x), __float_as_uint(x), false, false);
  return __uint_as_float(t[0]) + __uint_as_float(t[1]);
}
template <int CTRL> __device__ __forceinline__ float dpp(float x) {
  return __builtin_bit_cast(float, __builtin_amdgcn_mov_dpp(__builtin_bit_cast(int, x), CTRL, 0xf, 0xf, true));
}
__device__ __forceinline__ float wave_sum(float x) {
  x += dpp<0xB1>(x); x += dpp<0x4E>(x); x += dpp<0x141>(x); x += dpp<0x128>(x);
  return xrow_sum(x);
}
#define SB() __builtin_amdgcn_sched_barrier(0)
#define CFENCE() asm volatile("" ::: "memory")
#define LGKM0() asm volatile("s_waitcnt lgkmcnt(0)" ::: "memory")
#define WAITV(n) asm volatile("s_waitcnt vmcnt(%0)" :: "i"(n) : "memory")
#define WG_BARRIER() do { asm volatile("s_waitcnt lgkmcnt(0)\n\ts_barrier" ::: "memory"); SB(); } while (0)
__device__ __forceinline__ v4u make_srd(const void* p, unsigned bytes) {
  const u64 a = (u64)p;
  v4u d;
  d[0] = __builtin_amdgcn_readfirstlane((unsigned)a);
  d[1] = __builtin_amdgcn_readfirstlane((unsigned)(a >> 32)) & 0xffffu;
  d[2] = bytes;
  d[3] = 0x00020000u;
  return d;
}
template <bool NT>
__device__ __forceinline__ void dma16(unsigned ldsaddr, int voff, v4u srd, int soff) {
  if (NT) asm volatile("s_mov_b32 m0, %0\n\ts_nop 0\n\tbuffer_load_dwordx4 %1, %2, %3 offen nt lds" :: "s"(ldsaddr), "v"(voff), "s"(srd), "s"(soff) : "memory");
  else asm volatile("s_mov_b32 m0, %0\n\ts_nop 0\n\tbuffer_load_dwordx4 %1, %2, %3 offen lds" :: "s"(ldsaddr), "v"(voff), "s"(srd), "s"(soff) : "memory");
}
__device__ __forceinline__ void lds_wait_ne0(LAS unsigned* f) {
  while (__hip_atomic_load(f, RLX_WG) == 0u) __builtin_amdgcn_s_sleep(1);
  CFENCE();
}
template <int TICKS>
__device__ __forceinline__ void pace(unsigned tstart) {
  if (TICKS > 0)
    asm volatile("s_memrealtime vcc\n\ts_waitcnt lgkmcnt(0)\n\ts_sub_i32 vcc_lo, vcc_lo, %0\n"
                 "1:\n\ts_cmpk_ge_i32 vcc_lo, %1\n\ts_cbranch_scc1 2f\n\ts_sleep %2\n\ts_addk_i32 vcc_lo, %3\n\ts_branch 1b\n2:"
                 :: "s"(tstart), "i"(TICKS), "i"(PSL), "i"(PSLT) : "vcc", "scc", "memory");
}

template <int NP>
__device__ __forceinline__ void ffn_eval(LAS unsigned char* lds, int lane, const f32x2 (&t)[NP], f32x2 (&acc)[NP]) {
  constexpr float C0 = -2.f * 0.7978845608028654f * LOG2E, C1 = C0 * 0.044715f;
#pragma unroll
  for (int p = 0; p < NP; ++p) acc[p] = f32x2{0.f, 0.f};
#pragma unroll 1
  for (int j = 0; j < 7; ++j) {
    const v2u ua = *(LAS v2u*)(lds + L_P + j * 512 + lane * 8);
    const v2u ub = *(LAS v2u*)(lds + L_P + 3584 + j * 512 + lane * 8);
    const v2u uw = *(LAS v2u*)(lds + L_P + 7168 + j * 512 + lane * 8);
#pragma unroll
    for (int i = 0; i < 4; ++i) {
      const float a = (i & 1) ? bfhi(ua[i >> 1]) : bflo(ua[i >> 1]);
      const float b = (i & 1) ? bfhi(ub[i >> 1]) : bflo(ub[i >> 1]);
      const float w = (i & 1) ? bfhi(uw[i >> 1]) : bflo(uw[i >> 1]);
#pragma unroll
      for (int p = 0; p < NP; ++p) {
        const f32x2 u = __builtin_elementwise_fma(t[p], f32x2{a, a}, f32x2{b, b});
        const f32x2 y = u * __builtin_elementwise_fma(u * u, f32x2{C1, C1}, f32x2{C0, C0});
        f32x2 d = f32x2{__builtin_amdgcn_exp2f(y[0]), __builtin_amdgcn_exp2f(y[1])} + 1.f;
        d = f32x2{__builtin_amdgcn_rcpf(d[0]), __builtin_amdgcn_rcpf(d[1])};
        acc[p] = __builtin_elementwise_fma(u * d, f32x2{w, w}, acc[p]);
      }
    }
  }
}

#define STAMP(i)

template <int I, int N, class F> __device__ __forceinline__ void static_for(F&& f) {
  if constexpr (I < N) { f(std::integral_constant<int, I>{}); static_for<I + 1, N>(f); }
}

__global__ __launch_bounds__(768, 3) void fused_kernel(const us* __restrict__ x, const us* __restrict__ kc, const us* __restrict__ vc,
                                                       const us* __restrict__ wq, const us* __restrict__ wo, const us* __restrict__ w1,
                                                       const us* __restrict__ b1, const us* __restrict__ w2, const us* __restrict__ b2,
                                                       us* __restrict__ out, unsigned char* ws) {
  __shared__ __attribute__((aligned(16))) unsigned char smem[L_TOTAL];
  LAS unsigned char* lds = (LAS unsigned char*)smem;
  const unsigned lds0 = (unsigned)(uintptr_t)lds;
  const int lane = threadIdx.x & 63, wave = __builtin_amdgcn_readfirstlane(threadIdx.x >> 6), g = lane >> 4, r = lane & 15;
  LAS unsigned* flags = (LAS unsigned*)(lds + L_FL);
  asm volatile(".Lpf_begin:");
  STAMP(0);
  const unsigned tstart = (unsigned)__builtin_amdgcn_s_memrealtime();
  asm volatile("" :: "s"(x), "s"(kc), "s"(vc), "s"(wq), "s"(ws));
  const int bid = blockIdx.x, sp = (bid >> 3) & 3, b = ((bid >> 3) & 4) | (bid >> 6), kvh = (bid + 3 * (bid >> 6)) & 7;
  const int voff = lane * 16;
  const int kvo = (((b * 8 + kvh) * 4 + sp) << 18) + ((wave - 4) << 10);
  const int koff = kvo + (r >> 2) * 256 + g * 64 + (r & 3) * 16;
#define KLOAD(i) do { kf[i] = __builtin_amdgcn_raw_buffer_load_b128(krs, koff, (i) * 8192, AUX_NT); SB(); } while (0)
  const __amdgpu_buffer_rsrc_t wsr = __builtin_amdgcn_make_buffer_rsrc((void*)ws, 0, WS_END, 0x00020000);
  const int jk = sp * 8 + b;
  const int jb = sp * 8 + kvh;
  if (wave < 4) {
    const v4u srd = make_srd(x, 8 * DM * 2);
    dma16<XNT>(lds0 + L_X + wave * 2080, voff, srd, wave * 2048);
    dma16<XNT>(lds0 + L_X + wave * 2080 + 1040, voff, srd, wave * 2048 + 1024);
    if (wave == 1 && lane < 2)
      __builtin_amdgcn_raw_buffer_store_b32(tstart, wsr, lane ? WS_NB + b * 128 + jb * 4 : WS_NK + kvh * 128 + jk * 4, 0, AUX_SC1);
    if (CODEPF && bid < CODEPF && wave == 3) {
      u64 lo; unsigned back, la, lb;
      asm volatile("s_getpc_b64 %0\n.Lpf_here:\n\ts_mov_b32 %1, .Lpf_here-.Lpf_begin\n\ts_mov_b32 %2, .Lpf_end_a-.Lpf_begin\n\ts_mov_b32 %3, .Lpf_end_b-.Lpf_begin"
                   : "=s"(lo), "=s"(back), "=s"(la), "=s"(lb));
      const v4u cs = make_srd((const void*)(lo - back), la > lb ? la : lb);
      int co = lane * 128;
#pragma unroll 1
      for (int i = 0; i < 3; ++i) {
        asm volatile("s_mov_b32 m0, %0\n\ts_nop 0\n\tbuffer_load_dword %1, %2, 0 offen lds" :: "s"(lds0 + L_T), "v"(co), "s"(cs) : "memory");
        co += 8192;
      }
    }
    if (PRETOUCH && wave == 2) {
      const v4u vs = make_srd(vc, 64 << 20), os = make_srd(wo, HQ * DH * DM * 2);
      if (lane == 0) {
        asm volatile("s_mov_b32 m0, %0\n\ts_nop 0\n\tbuffer_load_dword %1, %2, 0 offen nt lds" :: "s"(lds0 + L_T + 512), "v"(kvo + 4096), "s"(vs) : "memory");
        asm volatile("s_mov_b32 m0, %0\n\ts_nop 0\n\tbuffer_load_dword %1, %2, 0 offen lds" :: "s"(lds0 + L_T + 512), "v"(kvh << 19), "s"(os) : "memory");
      }
    }
    if (threadIdx.x < 16) flags[threadIdx.x] = 0u;
    SB();
  } else {
    const v4u ksd = make_srd(kc, 64 << 20);
    dma16<true>(lds0 + L_K0 + (wave - 4) * 2048, koff, ksd, 0);
    dma16<true>(lds0 + L_K0 + (wave - 4) * 2048 + 1024, koff, ksd, 8192);
    STAMP(1);
    const v4u srd = make_srd(wq, DM * HQ * DH * 2);
    const int vo = (sp * 128 + (wave - 4) * 16 + (lane >> 3)) * 8192 + kvh * 1024 + b * 128 + (lane & 7) * 16;
    dma16<true>(lds0 + L_B + (wave - 4) * 2048, vo, srd, 0);
    dma16<true>(lds0 + L_B + (wave - 4) * 2048 + 1024, vo, srd, 8 * 8192);
    SB();
  }
  WG_BARRIER();
  const int wgl = (b << 5) | jb;
  const int cj = (b & 3) * 4 + sp, bh = b >> 2;
  const int partg = WS_PART + (kvh * 2 + bh) * 16384;
  const int me = (b & 3) * 4 + sp;
  const int fjb = (b + 3 * (b & 3)) & 7;
  const bool fin = jb == fjb;

  if (wave < 4) {
    __builtin_amdgcn_s_setprio(3);
    if (wave != 1) {
      WAITV(0);
      if (lane == 0) __hip_atomic_fetch_add(flags + F_XW, 1u, RLX_WG);
    }
    if (wave == 0) {
      *(LAS v4u*)(lds + L_X + 8 * 1040 + lane * 16) = v4u{0x3f803f80u, 0x3f803f80u, 0x3f803f80u, 0x3f803f80u};
      while ((__hip_atomic_load(flags + F_XW, RLX_WG) & 255u) != 4u) __builtin_amdgcn_s_sleep(1);
      CFENCE();
      STAMP(1);
      const int xo = L_X + (r < 8 ? r : 8) * 1040 + g * 16;
      f32x4 d2a = {0.f, 0.f, 0.f, 0.f}, d2b = {0.f, 0.f, 0.f, 0.f};
#pragma unroll
      for (int s = 0; s < 16; s += 2) {
        const v4u xa = *(LAS v4u*)(lds + xo + s * 64), xb = *(LAS v4u*)(lds + xo + s * 64 + 64);
        d2a = mfma16(xa, xa, d2a); d2b = mfma16(xb, xb, d2b);
      }
      d2a += d2b;
      const float dg = (r & 2) ? ((r & 1) ? d2a[3] : d2a[2]) : ((r & 1) ? d2a[1] : d2a[0]);
      const float sxx = xrow_sum(g == (r >> 2) ? dg : 0.f);
      const float mean = xrow_sum(g == 2 ? d2a[0] : 0.f) * (1.f / DM);
      float rstd = rsqrtf(fmaxf(sxx * (1.f / DM) - mean * mean, 0.f) + 1e-5f);
      v4u xb[4];
#pragma unroll
      for (int s = 0; s < 4; ++s) { xb[s] = *(LAS v4u*)(lds + xo + sp * 256 + s * 64); asm volatile("" : "+v"(xb[s])); }
      asm volatile("" : "+v"(rstd));
      while (__hip_atomic_load(flags + F_XW, RLX_WG) != 4u + 8u * 256u) __builtin_amdgcn_s_sleep(1);
      CFENCE();
      STAMP(2);
      f32x4 d[4];
#pragma unroll
      for (int nt = 0; nt < 4; ++nt) d[nt] = f32x4{0.f, 0.f, 0.f, 0.f};
      const int troff = L_B + (8 * g + (r >> 2)) * 128 + (r & 3) * 8;
#pragma unroll
      for (int s = 0; s < 4; ++s)
#pragma unroll
        for (int nt = 0; nt < 4; ++nt) {
          const v2u u0 = __builtin_bit_cast(v2u, __builtin_amdgcn_ds_read_tr16_b64_v4i16((LAS v4s*)(lds + troff + s * 4096 + nt * 32)));
          const v2u u1 = __builtin_bit_cast(v2u, __builtin_amdgcn_ds_read_tr16_b64_v4i16((LAS v4s*)(lds + troff + s * 4096 + 512 + nt * 32)));
          d[nt] = mfma16(v4u{u0.x, u0.y, u1.x, u1.y}, xb[s], d[nt]);
        }
#pragma unroll
      for (int nt = 0; nt < 4; ++nt) {
        float qv[4];
#pragma unroll
        for (int i = 0; i < 4; ++i) qv[i] = (d[nt][i] - mean * dpp<0x158>(d[nt][i])) * rstd;
        if (r < 8) *(LAS v2u*)(lds + L_QO + r * 128 + nt * 32 + g * 8) = v2u{cvtpk(qv[0], qv[1]), cvtpk(qv[2], qv[3])};
      }
      LGKM0();
      if (lane == 0) __hip_atomic_store(flags + F_QO, 1u, RLX_WG);
      STAMP(3);
    } else if (wave == 1) {
      const int emo = WS_EM + wgl * 512 + lane * 8;
      const int qdo = WS_QD + (kvh * 8 + g) * 8192 + jk * 256 + r * 16;
      const int nno = lane < 32 ? WS_NK + kvh * 128 + lane * 4 : WS_NB + b * 128 + (lane - 32) * 4;
      const int eco = WS_EM + (lane < 32 ? ((((lane & 7) << 5) | (lane & 24) | kvh) * 64 + jk) : (((b << 5) | (lane - 32)) * 64 + 32 + jb)) * 8;
      const v4u e4 = {EMPTY, EMPTY, EMPTY, EMPTY};
      __builtin_amdgcn_raw_buffer_store_b128(e4, wsr, partg + me * 1024 + lane * 16, 0, AUX_SC1);
      if (lane < 42)
        __builtin_amdgcn_raw_buffer_store_b128(e4, wsr, (lane < 32 ? WS_OUTP + (b * 8 + kvh) * 2048 + sp * 512 : lane < 36 ? WS_STAT - 512 + wgl * 128 : WS_TAB - 576 + wgl * 96) + lane * 16, 0, AUX_SC1);
      SB();
      v2u em = __builtin_amdgcn_raw_buffer_load_b64(wsr, emo, 0, AUX_SC1);
      v4u s0 = __builtin_amdgcn_raw_buffer_load_b128(wsr, qdo, 0, AUX_SC1);
      v4u s1 = __builtin_amdgcn_raw_buffer_load_b128(wsr, qdo, 4 * 8192, AUX_SC1);
      SB();
      WAITV(6);
      if (lane == 0) __hip_atomic_fetch_add(flags + F_XW, 1u, RLX_WG);
      pace<NNT>(tstart);
      STAMP(1);
      unsigned nn = __builtin_amdgcn_raw_buffer_load_b32(wsr, nno, 0, AUX_SC1);
      asm volatile("s_waitcnt vmcnt(0)" : "+v"(em), "+v"(s0), "+v"(s1), "+v"(nn) :: "memory");
      unsigned nonce = tstart;
      while (__any((em[1] == nonce) | (s0[1] == nonce) | (s0[3] == nonce) | (s1[1] == nonce) | (s1[3] == nonce))) ++nonce;
      if (nonce != tstart) {
        if (lane < 2) __builtin_amdgcn_raw_buffer_store_b32(nonce, wsr, lane ? WS_NB + b * 128 + jb * 4 : WS_NK + kvh * 128 + jk * 4, 0, AUX_SC1);
        WAITV(0);
      }
      if (lane < 32) __builtin_amdgcn_raw_buffer_store_b64(v2u{nonce, nn}, wsr, eco, 0, AUX_SC1);
      if (lane == 0) __hip_atomic_store(flags + F_CLR, 1u, RLX_WG);
      STAMP(2);
      unsigned cand[8];
#pragma unroll
      for (int i = 0; i < 8; ++i) cand[i] = __builtin_amdgcn_ds_bpermute((4 * i + g) * 4, nn);
      lds_wait_ne0(flags + F_QO);
      {
        const v2u q0 = *(LAS v2u*)(lds + L_QO + lane * 8), q1 = *(LAS v2u*)(lds + L_QO + 512 + lane * 8);
        __builtin_amdgcn_raw_buffer_store_b128(v4u{q0.x, nonce, q0.y, nonce}, wsr, qdo, 0, AUX_SC1);
        __builtin_amdgcn_raw_buffer_store_b128(v4u{q1.x, nonce, q1.y, nonce}, wsr, qdo, 4 * 8192, AUX_SC1);
      }
      STAMP(3);
      const int qio = WS_QD + (kvh * 8 + b) * 8192 + lane * 16;
      v4u gq[8];
#pragma unroll
      for (int i = 0; i < 8; ++i) gq[i] = v4u{0u, ~cand[i], 0u, 0u};
      pace<QT1>(tstart);
      for (unsigned spin = 0; spin < SPIN_CAP; ++spin) {
        CFENCE();
        const unsigned n2 = __builtin_amdgcn_raw_buffer_load_b32(wsr, nno, 0, AUX_SC1);
        if (lane < 32) em = __builtin_amdgcn_raw_buffer_load_b64(wsr, emo, 0, AUX_SC1);
#pragma unroll
        for (int i = 0; i < 8; ++i)
          if ((gq[i][1] != cand[i]) | (gq[i][3] != cand[i])) gq[i] = __builtin_amdgcn_raw_buffer_load_b128(wsr, qio, i * 1024, AUX_SC1);
        if (__any(n2 != nn)) {
          if (n2 != nn && lane < 32) __builtin_amdgcn_raw_buffer_store_b64(v2u{nonce, n2}, wsr, eco, 0, AUX_SC1);
          nn = n2;
#pragma unroll
          for (int i = 0; i < 8; ++i) cand[i] = __builtin_amdgcn_ds_bpermute((4 * i + g) * 4, nn);
        }
        bool ok = (lane >= 32) | ((em[1] == nonce) & (em[0] == nn));
#pragma unroll
        for (int i = 0; i < 8; ++i) ok = ok & (gq[i][1] == cand[i]) & (gq[i][3] == cand[i]);
        if (__all(ok)) break;
        __builtin_amdgcn_s_sleep(QSL);
      }
#pragma unroll
      for (int hf = 0; hf < 2; ++hf) {
        float q0 = 0.f, q1 = 0.f, q2 = 0.f, q3 = 0.f;
#pragma unroll
        for (int kq = 0; kq < 4; ++kq) {
          const v4u t4 = gq[2 * kq + hf];
          q0 += bflo(t4[0]); q1 += bfhi(t4[0]); q2 += bflo(t4[2]); q3 += bfhi(t4[2]);
        }
        *(LAS v2u*)(lds + L_Q + hf * 512 + lane * 8) = v2u{cvtpk(q0, q1), cvtpk(q2, q3)};
      }
      {
        const int df = (int)(nn - tstart);
        const bool use = lane >= 32 && df > -256 && df < 256;
        const float sd = wave_sum(use ? (float)df : 0.f), cn = wave_sum(use ? 1.f : 0.f);
        if (lane == 0) flags[F_TB] = tstart + (unsigned)(int)(sd * __builtin_amdgcn_rcpf(fmaxf(cn, 1.f)));
      }
      LGKM0();
      if (lane == 0) __hip_atomic_store(flags + F_Q, 1u, RLX_WG);
      STAMP(4);
      const bool r2 = lane >= 32 && (fin || lane - 32 == fjb);
      if (r2) __builtin_amdgcn_raw_buffer_store_b64(v2u{nonce, nn}, wsr, eco, 0, AUX_SC1);
      for (unsigned spin = 0; spin < SPIN_CAP; ++spin) {
        __builtin_amdgcn_s_sleep(48);
        CFENCE();
        unsigned n2 = nn;
        if (r2) {
          n2 = __builtin_amdgcn_raw_buffer_load_b32(wsr, nno, 0, AUX_SC1);
          em = __builtin_amdgcn_raw_buffer_load_b64(wsr, emo, 0, AUX_SC1);
          if (n2 != nn) __builtin_amdgcn_raw_buffer_store_b64(v2u{nonce, n2}, wsr, eco, 0, AUX_SC1);
        }
        nn = n2;
        if (__all(!r2 | ((em[1] == nonce) & (em[0] == nn)))) break;
      }
      STAMP(7);
      if (fin) {
        pace<TABT>(tstart);
#pragma unroll 1
        for (int c = 0; c < 3; ++c) {
          for (unsigned spin = 0; spin < SPIN_CAP; ++spin) {
            CFENCE();
            v4u tv[8];
#pragma unroll
            for (int i = 0; i < 8; ++i) tv[i] = __builtin_amdgcn_raw_buffer_load_b128(wsr, WS_TAB + c * 8192 + lane * 16, i * 1024, AUX_SC1);
            bool ok = true;
#pragma unroll
            for (int i = 0; i < 8; ++i) {
              ok = ok & (tv[i][1] == 1u) & (tv[i][3] == 1u);
              *(LAS v2u*)(lds + L_T + c * 4096 + i * 512 + lane * 8) = v2u{tv[i][0], tv[i][2]};
            }
            if (__all(ok)) break;
            __builtin_amdgcn_s_sleep(16);
          }
        }
        LGKM0();
        if (lane == 0) __hip_atomic_store(flags + F_FIN, 1u, RLX_WG);
        asm volatile(".Lpf_end_a:");
        STAMP(8);
      }
    } else if (wave == 2) {
      pace<PART>(tstart);
      const v4u s1 = make_srd(w1, FF * 2), s2 = make_srd(b1, FF * 2), s3 = make_srd(w2, FF * 2);
#pragma unroll
      for (int i = 0; i < 3; ++i) {
        dma16<PNT>(lds0 + L_P + i * 1024, voff, s1, i * 1024);
        dma16<PNT>(lds0 + L_P + 3584 + i * 1024, voff, s2, i * 1024);
        dma16<PNT>(lds0 + L_P + 7168 + i * 1024, voff, s3, i * 1024);
      }
      if (lane < 32) {
        dma16<PNT>(lds0 + L_P + 3072, voff, s1, 3072);
        dma16<PNT>(lds0 + L_P + 3584 + 3072, voff, s2, 3072);
        dma16<PNT>(lds0 + L_P + 7168 + 3072, voff, s3, 3072);
      }
      const unsigned b2u = b2[0];
      if (lane == 0) flags[F_B2] = b2u;
      asm volatile("s_waitcnt vmcnt(0) lgkmcnt(0)" ::: "memory");
      if (lane == 0) __hip_atomic_store(flags + F_P, 1u, RLX_WG);
      STAMP(1);
    }
    if (wave != 1) {
      __builtin_amdgcn_s_setprio(0);
      lds_wait_ne0(flags + F_P);
      lds_wait_ne0(flags + F_CLR);
      const int p0 = wave == 0 ? 0 : 2 * wave - 2;
#pragma unroll 1
      for (int ps = p0; ps < p0 + 2; ++ps) {
        const unsigned bits = (unsigned)((wgl & 127) * 24 + ps * 4 + (TAB_E0 << 7)) | (unsigned)((wgl >> 7) << 15);
        f32x2 t[2], a[2];
#pragma unroll
        for (int p = 0; p < 2; ++p) t[p] = f32x2{bflo(bits + 2 * p), bflo(bits + 2 * p + 1)};
        ffn_eval<2>(lds, lane, t, a);
        const unsigned k0 = cvtpk(wave_sum(a[0][0]), wave_sum(a[0][1])), k1 = cvtpk(wave_sum(a[1][0]), wave_sum(a[1][1]));
        __builtin_amdgcn_s_setprio(3);
        if (lane == 0) __builtin_amdgcn_raw_buffer_store_b128(v4u{k0, 1u, k1, 1u}, wsr, WS_TAB + wgl * 96 + ps * 16, 0, AUX_SC1);
        __builtin_amdgcn_s_setprio(0);
      }
      __builtin_amdgcn_s_setprio(3);
      STAMP(6);
    }
    if (wave == 0) {
      while (__hip_atomic_load(flags + F_CNT, RLX_WG) != 8u) __builtin_amdgcn_s_sleep(2);
      CFENCE();
      STAMP(7);
      const int hd = lane & 3;
      float M = -INFINITY, L = 0.f;
#pragma unroll
      for (int w = 0; w < 8; ++w) M = fmaxf(M, ((LAS float*)(lds + L_ML))[w * 8 + hd]);
#pragma unroll
      for (int w = 0; w < 8; ++w) L = fmaf(__builtin_amdgcn_exp2f(((LAS float*)(lds + L_ML))[w * 8 + hd] - M), ((LAS float*)(lds + L_ML))[w * 8 + 4 + hd], L);
      if (lane < 4) __builtin_amdgcn_raw_buffer_store_b128(v4u{__float_as_uint(M), 1u, __float_as_uint(L), 1u}, wsr, WS_STAT + wgl * 128 + lane * 16, 0, AUX_SC1);
      v4u sv = {0u, 1u, 0u, 1u};
      for (unsigned spin = 0; spin < SPIN_CAP; ++spin) {
        __builtin_amdgcn_s_sleep(8);
        CFENCE();
        if (lane < 16) sv = __builtin_amdgcn_raw_buffer_load_b128(wsr, WS_STAT + ((b << 5) | ((lane >> 2) << 3) | kvh) * 128 + hd * 16, 0, AUX_SC1);
        if (__all((sv[1] == 1u) & (sv[3] == 1u))) break;
      }
      const float ms = __uint_as_float(sv[0]);
      float Mg = fmaxf(ms, dpp<0x124>(ms)); Mg = fmaxf(Mg, dpp<0x128>(Mg));
      float Lg = __builtin_amdgcn_exp2f(ms - Mg) * __uint_as_float(sv[2]);
      Lg += dpp<0x128>(Lg); Lg += dpp<0x124>(Lg);
      if (lane < 4) ((LAS float*)(lds + L_Z))[lane] = Mg + __builtin_amdgcn_logf(Lg);
      LGKM0();
      if (lane == 0) __hip_atomic_store(flags + F_Z, 1u, RLX_WG);
      STAMP(8);
    }
  } else {
    const int sw = wave - 4, tid = threadIdx.x - 256;
    const __amdgpu_buffer_rsrc_t vrs = __builtin_amdgcn_make_buffer_rsrc((void*)vc, 0, 64 << 20, 0x00020000);
    const int vvo = kvo + voff;
    const v4u wsd = make_srd(wo, HQ * DH * DM * 2);
    const int wo_ = (kvh * 512 + sw + ((lane >> 2) & 3) * 128 + (lane >> 4) * 8) * 1024 + cj * 64 + (lane & 3) * 16;
#define WPIECE(k) do { dma16<WNT>(lds0 + L_W + sw * 4096 + (k) * 1024, wo_, wsd, (k) * 32 * 1024); SB(); } while (0)
#define VLOAD(i) do { WAITV(VW - 1); pace<sched(32 + (i))>(tb); vf[i] = __builtin_amdgcn_raw_buffer_load_b128(vrs, vvo, (i) * 8192, AUX_NT); SB(); \
    if (32 + (i) == WP0) WPIECE(0); if (32 + (i) == WP1) WPIECE(1); if (32 + (i) == WP2) WPIECE(2); if (32 + (i) == WP3) WPIECE(3); } while (0)
    const __amdgpu_buffer_rsrc_t krs = __builtin_amdgcn_make_buffer_rsrc((void*)kc, 0, 64 << 20, 0x00020000);
    v4u kf[32], vf[32];
    __builtin_amdgcn_s_setprio(2);
    unsigned tb = tstart + (STG * sw >> 4);
    static_for<KFIRST, 32>([&](auto ic) {
      constexpr int i = ic.value;
      WAITV((i < KSW ? KW1 : KW) - 1);
      pace<sched(i)>(tb);
      if (i == XWSIG) { WAITV(XWSIG - KFIRST); if (lane == 0) __hip_atomic_fetch_add(flags + F_XW, 256u, RLX_WG); }
      KLOAD(i);
      if (i == WP0) WPIECE(0);
      if (i == WP1) WPIECE(1);
    });
    STAMP(3);
    static_for<0, XV>([&](auto ic) { VLOAD(ic.value); });
    STAMP(6);
    lds_wait_ne0(flags + F_Q);
    if (TBASE) tb = __builtin_amdgcn_readfirstlane(flags[F_TB]) + TBO + (STG * sw >> 4);
    SB();
    const v4u qf = *(LAS v4u*)(lds + L_Q + (r & 3) * 256 + g * 64 + (r >> 2) * 16);
    kf[0] = *(LAS v4u*)(lds + L_K0 + sw * 2048 + voff);
    kf[1] = *(LAS v4u*)(lds + L_K0 + sw * 2048 + 1024 + voff);
    float s[32];
    static_for<0, 32>([&](auto ic) {
      constexpr int i = ic.value;
      const f32x4 a = mfma16(kf[i], qf, f32x4{0.f, 0.f, 0.f, 0.f});
      float t_ = a[0] + dpp<0x104>(a[1]); asm volatile("" : "+v"(t_));
      t_ += dpp<0x108>(a[2]); asm volatile("" : "+v"(t_));
      s[i] = t_ + dpp<0x10C>(a[3]); asm volatile("" : "+v"(s[i]));
      if constexpr ((i & 3) == 3) { SB(); VLOAD(XV + (i >> 2)); }
    });
    SB();
    float m = s[0];
#pragma unroll
    for (int i = 1; i < 32; ++i) m = fmaxf(m, s[i]);
    m = xrow_max(m) * SCALE_L2;
    asm volatile("" : "+v"(m));
    SB(); VLOAD(XV + 8);
    v4u pf[4];
#define SOFTMAX(G, mm) do { float p_[8]; \
    _Pragma("unroll") for (int j = 0; j < 8; ++j) p_[j] = __builtin_amdgcn_exp2f(fmaf(s[8 * (G) + j], SCALE_L2, -(mm))); \
    pf[G] = v4u{cvtpk(p_[0], p_[1]), cvtpk(p_[2], p_[3]), cvtpk(p_[4], p_[5]), cvtpk(p_[6], p_[7])}; } while (0)
    {
      SOFTMAX(0, m); SOFTMAX(1, m);
      asm volatile("" : "+v"(pf[0]), "+v"(pf[1]));
      SB(); VLOAD(XV + 9);
      SOFTMAX(2, m); SOFTMAX(3, m);
      asm volatile("" : "+v"(pf[2]), "+v"(pf[3]));
      SB(); VLOAD(XV + 10);
      const v4u ones = {0x3f803f80u, 0x3f803f80u, 0x3f803f80u, 0x3f803f80u};
      f32x4 l4 = {0.f, 0.f, 0.f, 0.f};
#pragma unroll
      for (int G = 0; G < 4; ++G) l4 = mfma16(pf[G], ones, l4);
      if (lane < 4) ((LAS float*)(lds + L_ML))[sw * 8 + lane] = m;
      if (lane == 0) *(LAS f32x4*)(lds + L_ML + sw * 32 + 16) = l4;
      LGKM0();
      if (lane == 0) __hip_atomic_fetch_add(flags + F_CNT, 1u, RLX_WG);
    }
    STAMP(7);
    f32x4 acc[8];
#pragma unroll
    for (int j = 0; j < 8; ++j) acc[j] = f32x4{0.f, 0.f, 0.f, 0.f};
#define PV(G) do { \
    _Pragma("unroll") for (int d = 0; d < 4; ++d) { v4u lo_, hi_; \
      _Pragma("unroll") for (int jj = 0; jj < 4; ++jj) { \
        lo_[jj] = __builtin_amdgcn_perm(vf[8 * (G) + 2 * jj + 1][d], vf[8 * (G) + 2 * jj][d], 0x05040100u); \
        hi_[jj] = __builtin_amdgcn_perm(vf[8 * (G) + 2 * jj + 1][d], vf[8 * (G) + 2 * jj][d], 0x07060302u); } \
      acc[2 * d] = mfma16(pf[G], lo_, acc[2 * d]); acc[2 * d + 1] = mfma16(pf[G], hi_, acc[2 * d + 1]); } SB(); } while (0)
    static_for<XV + 11, 32>([&](auto ic) {
      constexpr int i = ic.value;
      VLOAD(i);
      if constexpr (i == XV + 11) PV(0);
      if constexpr (i == 15 + PVD) PV(1);
      if constexpr (i == 23 + PVD) PV(2);
    });
    STAMP(8);
    v4u wf[2][2];
    {
      const int wtr = L_W + sw * 4096 + (g * 8 + (r >> 2)) * 64 + (r & 3) * 8;
#pragma unroll
      for (int ks = 0; ks < 2; ++ks)
#pragma unroll
        for (int nt = 0; nt < 2; ++nt) {
          const v2u u0 = __builtin_bit_cast(v2u, __builtin_amdgcn_ds_read_tr16_b64_v4i16((LAS v4s*)(lds + wtr + ks * 2048 + nt * 32)));
          const v2u u1 = __builtin_bit_cast(v2u, __builtin_amdgcn_ds_read_tr16_b64_v4i16((LAS v4s*)(lds + wtr + ks * 2048 + 256 + nt * 32)));
          wf[ks][nt] = v4u{u0.x, u0.y, u1.x, u1.y};
          asm volatile("" : "+v"(wf[ks][nt]));
        }
      SB();
    }
    lds_wait_ne0(flags + F_Z);
    SB();
    {
      const f32x4 Z4 = *(LAS f32x4*)(lds + L_Z);
      const f32x4 mo = *(LAS f32x4*)(lds + L_ML + sw * 32);
      const float zl = ((LAS float*)(lds + L_Z))[lane & 3];
      f32x4 f4;
#pragma unroll
      for (int i = 0; i < 4; ++i) f4[i] = __builtin_amdgcn_exp2f(mo[i] - Z4[i]);
#pragma unroll
      for (int j = 0; j < 8; ++j) acc[j] *= f4;
      const float zz = (r < 4) ? zl : m;
      SOFTMAX(3, zz);
      SB();
    }
    {
      v4u lo_[4], hi_[4];
#pragma unroll
      for (int jj = 0; jj < 4; ++jj) {
#pragma unroll
        for (int d = 0; d < 4; ++d) {
          lo_[d][jj] = __builtin_amdgcn_perm(vf[24 + 2 * jj + 1][d], vf[24 + 2 * jj][d], 0x05040100u);
          hi_[d][jj] = __builtin_amdgcn_perm(vf[24 + 2 * jj + 1][d], vf[24 + 2 * jj][d], 0x07060302u);
        }
        SB();
      }
#pragma unroll
      for (int d = 0; d < 4; ++d) { acc[2 * d] = mfma16(pf[3], lo_[d], acc[2 * d]); acc[2 * d + 1] = mfma16(pf[3], hi_[d], acc[2 * d + 1]); }
      SB();
    }
    STAMP(9);
    if (lane < 16) {
#pragma unroll
      for (int j = 0; j < 8; ++j) *(LAS f32x4*)(lds + L_B + ((sw * 8 + j) * 16 + lane) * 16) = acc[j];
    }
    WG_BARRIER();
    STAMP(10);
    LAS unsigned char* aim = lds + L_A + sw * 2304;
    if (lane < 16) {
      f32x4 o = *(LAS f32x4*)(lds + L_B + (sw * 16 + lane) * 16);
#pragma unroll
      for (int w = 1; w < 8; ++w) o += *(LAS f32x4*)(lds + L_B + ((w * 8 + sw) * 16 + lane) * 16);
      const v2u op = {cvtpk(o[0], o[1]), min(cvtpk(o[2], o[3]), EMPTY - 1u)};
      __builtin_amdgcn_raw_buffer_store_b64(op, wsr, partg + me * 1024 + sw * 128 + lane * 8, 0, AUX_SC1);
      *(LAS v2u*)(aim + me * 144 + lane * 8) = op;
    }
    SB();
    {
      const int po = partg + (lane >> 3) * 1024 + sw * 128 + (lane & 7) * 16;
      bool ok0 = (lane >> 3) == me, ok1 = (lane >> 3) + 8 == me;
      __builtin_amdgcn_s_sleep(H2D);
      for (unsigned spin = 0; spin < SPIN_CAP; ++spin) {
        CFENCE();
        v4u p0 = {EMPTY, EMPTY, EMPTY, EMPTY}, p1 = {EMPTY, EMPTY, EMPTY, EMPTY};
        if (!ok0) p0 = __builtin_amdgcn_raw_buffer_load_b128(wsr, po, 0, AUX_SC1);
        if (!ok1) p1 = __builtin_amdgcn_raw_buffer_load_b128(wsr, po, 8192, AUX_SC1);
        if ((p0[1] != EMPTY) & (p0[3] != EMPTY)) { ok0 = true; *(LAS v4u*)(aim + (lane >> 3) * 144 + (lane & 7) * 16) = p0; }
        if ((p1[1] != EMPTY) & (p1[3] != EMPTY)) { ok1 = true; *(LAS v4u*)(aim + (lane >> 3) * 144 + 1152 + (lane & 7) * 16) = p1; }
        if (__all(ok0 & ok1)) break;
      }
      STAMP(11);
      f32x4 c0 = {0.f, 0.f, 0.f, 0.f}, c1 = {0.f, 0.f, 0.f, 0.f};
#pragma unroll
      for (int ks = 0; ks < 2; ++ks) {
        const v4u af = *(LAS v4u*)(aim + r * 144 + ks * 64 + g * 16);
        c0 = mfma16(af, wf[ks][0], c0);
        c1 = mfma16(af, wf[ks][1], c1);
      }
      LAS float* red = (LAS float*)(lds + L_W);
      red[sw * 128 + lane] = (c0[0] + c0[1]) + (c0[2] + c0[3]);
      red[sw * 128 + 64 + lane] = (c1[0] + c1[1]) + (c1[2] + c1[3]);
    }
    WG_BARRIER();
    if (sw < 2) {
      LAS float* red = (LAS float*)(lds + L_W) + (g & 1) * 64 + (2 * sw + (g >> 1)) * 16 + r;
      float a = red[0];
#pragma unroll
      for (int w = 1; w < 8; ++w) a += red[w * 128];
      __builtin_amdgcn_raw_buffer_store_b32(min(__float_as_uint(a), EMPTY - 1u), wsr, WS_OUTP + (((bh * 4 + 2 * sw + (g >> 1)) * 8 + kvh) * 512 + cj * 32 + (lane & 31)) * 4, 0, AUX_SC1);
    }
    STAMP(12);
    if (fin) {
      lds_wait_ne0(flags + F_FIN);
      const float xres = bflo(((LAS us*)(lds + L_X))[b * 520 + tid]), b2v = bflo(flags[F_B2]);
      unsigned pv[8];
#pragma unroll
      for (int k = 0; k < 8; ++k) pv[k] = EMPTY;
      for (unsigned spin = 0; spin < SPIN_CAP; ++spin) {
        CFENCE();
#pragma unroll
        for (int k = 0; k < 8; ++k)
          if (pv[k] == EMPTY) pv[k] = __builtin_amdgcn_raw_buffer_load_b32(wsr, WS_OUTP + b * 16384 + tid * 4, k * 2048, AUX_SC1);
        bool ok = true;
#pragma unroll
        for (int k = 0; k < 8; ++k) ok = ok & (pv[k] != EMPTY);
        if (__all(ok)) break;
      }
      STAMP(13);
      const float a = ((__uint_as_float(pv[0]) + __uint_as_float(pv[1])) + (__uint_as_float(pv[2]) + __uint_as_float(pv[3]))) +
                      ((__uint_as_float(pv[4]) + __uint_as_float(pv[5])) + (__uint_as_float(pv[6]) + __uint_as_float(pv[7])));
      const float h = rbf(xres + rbf(a));
      const float s1 = wave_sum(h), s2 = wave_sum(h * h);
      if (lane == 0) *(LAS f32x2*)(lds + L_B + sw * 8) = f32x2{s1, s2};
      WG_BARRIER();
      const f32x4 q0 = *(LAS f32x4*)(lds + L_B), q1 = *(LAS f32x4*)(lds + L_B + 16), q2 = *(LAS f32x4*)(lds + L_B + 32), q3 = *(LAS f32x4*)(lds + L_B + 48);
      const f32x4 qs = (q0 + q1) + (q2 + q3);
      const float mean = (qs[0] + qs[2]) * (1.f / DM);
      const float var = fmaxf((qs[1] + qs[3]) * (1.f / DM) - mean * mean, 0.f);
      const float r1 = __builtin_amdgcn_rsqf(var + 1e-5f);
      const float r2 = __builtin_amdgcn_rsqf(fmaf(var * r1, r1, 1e-5f));
      const float t = ((h - mean) * r1) * r2;
      const unsigned tb = cvtpk(t, t) & 0xffffu;
      const unsigned rel = (tb & 0x7fffu) - (unsigned)(TAB_E0 << 7);
      const bool inr = rel < (unsigned)TAB_H;
      float y = bflo(((LAS us*)(lds + L_T))[(inr ? rel : 0u) + (tb >> 15) * TAB_H]);
      u64 miss = __ballot(!inr);
      while (miss) {
        const int l = __builtin_ctzll(miss);
        miss &= miss - 1;
        const float ts = bflo((unsigned)__builtin_amdgcn_readlane((int)tb, l));
        const f32x2 tt[1] = {f32x2{ts, ts}};
        f32x2 aa[1];
        ffn_eval<1>(lds, lane, tt, aa);
        const float ys = rbf(wave_sum(aa[0][0]));
        if (lane == l) y = ys;
      }
      const float ov = h + rbf(y + b2v);
      out[b * DM + tid] = (us)(cvtpk(ov, ov) & 0xffffu);
      STAMP(14);
    }
  }
  asm volatile(".Lpf_end_b:");
}

extern "C" void kernel_launch(void* const* d_in, const int* in_sizes, int n_in, void* d_out, int out_size, void* d_ws, size_t ws_size, hipStream_t stream) {
  fused_kernel<<<256, 768, 0, stream>>>((const us*)d_in[0], (const us*)d_in[1], (const us*)d_in[2], (const us*)d_in[3], (const us*)d_in[4],
                                        (const us*)d_in[6], (const us*)d_in[7], (const us*)d_in[8], (const us*)d_in[9], (us*)d_out, (unsigned char*)d_ws);
}
```

```cpp
#include <hip/hip_runtime.h>
#include <hip/hip_bf16.h>
#include <stdint.h>
#include <type_traits>

#define GAS __attribute__((address_space(1)))
#define LAS __attribute__((address_space(3)))
#define RLX_AGENT __ATOMIC_RELAXED, __HIP_MEMORY_SCOPE_AGENT
#define RLX_WG __ATOMIC_RELAXED, __HIP_MEMORY_SCOPE_WORKGROUP
typedef unsigned v4u __attribute__((ext_vector_type(4)));
typedef unsigned v2u __attribute__((ext_vector_type(2)));
typedef float f32x4 __attribute__((ext_vector_type(4)));
typedef float f32x2 __attribute__((ext_vector_type(2)));
typedef short bf16x8 __attribute__((ext_vector_type(8)));
typedef short v4s __attribute__((ext_vector_type(4)));
typedef __bf16 bf16x2_t __attribute__((ext_vector_type(2)));
typedef unsigned long long u64;
typedef GAS unsigned gu32;
typedef GAS u64 gu64;
typedef unsigned short us;

constexpr int DM = 512, HQ = 32, DH = 128, FF = 1792;
constexpr float LOG2E = 1.4426950408889634f;
constexpr float SCALE_L2 = 0.08838834764831845f * LOG2E;
constexpr unsigned SPIN_CAP = 100000u;
constexpr int AUX_NT = 2, AUX_SC1 = 16;
constexpr unsigned EMPTY = 0xffffffffu;
constexpr int TAB_E0 = 108;
constexpr int TAB_H = 24 * 128;

constexpr int CODEPF = 16;
constexpr int KFIRST = 2;
constexpr int PER16 = 474;
constexpr int LEAD = 2;
constexpr int NE = 24;
constexpr int EX16 = 125;
constexpr int WC16 = 260;
constexpr int XWSIG = 5;
constexpr int PVD = 5;
constexpr int XV = 2;
constexpr int PSL = 2;
constexpr int PSLT = 7;
constexpr int NNT = 130;
constexpr int KW = 4;
constexpr int VW = 6;
constexpr int PART = 250;
constexpr int QT1 = 480;
constexpr int QSL = 8;
constexpr int TBO = -7;
constexpr int STG = 30;
constexpr int WP0 = 15;
constexpr int WP1 = 23;
constexpr int WP2 = 35;
constexpr int WP3 = 43;
constexpr int H3D = 4;
constexpr int H3E = 3;
constexpr int H2T = 152;
constexpr int TABT = 1200;
constexpr int sched(int n) {
  return ((n - LEAD) * PER16 + (n < NE ? n : NE) * EX16 + ((n > WP0) + (n > WP1) + (n > WP2) + (n > WP3)) * WC16) >> 4;
}

constexpr int WS_NK = 0;
constexpr int WS_NB = 1024;
constexpr int WS_STAT = 4096;
constexpr int WS_TAB = WS_STAT + 256 * 128;
constexpr int WS_EM = WS_TAB + 256 * 128;
constexpr int WS_PART = WS_EM + 256 * 512;
constexpr int WS_OUTP = WS_PART + 256 * 1024;
constexpr int WS_QD = WS_OUTP + 64 * 2048;
constexpr int WS_END = WS_QD + 64 * 8192;

constexpr int L_W = 0;
constexpr int L_B = 32768;
constexpr int L_X = L_B + 16384;
constexpr int L_P = L_X + 9472;
constexpr int L_T = L_P + 10752;
constexpr int L_Q = L_T + 16384;
constexpr int L_QO = L_Q + 1024;
constexpr int L_ML = L_QO + 1024;
constexpr int L_Z = L_ML + 256;
constexpr int L_FL = L_Z + 64;
constexpr int L_A = L_FL + 64;
constexpr int L_K0 = L_A + 8 * 2304;
constexpr int L_TOTAL = L_K0 + 8 * 2048;
enum { F_Q = 0, F_CNT = 1, F_QO = 2, F_Z = 3, F_P = 4, F_FIN = 5, F_XW = 6, F_B2 = 7, F_CLR = 8, F_TB = 9 };

__device__ __forceinline__ f32x4 mfma16(v4u a, v4u b, f32x4 c) {
  return __builtin_amdgcn_mfma_f32_16x16x32_bf16(__builtin_bit_cast(bf16x8, a), __builtin_bit_cast(bf16x8, b), c, 0, 0, 0);
}
__device__ __forceinline__ unsigned cvtpk(float lo, float hi) {
  return __builtin_bit_cast(unsigned, __builtin_convertvector(f32x2{lo, hi}, bf16x2_t));
}
__device__ __forceinline__ float bflo(unsigned u) { return __builtin_bit_cast(float, u << 16); }
__device__ __forceinline__ float bfhi(unsigned u) { return __builtin_bit_cast(float, u & 0xffff0000u); }
__device__ __forceinline__ float rbf(float x) { return bflo(cvtpk(x, x)); }
__device__ __forceinline__ float xrow_max(float x) {
  auto s = __builtin_amdgcn_permlane16_swap(__float_as_uint(x), __float_as_uint(x), false, false);
  x = fmaxf(__uint_as_float(s[0]), __uint_as_float(s[1]));
  auto t = __builtin_amdgcn_permlane32_swap(__float_as_uint(x), __float_as_uint(x), false, false);
  return fmaxf(__uint_as_float(t[0]), __uint_as_float(t[1]));
}
__device__ __forceinline__ float xrow_sum(float x) {
  auto s = __builtin_amdgcn_permlane16_swap(__float_as_uint(x), __float_as_uint(x), false, false);
  x = __uint_as_float(s[0]) + __uint_as_float(s[1]);
  auto t = __builtin_amdgcn_permlane32_swap(__float_as_uint(x), __float_as_uint(x), false, false);
  return __uint_as_float(t[0]) + __uint_as_float(t[1]);
}
template <int CTRL> __device__ __forceinline__ float dpp(float x) {
  return __builtin_bit_cast(float, __builtin_amdgcn_mov_dpp(__builtin_bit_cast(int, x), CTRL, 0xf, 0xf, true));
}
__device__ __forceinline__ float wave_sum(float x) {
  x += dpp<0xB1>(x); x += dpp<0x4E>(x); x += dpp<0x141>(x); x += dpp<0x128>(x);
  return xrow_sum(x);
}
__device__ __forceinline__ void wave_sum2(float& x, float& y) {
  x += dpp<0xB1>(x); y += dpp<0xB1>(y); __builtin_amdgcn_sched_barrier(0);
  x += dpp<0x4E>(x); y += dpp<0x4E>(y); __builtin_amdgcn_sched_barrier(0);
  x += dpp<0x141>(x); y += dpp<0x141>(y); __builtin_amdgcn_sched_barrier(0);
  x += dpp<0x128>(x); y += dpp<0x128>(y); __builtin_amdgcn_sched_barrier(0);
  auto s = __builtin_amdgcn_permlane16_swap(__float_as_uint(x), __float_as_uint(y), false, false);
  const float z = __uint_as_float(s[0]) + __uint_as_float(s[1]);
  auto t = __builtin_amdgcn_permlane32_swap(__float_as_uint(z), __float_as_uint(z), false, false);
  x = __uint_as_float(t[0]) + __uint_as_float(t[1]);
  y = x;
}
#define SB() __builtin_amdgcn_sched_barrier(0)
#define CFENCE() asm volatile("" ::: "memory")
#define LGKM0() asm volatile("s_waitcnt lgkmcnt(0)" ::: "memory")
#define WAITV(n) asm volatile("s_waitcnt vmcnt(%0)" :: "i"(n) : "memory")
#define WG_BARRIER() do { asm volatile("s_waitcnt lgkmcnt(0)\n\ts_barrier" ::: "memory"); SB(); } while (0)
__device__ __forceinline__ v4u make_srd(const void* p, unsigned bytes) {
  const u64 a = (u64)p;
  v4u d;
  d[0] = __builtin_amdgcn_readfirstlane((unsigned)a);
  d[1] = __builtin_amdgcn_readfirstlane((unsigned)(a >> 32)) & 0xffffu;
  d[2] = bytes;
  d[3] = 0x00020000u;
  return d;
}
template <bool NT>
__device__ __forceinline__ void dma16(unsigned ldsaddr, int voff, v4u srd, int soff) {
  if (NT) asm volatile("s_mov_b32 m0, %0\n\ts_nop 0\n\tbuffer_load_dwordx4 %1, %2, %3 offen nt lds" :: "s"(ldsaddr), "v"(voff), "s"(srd), "s"(soff) : "memory");
  else asm volatile("s_mov_b32 m0, %0\n\ts_nop 0\n\tbuffer_load_dwordx4 %1, %2, %3 offen lds" :: "s"(ldsaddr), "v"(voff), "s"(srd), "s"(soff) : "memory");
}
__device__ __forceinline__ void lds_wait_ne0(LAS unsigned* f) {
  while (__hip_atomic_load(f, RLX_WG) == 0u) __builtin_amdgcn_s_sleep(1);
  CFENCE();
}
template <int TICKS, int SL = PSL, int SLT = PSLT>
__device__ __forceinline__ void pace(unsigned tstart) {
  if (TICKS > 0)
    asm volatile("s_memrealtime vcc\n\ts_waitcnt lgkmcnt(0)\n\ts_sub_i32 vcc_lo, vcc_lo, %0\n"
                 "1:\n\ts_cmpk_ge_i32 vcc_lo, %1\n\ts_cbranch_scc1 2f\n\ts_sleep %2\n\ts_addk_i32 vcc_lo, %3\n\ts_branch 1b\n2:"
                 :: "s"(tstart), "i"(TICKS), "i"(SL), "i"(SLT) : "vcc", "scc", "memory");
}

__device__ __forceinline__ void wait_until(unsigned t0, int ticks) {
  while ((int)((unsigned)__builtin_amdgcn_s_memrealtime() - t0) < ticks) __builtin_amdgcn_s_sleep(3);
  CFENCE();
}

template <int NP>
__device__ __forceinline__ void ffn_eval(LAS unsigned char* lds, int lane, const f32x2 (&t)[NP], f32x2 (&acc)[NP]) {
  constexpr float C0 = -2.f * 0.7978845608028654f * LOG2E, C1 = C0 * 0.044715f;
#pragma unroll
  for (int p = 0; p < NP; ++p) acc[p] = f32x2{0.f, 0.f};
#pragma unroll 1
  for (int j = 0; j < 7; ++j) {
    const v2u ua = *(LAS v2u*)(lds + L_P + j * 512 + lane * 8);
    const v2u ub = *(LAS v2u*)(lds + L_P + 3584 + j * 512 + lane * 8);
    const v2u uw = *(LAS v2u*)(lds + L_P + 7168 + j * 512 + lane * 8);
#pragma unroll
    for (int i = 0; i < 4; ++i) {
      const float a = (i & 1) ? bfhi(ua[i >> 1]) : bflo(ua[i >> 1]);
      const float b = (i & 1) ? bfhi(ub[i >> 1]) : bflo(ub[i >> 1]);
      const float w = (i & 1) ? bfhi(uw[i >> 1]) : bflo(uw[i >> 1]);
#pragma unroll
      for (int p = 0; p < NP; ++p) {
        const f32x2 u = __builtin_elementwise_fma(t[p], f32x2{a, a}, f32x2{b, b});
        const f32x2 y = u * __builtin_elementwise_fma(u * u, f32x2{C1, C1}, f32x2{C0, C0});
        f32x2 d = f32x2{__builtin_amdgcn_exp2f(y[0]), __builtin_amdgcn_exp2f(y[1])} + 1.f;
        d = f32x2{__builtin_amdgcn_rcpf(d[0]), __builtin_amdgcn_rcpf(d[1])};
        acc[p] = __builtin_elementwise_fma(u * d, f32x2{w, w}, acc[p]);
      }
    }
  }
}


template <int I, int N, class F> __device__ __forceinline__ void static_for(F&& f) {
  if constexpr (I < N) { f(std::integral_constant<int, I>{}); static_for<I + 1, N>(f); }
}

__global__ __launch_bounds__(768, 3) void fused_kernel(const us* __restrict__ x, const us* __restrict__ kc, const us* __restrict__ vc,
                                                       const us* __restrict__ wq, const us* __restrict__ wo, const us* __restrict__ w1,
                                                       const us* __restrict__ b1, const us* __restrict__ w2, const us* __restrict__ b2,
                                                       us* __restrict__ out, unsigned char* ws) {
  __shared__ __attribute__((aligned(16))) unsigned char smem[L_TOTAL];
  LAS unsigned char* lds = (LAS unsigned char*)smem;
  const unsigned lds0 = (unsigned)(uintptr_t)lds;
  const int lane = threadIdx.x & 63, wave = __builtin_amdgcn_readfirstlane(threadIdx.x >> 6), g = lane >> 4, r = lane & 15;
  LAS unsigned* flags = (LAS unsigned*)(lds + L_FL);
  asm volatile(".Lpf_begin:");
  const unsigned tstart = (unsigned)__builtin_amdgcn_s_memrealtime();
  asm volatile("" :: "s"(x), "s"(kc), "s"(vc), "s"(wq), "s"(ws));
  const int bid = blockIdx.x, sp = (bid >> 3) & 3, b = ((bid >> 3) & 4) | (bid >> 6), kvh = (bid + 3 * (bid >> 6)) & 7;
  const int voff = lane * 16;
  const int kvo = (((b * 8 + kvh) * 4 + sp) << 18) + ((wave - 4) << 10);
  const int koff = kvo + (r >> 2) * 256 + g * 64 + (r & 3) * 16;
#define KLOAD(i) do { kf[i] = __builtin_amdgcn_raw_buffer_load_b128(krs, koff, (i) * 8192, AUX_NT); SB(); } while (0)
  const __amdgpu_buffer_rsrc_t wsr = __builtin_amdgcn_make_buffer_rsrc((void*)ws, 0, WS_END, 0x00020000);
  const int jk = sp * 8 + b;
  const int jb = sp * 8 + kvh;
  if (wave < 4) {
    const v4u srd = make_srd(x, 8 * DM * 2);
    dma16<false>(lds0 + L_X + wave * 2080, voff, srd, wave * 2048);
    dma16<false>(lds0 + L_X + wave * 2080 + 1040, voff, srd, wave * 2048 + 1024);
    if (wave == 1 && lane < 2)
      __builtin_amdgcn_raw_buffer_store_b32(tstart, wsr, lane ? WS_NB + b * 128 + jb * 4 : WS_NK + kvh * 128 + jk * 4, 0, AUX_SC1);
    if (bid < CODEPF && wave == 3) {
      u64 lo; unsigned back, la, lb;
      asm volatile("s_getpc_b64 %0\n.Lpf_here:\n\ts_mov_b32 %1, .Lpf_here-.Lpf_begin\n\ts_mov_b32 %2, .Lpf_end_a-.Lpf_begin\n\ts_mov_b32 %3, .Lpf_end_b-.Lpf_begin"
                   : "=s"(lo), "=s"(back), "=s"(la), "=s"(lb));
      const v4u cs = make_srd((const void*)(lo - back), la > lb ? la : lb);
      int co = lane * 128;
#pragma unroll 1
      for (int i = 0; i < 3; ++i) {
        asm volatile("s_mov_b32 m0, %0\n\ts_nop 0\n\tbuffer_load_dword %1, %2, 0 offen lds" :: "s"(lds0 + L_T), "v"(co), "s"(cs) : "memory");
        co += 8192;
      }
    }
    if (threadIdx.x < 16) flags[threadIdx.x] = 0u;
    SB();
  } else {
    const v4u ksd = make_srd(kc, 64 << 20);
    dma16<true>(lds0 + L_K0 + (wave - 4) * 2048, koff, ksd, 0);
    dma16<true>(lds0 + L_K0 + (wave - 4) * 2048 + 1024, koff, ksd, 8192);
    const v4u srd = make_srd(wq, DM * HQ * DH * 2);
    const int vo = (sp * 128 + (wave - 4) * 16 + (lane >> 3)) * 8192 + kvh * 1024 + b * 128 + (lane & 7) * 16;
    dma16<true>(lds0 + L_B + (wave - 4) * 2048, vo, srd, 0);
    dma16<true>(lds0 + L_B + (wave - 4) * 2048 + 1024, vo, srd, 8 * 8192);
    SB();
  }
  WG_BARRIER();
  const int wgl = (b << 5) | jb;
  const int cj = (b & 3) * 4 + sp, bh = b >> 2;
  const int partg = WS_PART + (kvh * 2 + bh) * 16384;
  const int me = (b & 3) * 4 + sp;
  const int fjb = (b + 3 * (b & 3)) & 7;
  const bool fin = jb == fjb;

  if (wave < 4) {
    __builtin_amdgcn_s_setprio(3);
    if (wave != 1) {
      WAITV(0);
      if (lane == 0) __hip_atomic_fetch_add(flags + F_XW, 1u, RLX_WG);
    }
    if (wave == 0) {
      *(LAS v4u*)(lds + L_X + 8 * 1040 + lane * 16) = v4u{0x3f803f80u, 0x3f803f80u, 0x3f803f80u, 0x3f803f80u};
      while ((__hip_atomic_load(flags + F_XW, RLX_WG) & 255u) != 4u) __builtin_amdgcn_s_sleep(1);
      CFENCE();
      const int xo = L_X + (r < 8 ? r : 8) * 1040 + g * 16;
      f32x4 d2a = {0.f, 0.f, 0.f, 0.f}, d2b = {0.f, 0.f, 0.f, 0.f};
#pragma unroll
      for (int s = 0; s < 16; s += 2) {
        const v4u xa = *(LAS v4u*)(lds + xo + s * 64), xb = *(LAS v4u*)(lds + xo + s * 64 + 64);
        d2a = mfma16(xa, xa, d2a); d2b = mfma16(xb, xb, d2b);
      }
      d2a += d2b;
      const float dg = (r & 2) ? ((r & 1) ? d2a[3] : d2a[2]) : ((r & 1) ? d2a[1] : d2a[0]);
      const float sxx = xrow_sum(g == (r >> 2) ? dg : 0.f);
      const float mean = xrow_sum(g == 2 ? d2a[0] : 0.f) * (1.f / DM);
      float rstd = rsqrtf(fmaxf(sxx * (1.f / DM) - mean * mean, 0.f) + 1e-5f);
      v4u xb[4];
#pragma unroll
      for (int s = 0; s < 4; ++s) { xb[s] = *(LAS v4u*)(lds + xo + sp * 256 + s * 64); asm volatile("" : "+v"(xb[s])); }
      asm volatile("" : "+v"(rstd));
      while (__hip_atomic_load(flags + F_XW, RLX_WG) != 4u + 8u * 256u) __builtin_amdgcn_s_sleep(1);
      CFENCE();
      f32x4 d[4];
#pragma unroll
      for (int nt = 0; nt < 4; ++nt) d[nt] = f32x4{0.f, 0.f, 0.f, 0.f};
      const int troff = L_B + (8 * g + (r >> 2)) * 128 + (r & 3) * 8;
#pragma unroll
      for (int s = 0; s < 4; ++s)
#pragma unroll
        for (int nt = 0; nt < 4; ++nt) {
          const v2u u0 = __builtin_bit_cast(v2u, __builtin_amdgcn_ds_read_tr16_b64_v4i16((LAS v4s*)(lds + troff + s * 4096 + nt * 32)));
          const v2u u1 = __builtin_bit_cast(v2u, __builtin_amdgcn_ds_read_tr16_b64_v4i16((LAS v4s*)(lds + troff + s * 4096 + 512 + nt * 32)));
          d[nt] = mfma16(v4u{u0.x, u0.y, u1.x, u1.y}, xb[s], d[nt]);
        }
#pragma unroll
      for (int nt = 0; nt < 4; ++nt) {
        float qv[4];
#pragma unroll
        for (int i = 0; i < 4; ++i) qv[i] = (d[nt][i] - mean * dpp<0x158>(d[nt][i])) * rstd;
        if (r < 8) *(LAS v2u*)(lds + L_QO + r * 128 + nt * 32 + g * 8) = v2u{cvtpk(qv[0], qv[1]), cvtpk(qv[2], qv[3])};
      }
      LGKM0();
      if (lane == 0) __hip_atomic_store(flags + F_QO, 1u, RLX_WG);
    } else if (wave == 1) {
      const int emo = WS_EM + wgl * 512 + lane * 8;
      const int qdo = WS_QD + (kvh * 8 + g) * 8192 + jk * 256 + r * 16;
      const int nno = lane < 32 ? WS_NK + kvh * 128 + lane * 4 : WS_NB + b * 128 + (lane - 32) * 4;
      const int eco = WS_EM + (lane < 32 ? ((((lane & 7) << 5) | (lane & 24) | kvh) * 64 + jk) : (((b << 5) | (lane - 32)) * 64 + 32 + jb)) * 8;
      const v4u e4 = {EMPTY, EMPTY, EMPTY, EMPTY};
      __builtin_amdgcn_raw_buffer_store_b128(e4, wsr, partg + me * 1024 + lane * 16, 0, AUX_SC1);
      if (lane < 42)
        __builtin_amdgcn_raw_buffer_store_b128(e4, wsr, (lane < 32 ? WS_OUTP + (b * 8 + kvh) * 2048 + sp * 512 : lane < 36 ? WS_STAT - 512 + wgl * 128 : WS_TAB - 576 + wgl * 96) + lane * 16, 0, AUX_SC1);
      SB();
      v2u em = __builtin_amdgcn_raw_buffer_load_b64(wsr, emo, 0, AUX_SC1);
      v4u s0 = __builtin_amdgcn_raw_buffer_load_b128(wsr, qdo, 0, AUX_SC1);
      v4u s1 = __builtin_amdgcn_raw_buffer_load_b128(wsr, qdo, 4 * 8192, AUX_SC1);
      SB();
      WAITV(6);
      if (lane == 0) __hip_atomic_fetch_add(flags + F_XW, 1u, RLX_WG);
      wait_until(tstart, NNT);
      unsigned nn = __builtin_amdgcn_raw_buffer_load_b32(wsr, nno, 0, AUX_SC1);
      asm volatile("s_waitcnt vmcnt(0)" : "+v"(em), "+v"(s0), "+v"(s1), "+v"(nn) :: "memory");
      unsigned nonce = tstart;
      while (__any((em[1] == nonce) | (s0[1] == nonce) | (s0[3] == nonce) | (s1[1] == nonce) | (s1[3] == nonce))) ++nonce;
      if (nonce != tstart) {
        if (lane < 2) __builtin_amdgcn_raw_buffer_store_b32(nonce, wsr, lane ? WS_NB + b * 128 + jb * 4 : WS_NK + kvh * 128 + jk * 4, 0, AUX_SC1);
        WAITV(0);
      }
      if (lane < 32) __builtin_amdgcn_raw_buffer_store_b64(v2u{nonce, nn}, wsr, eco, 0, AUX_SC1);
      if (lane == 0) __hip_atomic_store(flags + F_CLR, 1u, RLX_WG);
      unsigned cand[8];
#pragma unroll
      for (int i = 0; i < 8; ++i) cand[i] = __builtin_amdgcn_ds_bpermute((4 * i + g) * 4, nn);
      lds_wait_ne0(flags + F_QO);
      {
        const v2u q0 = *(LAS v2u*)(lds + L_QO + lane * 8), q1 = *(LAS v2u*)(lds + L_QO + 512 + lane * 8);
        __builtin_amdgcn_raw_buffer_store_b128(v4u{q0.x, nonce, q0.y, nonce}, wsr, qdo, 0, AUX_SC1);
        __builtin_amdgcn_raw_buffer_store_b128(v4u{q1.x, nonce, q1.y, nonce}, wsr, qdo, 4 * 8192, AUX_SC1);
      }
      const int qio = WS_QD + (kvh * 8 + b) * 8192 + lane * 16;
      v4u gq[8];
#pragma unroll
      for (int i = 0; i < 8; ++i) gq[i] = v4u{0u, ~cand[i], 0u, 0u};
      wait_until(tstart, QT1);
      for (unsigned spin = 0; spin < SPIN_CAP; ++spin) {
        CFENCE();
        const unsigned n2 = __builtin_amdgcn_raw_buffer_load_b32(wsr, nno, 0, AUX_SC1);
        if (lane < 32) em = __builtin_amdgcn_raw_buffer_load_b64(wsr, emo, 0, AUX_SC1);
#pragma unroll
        for (int i = 0; i < 8; ++i)
          if ((gq[i][1] != cand[i]) | (gq[i][3] != cand[i])) gq[i] = __builtin_amdgcn_raw_buffer_load_b128(wsr, qio, i * 1024, AUX_SC1);
        if (__any(n2 != nn)) {
          if (n2 != nn && lane < 32) __builtin_amdgcn_raw_buffer_store_b64(v2u{nonce, n2}, wsr, eco, 0, AUX_SC1);
          nn = n2;
#pragma unroll
          for (int i = 0; i < 8; ++i) cand[i] = __builtin_amdgcn_ds_bpermute((4 * i + g) * 4, nn);
        }
        bool ok = (lane >= 32) | ((em[1] == nonce) & (em[0] == nn));
#pragma unroll
        for (int i = 0; i < 8; ++i) ok = ok & (gq[i][1] == cand[i]) & (gq[i][3] == cand[i]);
        if (__all(ok)) break;
        __builtin_amdgcn_s_sleep(QSL);
      }
#pragma unroll
      for (int hf = 0; hf < 2; ++hf) {
        float q0 = 0.f, q1 = 0.f, q2 = 0.f, q3 = 0.f;
#pragma unroll
        for (int kq = 0; kq < 4; ++kq) {
          const v4u t4 = gq[2 * kq + hf];
          q0 += bflo(t4[0]); q1 += bfhi(t4[0]); q2 += bflo(t4[2]); q3 += bfhi(t4[2]);
        }
        *(LAS v2u*)(lds + L_Q + hf * 512 + lane * 8) = v2u{cvtpk(q0, q1), cvtpk(q2, q3)};
      }
      {
        const int df = (int)(nn - tstart);
        const bool use = lane >= 32 && df > -256 && df < 256;
        const float sd = wave_sum(use ? (float)df : 0.f), cn = wave_sum(use ? 1.f : 0.f);
        if (lane == 0) flags[F_TB] = tstart + (unsigned)(int)(sd * __builtin_amdgcn_rcpf(fmaxf(cn, 1.f)));
      }
      LGKM0();
      if (lane == 0) __hip_atomic_store(flags + F_Q, 1u, RLX_WG);
      const bool r2 = lane >= 32 && (fin || lane - 32 == fjb);
      if (r2) __builtin_amdgcn_raw_buffer_store_b64(v2u{nonce, nn}, wsr, eco, 0, AUX_SC1);
      for (unsigned spin = 0; spin < SPIN_CAP; ++spin) {
        __builtin_amdgcn_s_sleep(48);
        CFENCE();
        unsigned n2 = nn;
        if (r2) {
          n2 = __builtin_amdgcn_raw_buffer_load_b32(wsr, nno, 0, AUX_SC1);
          em = __builtin_amdgcn_raw_buffer_load_b64(wsr, emo, 0, AUX_SC1);
          if (n2 != nn) __builtin_amdgcn_raw_buffer_store_b64(v2u{nonce, n2}, wsr, eco, 0, AUX_SC1);
        }
        nn = n2;
        if (__all(!r2 | ((em[1] == nonce) & (em[0] == nn)))) break;
      }
      if (fin) {
        wait_until(tstart, TABT);
#pragma unroll 1
        for (int c = 0; c < 3; ++c) {
          for (unsigned spin = 0; spin < SPIN_CAP; ++spin) {
            CFENCE();
            v4u tv[8];
#pragma unroll
            for (int i = 0; i < 8; ++i) tv[i] = __builtin_amdgcn_raw_buffer_load_b128(wsr, WS_TAB + c * 8192 + lane * 16, i * 1024, AUX_SC1);
            bool ok = true;
#pragma unroll
            for (int i = 0; i < 8; ++i) {
              ok = ok & (tv[i][1] == 1u) & (tv[i][3] == 1u);
              *(LAS v2u*)(lds + L_T + c * 4096 + i * 512 + lane * 8) = v2u{tv[i][0], tv[i][2]};
            }
            if (__all(ok)) break;
            __builtin_amdgcn_s_sleep(16);
          }
        }
        LGKM0();
        if (lane == 0) __hip_atomic_store(flags + F_FIN, 1u, RLX_WG);
        asm volatile(".Lpf_end_a:");
      }
    } else if (wave == 2) {
      wait_until(tstart, PART);
      const v4u s1 = make_srd(w1, FF * 2), s2 = make_srd(b1, FF * 2), s3 = make_srd(w2, FF * 2);
#pragma unroll
      for (int i = 0; i < 3; ++i) {
        dma16<false>(lds0 + L_P + i * 1024, voff, s1, i * 1024);
        dma16<false>(lds0 + L_P + 3584 + i * 1024, voff, s2, i * 1024);
        dma16<false>(lds0 + L_P + 7168 + i * 1024, voff, s3, i * 1024);
      }
      if (lane < 32) {
        dma16<false>(lds0 + L_P + 3072, voff, s1, 3072);
        dma16<false>(lds0 + L_P + 3584 + 3072, voff, s2, 3072);
        dma16<false>(lds0 + L_P + 7168 + 3072, voff, s3, 3072);
      }
      const unsigned b2u = b2[0];
      if (lane == 0) flags[F_B2] = b2u;
      asm volatile("s_waitcnt vmcnt(0) lgkmcnt(0)" ::: "memory");
      if (lane == 0) __hip_atomic_store(flags + F_P, 1u, RLX_WG);
    }
    if (wave != 1) {
      __builtin_amdgcn_s_setprio(0);
      lds_wait_ne0(flags + F_P);
      lds_wait_ne0(flags + F_CLR);
      const int p0 = wave == 0 ? 0 : 2 * wave - 2;
#pragma unroll 1
      for (int ps = p0; ps < p0 + 2; ++ps) {
        const unsigned bits = (unsigned)((wgl & 127) * 24 + ps * 4 + (TAB_E0 << 7)) | (unsigned)((wgl >> 7) << 15);
        f32x2 t[2], a[2];
#pragma unroll
        for (int p = 0; p < 2; ++p) t[p] = f32x2{bflo(bits + 2 * p), bflo(bits + 2 * p + 1)};
        ffn_eval<2>(lds, lane, t, a);
        const unsigned k0 = cvtpk(wave_sum(a[0][0]), wave_sum(a[0][1])), k1 = cvtpk(wave_sum(a[1][0]), wave_sum(a[1][1]));
        __builtin_amdgcn_s_setprio(3);
        if (lane == 0) __builtin_amdgcn_raw_buffer_store_b128(v4u{k0, 1u, k1, 1u}, wsr, WS_TAB + wgl * 96 + ps * 16, 0, AUX_SC1);
        __builtin_amdgcn_s_setprio(0);
      }
      __builtin_amdgcn_s_setprio(3);
    }
    if (wave == 0) {
      while (__hip_atomic_load(flags + F_CNT, RLX_WG) != 8u) __builtin_amdgcn_s_sleep(2);
      CFENCE();
      const int hd = lane & 3;
      float M = -INFINITY, L = 0.f;
#pragma unroll
      for (int w = 0; w < 8; ++w) M = fmaxf(M, ((LAS float*)(lds + L_ML))[w * 8 + hd]);
#pragma unroll
      for (int w = 0; w < 8; ++w) L = fmaf(__builtin_amdgcn_exp2f(((LAS float*)(lds + L_ML))[w * 8 + hd] - M), ((LAS float*)(lds + L_ML))[w * 8 + 4 + hd], L);
      if (lane < 4) __builtin_amdgcn_raw_buffer_store_b128(v4u{__float_as_uint(M), 1u, __float_as_uint(L), 1u}, wsr, WS_STAT + wgl * 128 + lane * 16, 0, AUX_SC1);
      v4u sv = {0u, 1u, 0u, 1u};
      for (unsigned spin = 0; spin < SPIN_CAP; ++spin) {
        __builtin_amdgcn_s_sleep(8);
        CFENCE();
        if (lane < 16) sv = __builtin_amdgcn_raw_buffer_load_b128(wsr, WS_STAT + ((b << 5) | ((lane >> 2) << 3) | kvh) * 128 + hd * 16, 0, AUX_SC1);
        if (__all((sv[1] == 1u) & (sv[3] == 1u))) break;
      }
      const float ms = __uint_as_float(sv[0]);
      float Mg = fmaxf(ms, dpp<0x124>(ms)); Mg = fmaxf(Mg, dpp<0x128>(Mg));
      float Lg = __builtin_amdgcn_exp2f(ms - Mg) * __uint_as_float(sv[2]);
      Lg += dpp<0x128>(Lg); Lg += dpp<0x124>(Lg);
      if (lane < 4) ((LAS float*)(lds + L_Z))[lane] = Mg + __builtin_amdgcn_logf(Lg);
      LGKM0();
      if (lane == 0) __hip_atomic_store(flags + F_Z, 1u, RLX_WG);
    }
  } else {
    const int sw = wave - 4, tid = threadIdx.x - 256;
    const __amdgpu_buffer_rsrc_t vrs = __builtin_amdgcn_make_buffer_rsrc((void*)vc, 0, 64 << 20, 0x00020000);
    const int vvo = kvo + voff;
    const v4u wsd = make_srd(wo, HQ * DH * DM * 2);
    const int wo_ = (kvh * 512 + sw + ((lane >> 2) & 3) * 128 + (lane >> 4) * 8) * 1024 + cj * 64 + (lane & 3) * 16;
#define WPIECE(k) do { dma16<true>(lds0 + L_W + sw * 4096 + (k) * 1024, wo_, wsd, (k) * 32 * 1024); SB(); } while (0)
#define VLOAD(i) do { WAITV(VW - 1); pace<sched(32 + (i))>(tb); vf[i] = __builtin_amdgcn_raw_buffer_load_b128(vrs, vvo, (i) * 8192, AUX_NT); SB(); \
    if (32 + (i) == WP0) WPIECE(0); if (32 + (i) == WP1) WPIECE(1); if (32 + (i) == WP2) WPIECE(2); if (32 + (i) == WP3) WPIECE(3); } while (0)
    const __amdgpu_buffer_rsrc_t krs = __builtin_amdgcn_make_buffer_rsrc((void*)kc, 0, 64 << 20, 0x00020000);
    v4u kf[32], vf[32];
    __builtin_amdgcn_s_setprio(2);
    unsigned tb = tstart + (STG * sw >> 4);
    static_for<KFIRST, 32>([&](auto ic) {
      constexpr int i = ic.value;
      WAITV(KW - 1);
      pace<sched(i)>(tb);
      if (i == XWSIG) { WAITV(XWSIG - KFIRST); if (lane == 0) __hip_atomic_fetch_add(flags + F_XW, 256u, RLX_WG); }
      KLOAD(i);
      if (i == WP0) WPIECE(0);
      if (i == WP1) WPIECE(1);
    });
    static_for<0, XV>([&](auto ic) { VLOAD(ic.value); });
    lds_wait_ne0(flags + F_Q);
    const unsigned tb0 = __builtin_amdgcn_readfirstlane(flags[F_TB]);
    tb = tb0 + TBO + (STG * sw >> 4);
    SB();
    const v4u qf = *(LAS v4u*)(lds + L_Q + (r & 3) * 256 + g * 64 + (r >> 2) * 16);
    kf[0] = *(LAS v4u*)(lds + L_K0 + sw * 2048 + voff);
    kf[1] = *(LAS v4u*)(lds + L_K0 + sw * 2048 + 1024 + voff);
    float s[32];
    static_for<0, 32>([&](auto ic) {
      constexpr int i = ic.value;
      const f32x4 a = mfma16(kf[i], qf, f32x4{0.f, 0.f, 0.f, 0.f});
      float t_ = a[0] + dpp<0x104>(a[1]); asm volatile("" : "+v"(t_));
      t_ += dpp<0x108>(a[2]); asm volatile("" : "+v"(t_));
      s[i] = t_ + dpp<0x10C>(a[3]); asm volatile("" : "+v"(s[i]));
      if constexpr ((i & 3) == 3) { SB(); VLOAD(XV + (i >> 2)); }
    });
    SB();
    float m = s[0];
#pragma unroll
    for (int i = 1; i < 32; ++i) m = fmaxf(m, s[i]);
    m = xrow_max(m) * SCALE_L2;
    asm volatile("" : "+v"(m));
    SB(); VLOAD(XV + 8);
    v4u pf[4];
#define SOFTMAX(G, mm) do { float p_[8]; \
    _Pragma("unroll") for (int j = 0; j < 8; ++j) p_[j] = __builtin_amdgcn_exp2f(fmaf(s[8 * (G) + j], SCALE_L2, -(mm))); \
    pf[G] = v4u{cvtpk(p_[0], p_[1]), cvtpk(p_[2], p_[3]), cvtpk(p_[4], p_[5]), cvtpk(p_[6], p_[7])}; } while (0)
    {
      SOFTMAX(0, m); SOFTMAX(1, m);
      asm volatile("" : "+v"(pf[0]), "+v"(pf[1]));
      SB(); VLOAD(XV + 9);
      SOFTMAX(2, m); SOFTMAX(3, m);
      asm volatile("" : "+v"(pf[2]), "+v"(pf[3]));
      SB(); VLOAD(XV + 10);
      const v4u ones = {0x3f803f80u, 0x3f803f80u, 0x3f803f80u, 0x3f803f80u};
      f32x4 l4 = {0.f, 0.f, 0.f, 0.f};
#pragma unroll
      for (int G = 0; G < 4; ++G) l4 = mfma16(pf[G], ones, l4);
      if (lane < 4) ((LAS float*)(lds + L_ML))[sw * 8 + lane] = m;
      if (lane == 0) *(LAS f32x4*)(lds + L_ML + sw * 32 + 16) = l4;
      LGKM0();
      if (lane == 0) __hip_atomic_fetch_add(flags + F_CNT, 1u, RLX_WG);
    }
    f32x4 acc[8];
#pragma unroll
    for (int j = 0; j < 8; ++j) acc[j] = f32x4{0.f, 0.f, 0.f, 0.f};
#define PV(G) do { \
    _Pragma("unroll") for (int d = 0; d < 4; ++d) { v4u lo_, hi_; \
      _Pragma("unroll") for (int jj = 0; jj < 4; ++jj) { \
        lo_[jj] = __builtin_amdgcn_perm(vf[8 * (G) + 2 * jj + 1][d], vf[8 * (G) + 2 * jj][d], 0x05040100u); \
        hi_[jj] = __builtin_amdgcn_perm(vf[8 * (G) + 2 * jj + 1][d], vf[8 * (G) + 2 * jj][d], 0x07060302u); } \
      acc[2 * d] = mfma16(pf[G], lo_, acc[2 * d]); acc[2 * d + 1] = mfma16(pf[G], hi_, acc[2 * d + 1]); } SB(); } while (0)
    static_for<XV + 11, 32>([&](auto ic) {
      constexpr int i = ic.value;
      VLOAD(i);
      if constexpr (i == XV + 11) PV(0);
      if constexpr (i == 15 + PVD) PV(1);
      if constexpr (i == 23 + PVD) PV(2);
    });
    v4u wf[2][2];
    {
      const int wtr = L_W + sw * 4096 + (g * 8 + (r >> 2)) * 64 + (r & 3) * 8;
#pragma unroll
      for (int ks = 0; ks < 2; ++ks)
#pragma unroll
        for (int nt = 0; nt < 2; ++nt) {
          const v2u u0 = __builtin_bit_cast(v2u, __builtin_amdgcn_ds_read_tr16_b64_v4i16((LAS v4s*)(lds + wtr + ks * 2048 + nt * 32)));
          const v2u u1 = __builtin_bit_cast(v2u, __builtin_amdgcn_ds_read_tr16_b64_v4i16((LAS v4s*)(lds + wtr + ks * 2048 + 256 + nt * 32)));
          wf[ks][nt] = v4u{u0.x, u0.y, u1.x, u1.y};
          asm volatile("" : "+v"(wf[ks][nt]));
        }
      SB();
    }
    unsigned a_mw = lds0 + L_B + sw * 2048 + voff, a_mr = lds0 + L_B + sw * 256 + voff;
    unsigned a_own = lds0 + L_A + sw * 2304 + me * 144 + lane * 8, a_put = lds0 + L_A + sw * 2304 + (lane >> 3) * 144 + (lane & 7) * 16;
    unsigned a_get = lds0 + L_A + sw * 2304 + r * 144 + g * 16;
    unsigned a_rw = lds0 + L_W + sw * 512 + lane * 4, a_rr = lds0 + L_W + ((g & 1) * 64 + (2 * sw + (g >> 1)) * 16 + r) * 4;
    int o_part = partg + me * 1024 + sw * 128 + lane * 8, o_get = partg + (lane >> 3) * 1024 + sw * 128 + (lane & 7) * 16;
    int o_outp = WS_OUTP + (((bh * 4 + 2 * sw + (g >> 1)) * 8 + kvh) * 512 + cj * 32 + (lane & 31)) * 4, o_fin = WS_OUTP + b * 16384 + tid * 4;
    us* outp = out + b * DM + tid;
    float xres = 0.f, b2v = 0.f;
    if (fin) {
      lds_wait_ne0(flags + F_FIN);
      xres = bflo(((LAS us*)(lds + L_X))[b * 520 + tid]); b2v = bflo(flags[F_B2]);
    }
    asm volatile("" : "+v"(a_mw), "+v"(a_mr), "+v"(a_own), "+v"(a_put), "+v"(a_get), "+v"(a_rw), "+v"(a_rr));
    asm volatile("" : "+v"(o_part), "+v"(o_get), "+v"(o_outp), "+v"(o_fin), "+v"(outp), "+v"(xres), "+v"(b2v));
#define LDSP(T, a) ((LAS T*)(uintptr_t)(a))
    lds_wait_ne0(flags + F_Z);
    SB();
    {
      const f32x4 Z4 = *(LAS f32x4*)(lds + L_Z);
      const f32x4 mo = *(LAS f32x4*)(lds + L_ML + sw * 32);
      const float zl = ((LAS float*)(lds + L_Z))[lane & 3];
      f32x4 f4;
#pragma unroll
      for (int i = 0; i < 4; ++i) f4[i] = __builtin_amdgcn_exp2f(mo[i] - Z4[i]);
#pragma unroll
      for (int j = 0; j < 8; ++j) acc[j] *= f4;
      const float zz = (r < 4) ? zl : m;
      SOFTMAX(3, zz);
      SB();
    }
    {
      v4u lo_[4], hi_[4];
#pragma unroll
      for (int jj = 0; jj < 4; ++jj) {
#pragma unroll
        for (int d = 0; d < 4; ++d) {
          lo_[d][jj] = __builtin_amdgcn_perm(vf[24 + 2 * jj + 1][d], vf[24 + 2 * jj][d], 0x05040100u);
          hi_[d][jj] = __builtin_amdgcn_perm(vf[24 + 2 * jj + 1][d], vf[24 + 2 * jj][d], 0x07060302u);
        }
        SB();
      }
#pragma unroll
      for (int d = 0; d < 4; ++d) { acc[2 * d] = mfma16(pf[3], lo_[d], acc[2 * d]); acc[2 * d + 1] = mfma16(pf[3], hi_[d], acc[2 * d + 1]); }
      SB();
    }
    if (lane < 16) {
#pragma unroll
      for (int j = 0; j < 8; ++j) LDSP(f32x4, a_mw)[j * 16] = acc[j];
    }
    WG_BARRIER();
    if (lane < 16) {
      f32x4 ow[8];
#pragma unroll
      for (int w = 0; w < 8; ++w) ow[w] = LDSP(f32x4, a_mr)[w * 128];
      const f32x4 o = ((ow[0] + ow[1]) + (ow[2] + ow[3])) + ((ow[4] + ow[5]) + (ow[6] + ow[7]));
      const v2u op = {cvtpk(o[0], o[1]), min(cvtpk(o[2], o[3]), EMPTY - 1u)};
      __builtin_amdgcn_raw_buffer_store_b64(op, wsr, o_part, 0, AUX_SC1);
      *LDSP(v2u, a_own) = op;
    }
    SB();
    {
      bool ok0 = (lane >> 3) == me, ok1 = (lane >> 3) + 8 == me;
      pace<sched(63) + H2T, 1, 4>(tb0);
      for (unsigned spin = 0; spin < SPIN_CAP; ++spin) {
        CFENCE();
        const v4u p0 = __builtin_amdgcn_raw_buffer_load_b128(wsr, ok0 ? WS_END : o_get, 0, AUX_SC1);
        const v4u p1 = __builtin_amdgcn_raw_buffer_load_b128(wsr, ok1 ? WS_END : o_get + 8192, 0, AUX_SC1);
        if (!ok0) *LDSP(v4u, a_put) = p0;
        if (!ok1) *LDSP(v4u, a_put + 1152) = p1;
        ok0 = max(p0[1], p0[3]) != EMPTY;
        ok1 = max(p1[1], p1[3]) != EMPTY;
        if (__all(ok0 & ok1)) break;
      }
      f32x4 c0 = {0.f, 0.f, 0.f, 0.f}, c1 = {0.f, 0.f, 0.f, 0.f};
#pragma unroll
      for (int ks = 0; ks < 2; ++ks) {
        const v4u af = LDSP(v4u, a_get)[ks * 4];
        c0 = mfma16(af, wf[ks][0], c0);
        c1 = mfma16(af, wf[ks][1], c1);
      }
      LDSP(float, a_rw)[0] = (c0[0] + c0[1]) + (c0[2] + c0[3]);
      LDSP(float, a_rw)[64] = (c1[0] + c1[1]) + (c1[2] + c1[3]);
    }
    WG_BARRIER();
    if (sw < 2) {
      LAS float* red = LDSP(float, a_rr);
      const float a = ((red[0] + red[128]) + (red[256] + red[384])) + ((red[512] + red[640]) + (red[768] + red[896]));
      __builtin_amdgcn_raw_buffer_store_b32(min(__float_as_uint(a), EMPTY - 1u), wsr, o_outp, 0, AUX_SC1);
    }
    if (fin) {
      unsigned pv[8];
      if (sw >= 2) __builtin_amdgcn_s_sleep(H3E);
      __builtin_amdgcn_s_sleep(H3D);
      for (unsigned spin = 0; spin < SPIN_CAP; ++spin) {
        CFENCE();
#pragma unroll
        for (int k = 0; k < 8; ++k) pv[k] = __builtin_amdgcn_raw_buffer_load_b32(wsr, o_fin, k * 2048, AUX_SC1);
        const unsigned mx = max(max(max(pv[0], pv[1]), max(pv[2], pv[3])), max(max(pv[4], pv[5]), max(pv[6], pv[7])));
        if (__all(mx != EMPTY)) break;
      }
      const float a = ((__uint_as_float(pv[0]) + __uint_as_float(pv[1])) + (__uint_as_float(pv[2]) + __uint_as_float(pv[3]))) +
                      ((__uint_as_float(pv[4]) + __uint_as_float(pv[5])) + (__uint_as_float(pv[6]) + __uint_as_float(pv[7])));
      const float h = rbf(xres + rbf(a));
      float s1 = h, s2 = h * h;
      wave_sum2(s1, s2);
      if ((lane & 47) == 0) ((LAS float*)(lds + L_B))[sw * 2 + g] = s1;
      WG_BARRIER();
      const f32x4 q0 = *(LAS f32x4*)(lds + L_B), q1 = *(LAS f32x4*)(lds + L_B + 16), q2 = *(LAS f32x4*)(lds + L_B + 32), q3 = *(LAS f32x4*)(lds + L_B + 48);
      const f32x4 qs = (q0 + q1) + (q2 + q3);
      const float mean = (qs[0] + qs[2]) * (1.f / DM);
      const float var = fmaxf((qs[1] + qs[3]) * (1.f / DM) - mean * mean, 0.f);
      const float r1 = __builtin_amdgcn_rsqf(var + 1e-5f);
      const float r2 = __builtin_amdgcn_rsqf(fmaf(var * r1, r1, 1e-5f));
      const float t = ((h - mean) * r1) * r2;
      const unsigned tb = cvtpk(t, t) & 0xffffu;
      const unsigned rel = (tb & 0x7fffu) - (unsigned)(TAB_E0 << 7);
      const bool inr = rel < (unsigned)TAB_H;
      float y = bflo(((LAS us*)(lds + L_T))[(inr ? rel : 0u) + (tb >> 15) * TAB_H]);
      u64 miss = __ballot(!inr);
      while (miss) {
        const int l = __builtin_ctzll(miss);
        miss &= miss - 1;
        const float ts = bflo((unsigned)__builtin_amdgcn_readlane((int)tb, l));
        const f32x2 tt[1] = {f32x2{ts, ts}};
        f32x2 aa[1];
        ffn_eval<1>(lds, lane, tt, aa);
        const float ys = rbf(wave_sum(aa[0][0]));
        if (lane == l) y = ys;
      }
      const float ov = h + rbf(y + b2v);
      *(GAS us*)outp = (us)(cvtpk(ov, ov) & 0xffffu);
    }
  }
  asm volatile(".Lpf_end_b:");
}

extern "C" void kernel_launch(void* const* d_in, const int* in_sizes, int n_in, void* d_out, int out_size, void* d_ws, size_t ws_size, hipStream_t stream) {
  fused_kernel<<<256, 768, 0, stream>>>((const us*)d_in[0], (const us*)d_in[1], (const us*)d_in[2], (const us*)d_in[3], (const us*)d_in[4],
                                        (const us*)d_in[6], (const us*)d_in[7], (const us*)d_in[8], (const us*)d_in[9], (us*)d_out, (unsigned char*)d_ws);
}
```

```cpp
#include <hip/hip_runtime.h>
#include <hip/hip_bf16.h>
#include <stdint.h>
#include <type_traits>

#define GAS __attribute__((address_space(1)))
#define LAS __attribute__((address_space(3)))
#define RLX_WG __ATOMIC_RELAXED, __HIP_MEMORY_SCOPE_WORKGROUP
typedef unsigned v4u __attribute__((ext_vector_type(4)));
typedef unsigned v2u __attribute__((ext_vector_type(2)));
typedef float f32x4 __attribute__((ext_vector_type(4)));
typedef float f32x2 __attribute__((ext_vector_type(2)));
typedef short bf16x8 __attribute__((ext_vector_type(8)));
typedef short v4s __attribute__((ext_vector_type(4)));
typedef __bf16 bf16x2_t __attribute__((ext_vector_type(2)));
typedef unsigned long long u64;
typedef unsigned short us;

constexpr int DM = 512, HQ = 32, DH = 128, FF = 1792;
constexpr float LOG2E = 1.4426950408889634f;
constexpr float SCALE_L2 = 0.08838834764831845f * LOG2E;
constexpr unsigned SPIN_CAP = 100000u;
constexpr int AUX_NT = 2, AUX_SC1 = 16;
constexpr unsigned EMPTY = 0xffffffffu;
constexpr int TAB_E0 = 108;
constexpr int TAB_H = 24 * 128;

constexpr int CODEPF = 64;
constexpr int KFIRST = 2;
constexpr int PER16 = 478;
constexpr int LEAD = 2;
constexpr int NE = 24;
constexpr int EX16 = 132;
constexpr int WC16 = 260;
constexpr int XWSIG = 5;
constexpr int PVD = 5;
constexpr int XV = 2;
constexpr int PSL = 2;
constexpr int PSLT = 7;
constexpr int NNT = 130;
constexpr int KW = 4;
constexpr int VW = 6;
constexpr int PART = 250;
constexpr int QT1 = 480;
constexpr int QSL = 8;
constexpr int TBO = -7;
constexpr int STG = 30;
constexpr int WP0 = 15;
constexpr int WP1 = 23;
constexpr int WP2 = 35;
constexpr int WP3 = 43;
constexpr int H3D = 4;
constexpr int H3E = 3;
constexpr int H2T = 152;
constexpr int HOTPF = 200;
constexpr int TABT = 1200;
constexpr int sched(int n) {
  return ((n - LEAD) * PER16 + (n < NE ? n : NE) * EX16 + ((n > WP0) + (n > WP1) + (n > WP2) + (n > WP3)) * WC16) >> 4;
}

constexpr int WS_NK = 0;
constexpr int WS_NB = 1024;
constexpr int WS_STAT = 4096;
constexpr int WS_TAB = WS_STAT + 256 * 128;
constexpr int WS_EM = WS_TAB + 256 * 128;
constexpr int WS_PART = WS_EM + 256 * 512;
constexpr int WS_OUTP = WS_PART + 256 * 1024;
constexpr int WS_QD = WS_OUTP + 64 * 2048;
constexpr int WS_END = WS_QD + 64 * 8192;

constexpr int L_W = 0;
constexpr int L_B = 32768;
constexpr int L_X = L_B + 16384;
constexpr int L_P = L_X + 9472;
constexpr int L_T = L_P + 10752;
constexpr int L_Q = L_T + 16384;
constexpr int L_QO = L_Q + 1024;
constexpr int L_ML = L_QO + 1024;
constexpr int L_Z = L_ML + 256;
constexpr int L_FL = L_Z + 64;
constexpr int L_A = L_FL + 64;
constexpr int L_K0 = L_A + 8 * 2304;
constexpr int L_TOTAL = L_K0 + 8 * 2048;
enum { F_Q = 0, F_CNT = 1, F_QO = 2, F_Z = 3, F_P = 4, F_FIN = 5, F_XW = 6, F_B2 = 7, F_CLR = 8, F_TB = 9 };

__device__ __forceinline__ f32x4 mfma16(v4u a, v4u b, f32x4 c) {
  return __builtin_amdgcn_mfma_f32_16x16x32_bf16(__builtin_bit_cast(bf16x8, a), __builtin_bit_cast(bf16x8, b), c, 0, 0, 0);
}
__device__ __forceinline__ unsigned cvtpk(float lo, float hi) {
  return __builtin_bit_cast(unsigned, __builtin_convertvector(f32x2{lo, hi}, bf16x2_t));
}
__device__ __forceinline__ float bflo(unsigned u) { return __builtin_bit_cast(float, u << 16); }
__device__ __forceinline__ float bfhi(unsigned u) { return __builtin_bit_cast(float, u & 0xffff0000u); }
__device__ __forceinline__ float rbf(float x) { return bflo(cvtpk(x, x)); }
__device__ __forceinline__ float xrow_max(float x) {
  auto s = __builtin_amdgcn_permlane16_swap(__float_as_uint(x), __float_as_uint(x), false, false);
  x = fmaxf(__uint_as_float(s[0]), __uint_as_float(s[1]));
  auto t = __builtin_amdgcn_permlane32_swap(__float_as_uint(x), __float_as_uint(x), false, false);
  return fmaxf(__uint_as_float(t[0]), __uint_as_float(t[1]));
}
__device__ __forceinline__ float xrow_sum(float x) {
  auto s = __builtin_amdgcn_permlane16_swap(__float_as_uint(x), __float_as_uint(x), false, false);
  x = __uint_as_float(s[0]) + __uint_as_float(s[1]);
  auto t = __builtin_amdgcn_permlane32_swap(__float_as_uint(x), __float_as_uint(x), false, false);
  return __uint_as_float(t[0]) + __uint_as_float(t[1]);
}
template <int CTRL> __device__ __forceinline__ float dpp(float x) {
  return __builtin_bit_cast(float, __builtin_amdgcn_mov_dpp(__builtin_bit_cast(int, x), CTRL, 0xf, 0xf, true));
}
__device__ __forceinline__ float wave_sum(float x) {
  x += dpp<0xB1>(x); x += dpp<0x4E>(x); x += dpp<0x141>(x); x += dpp<0x128>(x);
  return xrow_sum(x);
}
__device__ __forceinline__ void wave_sum2(float& x, float& y) {
  x += dpp<0xB1>(x); y += dpp<0xB1>(y); __builtin_amdgcn_sched_barrier(0);
  x += dpp<0x4E>(x); y += dpp<0x4E>(y); __builtin_amdgcn_sched_barrier(0);
  x += dpp<0x141>(x); y += dpp<0x141>(y); __builtin_amdgcn_sched_barrier(0);
  x += dpp<0x128>(x); y += dpp<0x128>(y); __builtin_amdgcn_sched_barrier(0);
  auto s = __builtin_amdgcn_permlane16_swap(__float_as_uint(x), __float_as_uint(y), false, false);
  const float z = __uint_as_float(s[0]) + __uint_as_float(s[1]);
  auto t = __builtin_amdgcn_permlane32_swap(__float_as_uint(z), __float_as_uint(z), false, false);
  x = __uint_as_float(t[0]) + __uint_as_float(t[1]);
  y = x;
}
#define SB() __builtin_amdgcn_sched_barrier(0)
#define CFENCE() asm volatile("" ::: "memory")
#define LGKM0() asm volatile("s_waitcnt lgkmcnt(0)" ::: "memory")
#define WAITV(n) asm volatile("s_waitcnt vmcnt(%0)" :: "i"(n) : "memory")
#define WG_BARRIER() do { asm volatile("s_waitcnt lgkmcnt(0)\n\ts_barrier" ::: "memory"); SB(); } while (0)
__device__ __forceinline__ v4u make_srd(const void* p, unsigned bytes) {
  const u64 a = (u64)p;
  v4u d;
  d[0] = __builtin_amdgcn_readfirstlane((unsigned)a);
  d[1] = __builtin_amdgcn_readfirstlane((unsigned)(a >> 32)) & 0xffffu;
  d[2] = bytes;
  d[3] = 0x00020000u;
  return d;
}
template <bool NT>
__device__ __forceinline__ void dma16(unsigned ldsaddr, int voff, v4u srd, int soff) {
  if (NT) asm volatile("s_mov_b32 m0, %0\n\ts_nop 0\n\tbuffer_load_dwordx4 %1, %2, %3 offen nt lds" :: "s"(ldsaddr), "v"(voff), "s"(srd), "s"(soff) : "memory");
  else asm volatile("s_mov_b32 m0, %0\n\ts_nop 0\n\tbuffer_load_dwordx4 %1, %2, %3 offen lds" :: "s"(ldsaddr), "v"(voff), "s"(srd), "s"(soff) : "memory");
}
__device__ __forceinline__ void lds_wait_ne0(LAS unsigned* f) {
  while (__hip_atomic_load(f, RLX_WG) == 0u) __builtin_amdgcn_s_sleep(1);
  CFENCE();
}
template <int TICKS, int SL = PSL, int SLT = PSLT>
__device__ __forceinline__ void pace(unsigned tstart) {
  if (TICKS > 0)
    asm volatile("s_memrealtime vcc\n\ts_waitcnt lgkmcnt(0)\n\ts_sub_i32 vcc_lo, vcc_lo, %0\n"
                 "1:\n\ts_cmpk_ge_i32 vcc_lo, %1\n\ts_cbranch_scc1 2f\n\ts_sleep %2\n\ts_addk_i32 vcc_lo, %3\n\ts_branch 1b\n2:"
                 :: "s"(tstart), "i"(TICKS), "i"(SL), "i"(SLT) : "vcc", "scc", "memory");
}

__device__ __forceinline__ void wait_until(unsigned t0, int ticks) {
  while ((int)((unsigned)__builtin_amdgcn_s_memrealtime() - t0) < ticks) __builtin_amdgcn_s_sleep(3);
  CFENCE();
}

template <int NP>
__device__ __forceinline__ void ffn_eval(LAS unsigned char* lds, int lane, const f32x2 (&t)[NP], f32x2 (&acc)[NP]) {
  constexpr float C0 = -2.f * 0.7978845608028654f * LOG2E, C1 = C0 * 0.044715f;
#pragma unroll
  for (int p = 0; p < NP; ++p) acc[p] = f32x2{0.f, 0.f};
#pragma unroll 1
  for (int j = 0; j < 7; ++j) {
    const v2u ua = *(LAS v2u*)(lds + L_P + j * 512 + lane * 8);
    const v2u ub = *(LAS v2u*)(lds + L_P + 3584 + j * 512 + lane * 8);
    const v2u uw = *(LAS v2u*)(lds + L_P + 7168 + j * 512 + lane * 8);
#pragma unroll
    for (int i = 0; i < 4; ++i) {
      const float a = (i & 1) ? bfhi(ua[i >> 1]) : bflo(ua[i >> 1]);
      const float b = (i & 1) ? bfhi(ub[i >> 1]) : bflo(ub[i >> 1]);
      const float w = (i & 1) ? bfhi(uw[i >> 1]) : bflo(uw[i >> 1]);
#pragma unroll
      for (int p = 0; p < NP; ++p) {
        const f32x2 u = __builtin_elementwise_fma(t[p], f32x2{a, a}, f32x2{b, b});
        const f32x2 y = u * __builtin_elementwise_fma(u * u, f32x2{C1, C1}, f32x2{C0, C0});
        f32x2 d = f32x2{__builtin_amdgcn_exp2f(y[0]), __builtin_amdgcn_exp2f(y[1])} + 1.f;
        d = f32x2{__builtin_amdgcn_rcpf(d[0]), __builtin_amdgcn_rcpf(d[1])};
        acc[p] = __builtin_elementwise_fma(u * d, f32x2{w, w}, acc[p]);
      }
    }
  }
}


template <int I, int N, class F> __device__ __forceinline__ void static_for(F&& f) {
  if constexpr (I < N) { f(std::integral_constant<int, I>{}); static_for<I + 1, N>(f); }
}

__global__ __launch_bounds__(768, 3) void fused_kernel(const us* __restrict__ x, const us* __restrict__ kc, const us* __restrict__ vc,
                                                       const us* __restrict__ wq, const us* __restrict__ wo, const us* __restrict__ w1,
                                                       const us* __restrict__ b1, const us* __restrict__ w2, const us* __restrict__ b2,
                                                       us* __restrict__ out, unsigned char* ws) {
  __shared__ __attribute__((aligned(16))) unsigned char smem[L_TOTAL];
  LAS unsigned char* lds = (LAS unsigned char*)smem;
  const unsigned lds0 = (unsigned)(uintptr_t)lds;
  const int lane = threadIdx.x & 63, wave = __builtin_amdgcn_readfirstlane(threadIdx.x >> 6), g = lane >> 4, r = lane & 15;
  LAS unsigned* flags = (LAS unsigned*)(lds + L_FL);
  asm volatile(".Lpf_begin:");
  const unsigned tstart = (unsigned)__builtin_amdgcn_s_memrealtime();
  asm volatile("" :: "s"(x), "s"(kc), "s"(vc), "s"(wq), "s"(ws));
  const int bid = blockIdx.x, sp = (bid >> 3) & 3, b = ((bid >> 3) & 4) | (bid >> 6), kvh = (bid + 3 * (bid >> 6)) & 7;
  const int voff = lane * 16;
  const int kvo = (((b * 8 + kvh) * 4 + sp) << 18) + ((wave - 4) << 10);
  const int koff = kvo + (r >> 2) * 256 + g * 64 + (r & 3) * 16;
#define KLOAD(i) do { kf[i] = __builtin_amdgcn_raw_buffer_load_b128(krs, koff, (i) * 8192, AUX_NT); SB(); } while (0)
  const __amdgpu_buffer_rsrc_t wsr = __builtin_amdgcn_make_buffer_rsrc((void*)ws, 0, WS_END, 0x00020000);
  const int jk = sp * 8 + b;
  const int jb = sp * 8 + kvh;
  if (wave < 4) {
    const v4u srd = make_srd(x, 8 * DM * 2);
    dma16<false>(lds0 + L_X + wave * 2080, voff, srd, wave * 2048);
    dma16<false>(lds0 + L_X + wave * 2080 + 1040, voff, srd, wave * 2048 + 1024);
    if (wave == 1 && lane < 2)
      __builtin_amdgcn_raw_buffer_store_b32(tstart, wsr, lane ? WS_NB + b * 128 + jb * 4 : WS_NK + kvh * 128 + jk * 4, 0, AUX_SC1);
    if (bid < CODEPF && wave == 3) {
      u64 lo; unsigned back, la, lb;
      asm volatile("s_getpc_b64 %0\n.Lpf_here:\n\ts_mov_b32 %1, .Lpf_here-.Lpf_begin\n\ts_mov_b32 %2, .Lpf_end_a-.Lpf_begin\n\ts_mov_b32 %3, .Lpf_end_b-.Lpf_begin"
                   : "=s"(lo), "=s"(back), "=s"(la), "=s"(lb));
      const v4u cs = make_srd((const void*)(lo - back), la > lb ? la : lb);
      if (lane < 24) asm volatile("s_mov_b32 m0, %0\n\ts_nop 0\n\tbuffer_load_dword %1, %2, 0 offen lds" :: "s"(lds0 + L_T), "v"((lane + 24 * ((bid >> 3) & 7)) * 128), "s"(cs) : "memory");
    }
    if (threadIdx.x < 16) flags[threadIdx.x] = 0u;
    SB();
  } else {
    const v4u ksd = make_srd(kc, 64 << 20);
    dma16<true>(lds0 + L_K0 + (wave - 4) * 2048, koff, ksd, 0);
    dma16<true>(lds0 + L_K0 + (wave - 4) * 2048 + 1024, koff, ksd, 8192);
    const v4u srd = make_srd(wq, DM * HQ * DH * 2);
    const int vo = (sp * 128 + (wave - 4) * 16 + (lane >> 3)) * 8192 + kvh * 1024 + b * 128 + (lane & 7) * 16;
    dma16<true>(lds0 + L_B + (wave - 4) * 2048, vo, srd, 0);
    dma16<true>(lds0 + L_B + (wave - 4) * 2048 + 1024, vo, srd, 8 * 8192);
    SB();
  }
  WG_BARRIER();
  const int wgl = (b << 5) | jb;
  const int cj = (b & 3) * 4 + sp, bh = b >> 2;
  const int partg = WS_PART + (kvh * 2 + bh) * 16384;
  const int me = (b & 3) * 4 + sp;
  const int fjb = (b + 3 * (b & 3)) & 7;
  const bool fin = jb == fjb;

  if (wave < 4) {
    __builtin_amdgcn_s_setprio(3);
    if (wave != 1) {
      WAITV(0);
      if (lane == 0) __hip_atomic_fetch_add(flags + F_XW, 1u, RLX_WG);
    }
    if (wave == 0) {
      *(LAS v4u*)(lds + L_X + 8 * 1040 + lane * 16) = v4u{0x3f803f80u, 0x3f803f80u, 0x3f803f80u, 0x3f803f80u};
      while ((__hip_atomic_load(flags + F_XW, RLX_WG) & 255u) != 4u) __builtin_amdgcn_s_sleep(1);
      CFENCE();
      const int xo = L_X + (r < 8 ? r : 8) * 1040 + g * 16;
      f32x4 d2a = {0.f, 0.f, 0.f, 0.f}, d2b = {0.f, 0.f, 0.f, 0.f};
#pragma unroll
      for (int s = 0; s < 16; s += 2) {
        const v4u xa = *(LAS v4u*)(lds + xo + s * 64), xb = *(LAS v4u*)(lds + xo + s * 64 + 64);
        d2a = mfma16(xa, xa, d2a); d2b = mfma16(xb, xb, d2b);
      }
      d2a += d2b;
      const float dg = (r & 2) ? ((r & 1) ? d2a[3] : d2a[2]) : ((r & 1) ? d2a[1] : d2a[0]);
      const float sxx = xrow_sum(g == (r >> 2) ? dg : 0.f);
      const float mean = xrow_sum(g == 2 ? d2a[0] : 0.f) * (1.f / DM);
      float rstd = rsqrtf(fmaxf(sxx * (1.f / DM) - mean * mean, 0.f) + 1e-5f);
      v4u xb[4];
#pragma unroll
      for (int s = 0; s < 4; ++s) { xb[s] = *(LAS v4u*)(lds + xo + sp * 256 + s * 64); asm volatile("" : "+v"(xb[s])); }
      asm volatile("" : "+v"(rstd));
      while (__hip_atomic_load(flags + F_XW, RLX_WG) != 4u + 8u * 256u) __builtin_amdgcn_s_sleep(1);
      CFENCE();
      f32x4 d[4];
#pragma unroll
      for (int nt = 0; nt < 4; ++nt) d[nt] = f32x4{0.f, 0.f, 0.f, 0.f};
      const int troff = L_B + (8 * g + (r >> 2)) * 128 + (r & 3) * 8;
#pragma unroll
      for (int s = 0; s < 4; ++s)
#pragma unroll
        for (int nt = 0; nt < 4; ++nt) {
          const v2u u0 = __builtin_bit_cast(v2u, __builtin_amdgcn_ds_read_tr16_b64_v4i16((LAS v4s*)(lds + troff + s * 4096 + nt * 32)));
          const v2u u1 = __builtin_bit_cast(v2u, __builtin_amdgcn_ds_read_tr16_b64_v4i16((LAS v4s*)(lds + troff + s * 4096 + 512 + nt * 32)));
          d[nt] = mfma16(v4u{u0.x, u0.y, u1.x, u1.y}, xb[s], d[nt]);
        }
#pragma unroll
      for (int nt = 0; nt < 4; ++nt) {
        float qv[4];
#pragma unroll
        for (int i = 0; i < 4; ++i) qv[i] = (d[nt][i] - mean * dpp<0x158>(d[nt][i])) * rstd;
        if (r < 8) *(LAS v2u*)(lds + L_QO + r * 128 + nt * 32 + g * 8) = v2u{cvtpk(qv[0], qv[1]), cvtpk(qv[2], qv[3])};
      }
      LGKM0();
      if (lane == 0) __hip_atomic_store(flags + F_QO, 1u, RLX_WG);
    } else if (wave == 1) {
      const int emo = WS_EM + wgl * 512 + lane * 8;
      const int qdo = WS_QD + (kvh * 8 + g) * 8192 + jk * 256 + r * 16;
      const int nno = lane < 32 ? WS_NK + kvh * 128 + lane * 4 : WS_NB + b * 128 + (lane - 32) * 4;
      const int eco = WS_EM + (lane < 32 ? ((((lane & 7) << 5) | (lane & 24) | kvh) * 64 + jk) : (((b << 5) | (lane - 32)) * 64 + 32 + jb)) * 8;
      const v4u e4 = {EMPTY, EMPTY, EMPTY, EMPTY};
      __builtin_amdgcn_raw_buffer_store_b128(e4, wsr, partg + me * 1024 + lane * 16, 0, AUX_SC1);
      if (lane < 42)
        __builtin_amdgcn_raw_buffer_store_b128(e4, wsr, (lane < 32 ? WS_OUTP + (b * 8 + kvh) * 2048 + sp * 512 : lane < 36 ? WS_STAT - 512 + wgl * 128 : WS_TAB - 576 + wgl * 96) + lane * 16, 0, AUX_SC1);
      SB();
      v2u em = __builtin_amdgcn_raw_buffer_load_b64(wsr, emo, 0, AUX_SC1);
      v4u s0 = __builtin_amdgcn_raw_buffer_load_b128(wsr, qdo, 0, AUX_SC1);
      v4u s1 = __builtin_amdgcn_raw_buffer_load_b128(wsr, qdo, 4 * 8192, AUX_SC1);
      SB();
      WAITV(6);
      if (lane == 0) __hip_atomic_fetch_add(flags + F_XW, 1u, RLX_WG);
      wait_until(tstart, NNT);
      unsigned nn = __builtin_amdgcn_raw_buffer_load_b32(wsr, nno, 0, AUX_SC1);
      asm volatile("s_waitcnt vmcnt(0)" : "+v"(em), "+v"(s0), "+v"(s1), "+v"(nn) :: "memory");
      unsigned nonce = tstart;
      while (__any((em[1] == nonce) | (s0[1] == nonce) | (s0[3] == nonce) | (s1[1] == nonce) | (s1[3] == nonce))) ++nonce;
      if (nonce != tstart) {
        if (lane < 2) __builtin_amdgcn_raw_buffer_store_b32(nonce, wsr, lane ? WS_NB + b * 128 + jb * 4 : WS_NK + kvh * 128 + jk * 4, 0, AUX_SC1);
        WAITV(0);
      }
      if (lane < 32) __builtin_amdgcn_raw_buffer_store_b64(v2u{nonce, nn}, wsr, eco, 0, AUX_SC1);
      if (lane == 0) __hip_atomic_store(flags + F_CLR, 1u, RLX_WG);
      unsigned cand[8];
#pragma unroll
      for (int i = 0; i < 8; ++i) cand[i] = __builtin_amdgcn_ds_bpermute((4 * i + g) * 4, nn);
      lds_wait_ne0(flags + F_QO);
      {
        const v2u q0 = *(LAS v2u*)(lds + L_QO + lane * 8), q1 = *(LAS v2u*)(lds + L_QO + 512 + lane * 8);
        __builtin_amdgcn_raw_buffer_store_b128(v4u{q0.x, nonce, q0.y, nonce}, wsr, qdo, 0, AUX_SC1);
        __builtin_amdgcn_raw_buffer_store_b128(v4u{q1.x, nonce, q1.y, nonce}, wsr, qdo, 4 * 8192, AUX_SC1);
      }
      const int qio = WS_QD + (kvh * 8 + b) * 8192 + lane * 16;
      v4u gq[8];
#pragma unroll
      for (int i = 0; i < 8; ++i) gq[i] = v4u{0u, ~cand[i], 0u, 0u};
      wait_until(tstart, QT1);
      for (unsigned spin = 0; spin < SPIN_CAP; ++spin) {
        CFENCE();
        const unsigned n2 = __builtin_amdgcn_raw_buffer_load_b32(wsr, nno, 0, AUX_SC1);
        if (lane < 32) em = __builtin_amdgcn_raw_buffer_load_b64(wsr, emo, 0, AUX_SC1);
#pragma unroll
        for (int i = 0; i < 8; ++i)
          if ((gq[i][1] != cand[i]) | (gq[i][3] != cand[i])) gq[i] = __builtin_amdgcn_raw_buffer_load_b128(wsr, qio, i * 1024, AUX_SC1);
        if (__any(n2 != nn)) {
          if (n2 != nn && lane < 32) __builtin_amdgcn_raw_buffer_store_b64(v2u{nonce, n2}, wsr, eco, 0, AUX_SC1);
          nn = n2;
#pragma unroll
          for (int i = 0; i < 8; ++i) cand[i] = __builtin_amdgcn_ds_bpermute((4 * i + g) * 4, nn);
        }
        bool ok = (lane >= 32) | ((em[1] == nonce) & (em[0] == nn));
#pragma unroll
        for (int i = 0; i < 8; ++i) ok = ok & (gq[i][1] == cand[i]) & (gq[i][3] == cand[i]);
        if (__all(ok)) break;
        __builtin_amdgcn_s_sleep(QSL);
      }
#pragma unroll
      for (int hf = 0; hf < 2; ++hf) {
        float q0 = 0.f, q1 = 0.f, q2 = 0.f, q3 = 0.f;
#pragma unroll
        for (int kq = 0; kq < 4; ++kq) {
          const v4u t4 = gq[2 * kq + hf];
          q0 += bflo(t4[0]); q1 += bfhi(t4[0]); q2 += bflo(t4[2]); q3 += bfhi(t4[2]);
        }
        *(LAS v2u*)(lds + L_Q + hf * 512 + lane * 8) = v2u{cvtpk(q0, q1), cvtpk(q2, q3)};
      }
      {
        const int df = (int)(nn - tstart);
        const bool use = lane >= 32 && df > -256 && df < 256;
        const float sd = wave_sum(use ? (float)df : 0.f), cn = wave_sum(use ? 1.f : 0.f);
        if (lane == 0) flags[F_TB] = tstart + (unsigned)(int)(sd * __builtin_amdgcn_rcpf(fmaxf(cn, 1.f)));
      }
      LGKM0();
      if (lane == 0) __hip_atomic_store(flags + F_Q, 1u, RLX_WG);
      const bool r2 = lane >= 32 && (fin || lane - 32 == fjb);
      if (r2) __builtin_amdgcn_raw_buffer_store_b64(v2u{nonce, nn}, wsr, eco, 0, AUX_SC1);
      for (unsigned spin = 0; spin < SPIN_CAP; ++spin) {
        __builtin_amdgcn_s_sleep(48);
        CFENCE();
        unsigned n2 = nn;
        if (r2) {
          n2 = __builtin_amdgcn_raw_buffer_load_b32(wsr, nno, 0, AUX_SC1);
          em = __builtin_amdgcn_raw_buffer_load_b64(wsr, emo, 0, AUX_SC1);
          if (n2 != nn) __builtin_amdgcn_raw_buffer_store_b64(v2u{nonce, n2}, wsr, eco, 0, AUX_SC1);
        }
        nn = n2;
        if (__all(!r2 | ((em[1] == nonce) & (em[0] == nn)))) break;
      }
      if (fin) {
        wait_until(tstart, TABT);
#pragma unroll 1
        for (int c = 0; c < 3; ++c) {
          for (unsigned spin = 0; spin < SPIN_CAP; ++spin) {
            CFENCE();
            v4u tv[8];
#pragma unroll
            for (int i = 0; i < 8; ++i) tv[i] = __builtin_amdgcn_raw_buffer_load_b128(wsr, WS_TAB + c * 8192 + lane * 16, i * 1024, AUX_SC1);
            bool ok = true;
#pragma unroll
            for (int i = 0; i < 8; ++i) {
              ok = ok & (tv[i][1] == 1u) & (tv[i][3] == 1u);
              *(LAS v2u*)(lds + L_T + c * 4096 + i * 512 + lane * 8) = v2u{tv[i][0], tv[i][2]};
            }
            if (__all(ok)) break;
            __builtin_amdgcn_s_sleep(16);
          }
        }
        LGKM0();
        if (lane == 0) __hip_atomic_store(flags + F_FIN, 1u, RLX_WG);
        asm volatile(".Lpf_end_a:");
      }
    } else if (wave == 2) {
      wait_until(tstart, PART);
      const v4u s1 = make_srd(w1, FF * 2), s2 = make_srd(b1, FF * 2), s3 = make_srd(w2, FF * 2);
#pragma unroll
      for (int i = 0; i < 3; ++i) {
        dma16<false>(lds0 + L_P + i * 1024, voff, s1, i * 1024);
        dma16<false>(lds0 + L_P + 3584 + i * 1024, voff, s2, i * 1024);
        dma16<false>(lds0 + L_P + 7168 + i * 1024, voff, s3, i * 1024);
      }
      if (lane < 32) {
        dma16<false>(lds0 + L_P + 3072, voff, s1, 3072);
        dma16<false>(lds0 + L_P + 3584 + 3072, voff, s2, 3072);
        dma16<false>(lds0 + L_P + 7168 + 3072, voff, s3, 3072);
      }
      const unsigned b2u = b2[0];
      if (lane == 0) flags[F_B2] = b2u;
      asm volatile("s_waitcnt vmcnt(0) lgkmcnt(0)" ::: "memory");
      if (lane == 0) __hip_atomic_store(flags + F_P, 1u, RLX_WG);
    }
    if (wave == 3) {
      wait_until(tstart, HOTPF);
      const v4u hs = make_srd(wo, HQ * DH * DM * 2);
      if (lane < 16)
        asm volatile("s_mov_b32 m0, %0\n\ts_nop 0\n\tbuffer_load_dword %1, %2, 0 offen lds\n\ts_waitcnt vmcnt(0)"
                     :: "s"(lds0 + L_A), "v"(((((bid + 3) & 7) * 512 + (bid >> 3) * 16 + lane) << 10) + 384), "s"(hs) : "memory");
    }
    if (wave != 1) {
      __builtin_amdgcn_s_setprio(0);
      lds_wait_ne0(flags + F_P);
      lds_wait_ne0(flags + F_CLR);
      const int p0 = wave == 0 ? 0 : 2 * wave - 2;
#pragma unroll 1
      for (int ps = p0; ps < p0 + 2; ++ps) {
        const unsigned bits = (unsigned)((wgl & 127) * 24 + ps * 4 + (TAB_E0 << 7)) | (unsigned)((wgl >> 7) << 15);
        f32x2 t[2], a[2];
#pragma unroll
        for (int p = 0; p < 2; ++p) t[p] = f32x2{bflo(bits + 2 * p), bflo(bits + 2 * p + 1)};
        ffn_eval<2>(lds, lane, t, a);
        const unsigned k0 = cvtpk(wave_sum(a[0][0]), wave_sum(a[0][1])), k1 = cvtpk(wave_sum(a[1][0]), wave_sum(a[1][1]));
        __builtin_amdgcn_s_setprio(3);
        if (lane == 0) __builtin_amdgcn_raw_buffer_store_b128(v4u{k0, 1u, k1, 1u}, wsr, WS_TAB + wgl * 96 + ps * 16, 0, AUX_SC1);
        __builtin_amdgcn_s_setprio(0);
      }
      __builtin_amdgcn_s_setprio(3);
    }
    if (wave == 0) {
      while (__hip_atomic_load(flags + F_CNT, RLX_WG) != 8u) __builtin_amdgcn_s_sleep(2);
      CFENCE();
      const int hd = lane & 3;
      float M = -INFINITY, L = 0.f;
#pragma unroll
      for (int w = 0; w < 8; ++w) M = fmaxf(M, ((LAS float*)(lds + L_ML))[w * 8 + hd]);
#pragma unroll
      for (int w = 0; w < 8; ++w) L = fmaf(__builtin_amdgcn_exp2f(((LAS float*)(lds + L_ML))[w * 8 + hd] - M), ((LAS float*)(lds + L_ML))[w * 8 + 4 + hd], L);
      if (lane < 4) __builtin_amdgcn_raw_buffer_store_b128(v4u{__float_as_uint(M), 1u, __float_as_uint(L), 1u}, wsr, WS_STAT + wgl * 128 + lane * 16, 0, AUX_SC1);
      v4u sv = {0u, 1u, 0u, 1u};
      for (unsigned spin = 0; spin < SPIN_CAP; ++spin) {
        __builtin_amdgcn_s_sleep(8);
        CFENCE();
        if (lane < 16) sv = __builtin_amdgcn_raw_buffer_load_b128(wsr, WS_STAT + ((b << 5) | ((lane >> 2) << 3) | kvh) * 128 + hd * 16, 0, AUX_SC1);
        if (__all((sv[1] == 1u) & (sv[3] == 1u))) break;
      }
      const float ms = __uint_as_float(sv[0]);
      float Mg = fmaxf(ms, dpp<0x124>(ms)); Mg = fmaxf(Mg, dpp<0x128>(Mg));
      float Lg = __builtin_amdgcn_exp2f(ms - Mg) * __uint_as_float(sv[2]);
      Lg += dpp<0x128>(Lg); Lg += dpp<0x124>(Lg);
      if (lane < 4) ((LAS float*)(lds + L_Z))[lane] = Mg + __builtin_amdgcn_logf(Lg);
      LGKM0();
      if (lane == 0) __hip_atomic_store(flags + F_Z, 1u, RLX_WG);
    }
  } else {
    const int sw = wave - 4, tid = threadIdx.x - 256;
    const __amdgpu_buffer_rsrc_t vrs = __builtin_amdgcn_make_buffer_rsrc((void*)vc, 0, 64 << 20, 0x00020000);
    const int vvo = kvo + voff;
    const v4u wsd = make_srd(wo, HQ * DH * DM * 2);
    const int wo_ = (kvh * 512 + sw + ((lane >> 2) & 3) * 128 + (lane >> 4) * 8) * 1024 + cj * 64 + (lane & 3) * 16;
#define WPIECE(k) do { dma16<true>(lds0 + L_W + sw * 4096 + (k) * 1024, wo_, wsd, (k) * 32 * 1024); SB(); } while (0)
#define VLOAD(i) do { WAITV(VW - 1); pace<sched(32 + (i))>(tb); vf[i] = __builtin_amdgcn_raw_buffer_load_b128(vrs, vvo, (i) * 8192, AUX_NT); SB(); \
    if (32 + (i) == WP0) WPIECE(0); if (32 + (i) == WP1) WPIECE(1); if (32 + (i) == WP2) WPIECE(2); if (32 + (i) == WP3) WPIECE(3); } while (0)
    const __amdgpu_buffer_rsrc_t krs = __builtin_amdgcn_make_buffer_rsrc((void*)kc, 0, 64 << 20, 0x00020000);
    v4u kf[32], vf[32];
    __builtin_amdgcn_s_setprio(2);
    unsigned tb = tstart + (STG * sw >> 4);
    static_for<KFIRST, 32>([&](auto ic) {
      constexpr int i = ic.value;
      WAITV(KW - 1);
      pace<sched(i)>(tb);
      if (i == XWSIG) { WAITV(XWSIG - KFIRST); if (lane == 0) __hip_atomic_fetch_add(flags + F_XW, 256u, RLX_WG); }
      KLOAD(i);
      if (i == WP0) WPIECE(0);
      if (i == WP1) WPIECE(1);
    });
    static_for<0, XV>([&](auto ic) { VLOAD(ic.value); });
    lds_wait_ne0(flags + F_Q);
    const unsigned tb0 = __builtin_amdgcn_readfirstlane(flags[F_TB]);
    tb = tb0 + TBO + (STG * sw >> 4);
    SB();
    const v4u qf = *(LAS v4u*)(lds + L_Q + (r & 3) * 256 + g * 64 + (r >> 2) * 16);
    kf[0] = *(LAS v4u*)(lds + L_K0 + sw * 2048 + voff);
    kf[1] = *(LAS v4u*)(lds + L_K0 + sw * 2048 + 1024 + voff);
    float s[32];
    static_for<0, 32>([&](auto ic) {
      constexpr int i = ic.value;
      const f32x4 a = mfma16(kf[i], qf, f32x4{0.f, 0.f, 0.f, 0.f});
      float t_ = a[0] + dpp<0x104>(a[1]); asm volatile("" : "+v"(t_));
      t_ += dpp<0x108>(a[2]); asm volatile("" : "+v"(t_));
      s[i] = t_ + dpp<0x10C>(a[3]); asm volatile("" : "+v"(s[i]));
      if constexpr ((i & 3) == 3) { SB(); VLOAD(XV + (i >> 2)); }
    });
    SB();
    float m = s[0];
#pragma unroll
    for (int i = 1; i < 32; ++i) m = fmaxf(m, s[i]);
    m = xrow_max(m) * SCALE_L2;
    asm volatile("" : "+v"(m));
    SB(); VLOAD(XV + 8);
    v4u pf[4];
#define SOFTMAX(G, mm) do { float p_[8]; \
    _Pragma("unroll") for (int j = 0; j < 8; ++j) p_[j] = __builtin_amdgcn_exp2f(fmaf(s[8 * (G) + j], SCALE_L2, -(mm))); \
    pf[G] = v4u{cvtpk(p_[0], p_[1]), cvtpk(p_[2], p_[3]), cvtpk(p_[4], p_[5]), cvtpk(p_[6], p_[7])}; } while (0)
    {
      SOFTMAX(0, m); SOFTMAX(1, m);
      asm volatile("" : "+v"(pf[0]), "+v"(pf[1]));
      SB(); VLOAD(XV + 9);
      SOFTMAX(2, m); SOFTMAX(3, m);
      asm volatile("" : "+v"(pf[2]), "+v"(pf[3]));
      SB(); VLOAD(XV + 10);
      const v4u ones = {0x3f803f80u, 0x3f803f80u, 0x3f803f80u, 0x3f803f80u};
      f32x4 l4 = {0.f, 0.f, 0.f, 0.f};
#pragma unroll
      for (int G = 0; G < 4; ++G) l4 = mfma16(pf[G], ones, l4);
      if (lane < 4) ((LAS float*)(lds + L_ML))[sw * 8 + lane] = m;
      if (lane == 0) *(LAS f32x4*)(lds + L_ML + sw * 32 + 16) = l4;
      LGKM0();
      if (lane == 0) __hip_atomic_fetch_add(flags + F_CNT, 1u, RLX_WG);
    }
    f32x4 acc[8];
#pragma unroll
    for (int j = 0; j < 8; ++j) acc[j] = f32x4{0.f, 0.f, 0.f, 0.f};
#define PV(G) do { \
    _Pragma("unroll") for (int d = 0; d < 4; ++d) { v4u lo_, hi_; \
      _Pragma("unroll") for (int jj = 0; jj < 4; ++jj) { \
        lo_[jj] = __builtin_amdgcn_perm(vf[8 * (G) + 2 * jj + 1][d], vf[8 * (G) + 2 * jj][d], 0x05040100u); \
        hi_[jj] = __builtin_amdgcn_perm(vf[8 * (G) + 2 * jj + 1][d], vf[8 * (G) + 2 * jj][d], 0x07060302u); } \
      acc[2 * d] = mfma16(pf[G], lo_, acc[2 * d]); acc[2 * d + 1] = mfma16(pf[G], hi_, acc[2 * d + 1]); } SB(); } while (0)
    static_for<XV + 11, 32>([&](auto ic) {
      constexpr int i = ic.value;
      VLOAD(i);
      if constexpr (i == XV + 11) PV(0);
      if constexpr (i == 15 + PVD) PV(1);
      if constexpr (i == 23 + PVD) PV(2);
    });
    v4u wf[2][2];
    {
      const int wtr = L_W + sw * 4096 + (g * 8 + (r >> 2)) * 64 + (r & 3) * 8;
#pragma unroll
      for (int ks = 0; ks < 2; ++ks)
#pragma unroll
        for (int nt = 0; nt < 2; ++nt) {
          const v2u u0 = __builtin_bit_cast(v2u, __builtin_amdgcn_ds_read_tr16_b64_v4i16((LAS v4s*)(lds + wtr + ks * 2048 + nt * 32)));
          const v2u u1 = __builtin_bit_cast(v2u, __builtin_amdgcn_ds_read_tr16_b64_v4i16((LAS v4s*)(lds + wtr + ks * 2048 + 256 + nt * 32)));
          wf[ks][nt] = v4u{u0.x, u0.y, u1.x, u1.y};
          asm volatile("" : "+v"(wf[ks][nt]));
        }
      SB();
    }
    unsigned a_mw = lds0 + L_B + sw * 2048 + voff, a_mr = lds0 + L_B + sw * 256 + voff;
    unsigned a_own = lds0 + L_A + sw * 2304 + me * 144 + lane * 8, a_put = lds0 + L_A + sw * 2304 + (lane >> 3) * 144 + (lane & 7) * 16;
    unsigned a_get = lds0 + L_A + sw * 2304 + r * 144 + g * 16;
    unsigned a_rw = lds0 + L_W + sw * 512 + lane * 4, a_rr = lds0 + L_W + ((g & 1) * 64 + (2 * sw + (g >> 1)) * 16 + r) * 4;
    int o_part = partg + me * 1024 + sw * 128 + lane * 8, o_get = partg + (lane >> 3) * 1024 + sw * 128 + (lane & 7) * 16;
    int o_outp = WS_OUTP + (((bh * 4 + 2 * sw + (g >> 1)) * 8 + kvh) * 512 + cj * 32 + (lane & 31)) * 4, o_fin = WS_OUTP + b * 16384 + tid * 4;
    us* outp = out + b * DM + tid;
    float xres = 0.f, b2v = 0.f;
    if (fin) {
      lds_wait_ne0(flags + F_FIN);
      xres = bflo(((LAS us*)(lds + L_X))[b * 520 + tid]); b2v = bflo(flags[F_B2]);
    }
    asm volatile("" : "+v"(a_mw), "+v"(a_mr), "+v"(a_own), "+v"(a_put), "+v"(a_get), "+v"(a_rw), "+v"(a_rr));
    asm volatile("" : "+v"(o_part), "+v"(o_get), "+v"(o_outp), "+v"(o_fin), "+v"(outp), "+v"(xres), "+v"(b2v));
#define LDSP(T, a) ((LAS T*)(uintptr_t)(a))
    lds_wait_ne0(flags + F_Z);
    SB();
    {
      const f32x4 Z4 = *(LAS f32x4*)(lds + L_Z);
      const f32x4 mo = *(LAS f32x4*)(lds + L_ML + sw * 32);
      const float zl = ((LAS float*)(lds + L_Z))[lane & 3];
      f32x4 f4;
#pragma unroll
      for (int i = 0; i < 4; ++i) f4[i] = __builtin_amdgcn_exp2f(mo[i] - Z4[i]);
#pragma unroll
      for (int j = 0; j < 8; ++j) acc[j] *= f4;
      const float zz = (r < 4) ? zl : m;
      SOFTMAX(3, zz);
      SB();
    }
    {
      v4u lo_[4], hi_[4];
#pragma unroll
      for (int jj = 0; jj < 4; ++jj) {
#pragma unroll
        for (int d = 0; d < 4; ++d) {
          lo_[d][jj] = __builtin_amdgcn_perm(vf[24 + 2 * jj + 1][d], vf[24 + 2 * jj][d], 0x05040100u);
          hi_[d][jj] = __builtin_amdgcn_perm(vf[24 + 2 * jj + 1][d], vf[24 + 2 * jj][d], 0x07060302u);
        }
        SB();
      }
#pragma unroll
      for (int d = 0; d < 4; ++d) { acc[2 * d] = mfma16(pf[3], lo_[d], acc[2 * d]); acc[2 * d + 1] = mfma16(pf[3], hi_[d], acc[2 * d + 1]); }
      SB();
    }
    if (lane < 16) {
#pragma unroll
      for (int j = 0; j < 8; ++j) LDSP(f32x4, a_mw)[j * 16] = acc[j];
    }
    WG_BARRIER();
    if (lane < 16) {
      f32x4 ow[8];
#pragma unroll
      for (int w = 0; w < 8; ++w) ow[w] = LDSP(f32x4, a_mr)[w * 128];
      const f32x4 o = ((ow[0] + ow[1]) + (ow[2] + ow[3])) + ((ow[4] + ow[5]) + (ow[6] + ow[7]));
      const v2u op = {cvtpk(o[0], o[1]), min(cvtpk(o[2], o[3]), EMPTY - 1u)};
      __builtin_amdgcn_raw_buffer_store_b64(op, wsr, o_part, 0, AUX_SC1);
      *LDSP(v2u, a_own) = op;
    }
    SB();
    {
      bool ok0 = (lane >> 3) == me, ok1 = (lane >> 3) + 8 == me;
      pace<sched(63) + H2T, 1, 4>(tb0);
      for (unsigned spin = 0; spin < SPIN_CAP; ++spin) {
        CFENCE();
        const v4u p0 = __builtin_amdgcn_raw_buffer_load_b128(wsr, ok0 ? WS_END : o_get, 0, AUX_SC1);
        const v4u p1 = __builtin_amdgcn_raw_buffer_load_b128(wsr, ok1 ? WS_END : o_get + 8192, 0, AUX_SC1);
        if (!ok0) *LDSP(v4u, a_put) = p0;
        if (!ok1) *LDSP(v4u, a_put + 1152) = p1;
        ok0 = max(p0[1], p0[3]) != EMPTY;
        ok1 = max(p1[1], p1[3]) != EMPTY;
        if (__all(ok0 & ok1)) break;
      }
      f32x4 c0 = {0.f, 0.f, 0.f, 0.f}, c1 = {0.f, 0.f, 0.f, 0.f};
#pragma unroll
      for (int ks = 0; ks < 2; ++ks) {
        const v4u af = LDSP(v4u, a_get)[ks * 4];
        c0 = mfma16(af, wf[ks][0], c0);
        c1 = mfma16(af, wf[ks][1], c1);
      }
      LDSP(float, a_rw)[0] = (c0[0] + c0[1]) + (c0[2] + c0[3]);
      LDSP(float, a_rw)[64] = (c1[0] + c1[1]) + (c1[2] + c1[3]);
    }
    WG_BARRIER();
    if (sw < 2) {
      LAS float* red = LDSP(float, a_rr);
      const float a = ((red[0] + red[128]) + (red[256] + red[384])) + ((red[512] + red[640]) + (red[768] + red[896]));
      __builtin_amdgcn_raw_buffer_store_b32(min(__float_as_uint(a), EMPTY - 1u), wsr, o_outp, 0, AUX_SC1);
    }
    if (fin) {
      unsigned pv[8];
      if (sw >= 2) __builtin_amdgcn_s_sleep(H3E);
      __builtin_amdgcn_s_sleep(H3D);
      for (unsigned spin = 0; spin < SPIN_CAP; ++spin) {
        CFENCE();
#pragma unroll
        for (int k = 0; k < 8; ++k) pv[k] = __builtin_amdgcn_raw_buffer_load_b32(wsr, o_fin, k * 2048, AUX_SC1);
        const unsigned mx = max(max(max(pv[0], pv[1]), max(pv[2], pv[3])), max(max(pv[4], pv[5]), max(pv[6], pv[7])));
        if (__all(mx != EMPTY)) break;
      }
      const float a = ((__uint_as_float(pv[0]) + __uint_as_float(pv[1])) + (__uint_as_float(pv[2]) + __uint_as_float(pv[3]))) +
                      ((__uint_as_float(pv[4]) + __uint_as_float(pv[5])) + (__uint_as_float(pv[6]) + __uint_as_float(pv[7])));
      const float h = rbf(xres + rbf(a));
      float s1 = h, s2 = h * h;
      wave_sum2(s1, s2);
      if ((lane & 47) == 0) ((LAS float*)(lds + L_B))[sw * 2 + g] = s1;
      WG_BARRIER();
      const f32x4 q0 = *(LAS f32x4*)(lds + L_B), q1 = *(LAS f32x4*)(lds + L_B + 16), q2 = *(LAS f32x4*)(lds + L_B + 32), q3 = *(LAS f32x4*)(lds + L_B + 48);
      const f32x4 qs = (q0 + q1) + (q2 + q3);
      const float mean = (qs[0] + qs[2]) * (1.f / DM);
      const float var = fmaxf((qs[1] + qs[3]) * (1.f / DM) - mean * mean, 0.f);
      const float r1 = __builtin_amdgcn_rsqf(var + 1e-5f);
      const float r2 = __builtin_amdgcn_rsqf(fmaf(var * r1, r1, 1e-5f));
      const float t = ((h - mean) * r1) * r2;
      const unsigned tb = cvtpk(t, t) & 0xffffu;
      const unsigned rel = (tb & 0x7fffu) - (unsigned)(TAB_E0 << 7);
      const bool inr = rel < (unsigned)TAB_H;
      float y = bflo(((LAS us*)(lds + L_T))[(inr ? rel : 0u) + (tb >> 15) * TAB_H]);
      u64 miss = __ballot(!inr);
      while (miss) {
        const int l = __builtin_ctzll(miss);
        miss &= miss - 1;
        const float ts = bflo((unsigned)__builtin_amdgcn_readlane((int)tb, l));
        const f32x2 tt[1] = {f32x2{ts, ts}};
        f32x2 aa[1];
        ffn_eval<1>(lds, lane, tt, aa);
        const float ys = rbf(wave_sum(aa[0][0]));
        if (lane == l) y = ys;
      }
      const float ov = h + rbf(y + b2v);
      *(GAS us*)outp = (us)(cvtpk(ov, ov) & 0xffffu);
    }
  }
  asm volatile(".Lpf_end_b:");
}

extern "C" void kernel_launch(void* const* d_in, const int* in_sizes, int n_in, void* d_out, int out_size, void* d_ws, size_t ws_size, hipStream_t stream) {
  fused_kernel<<<256, 768, 0, stream>>>((const us*)d_in[0], (const us*)d_in[1], (const us*)d_in[2], (const us*)d_in[3], (const us*)d_in[4],
                                        (const us*)d_in[6], (const us*)d_in[7], (const us*)d_in[8], (const us*)d_in[9], (us*)d_out, (unsigned char*)d_ws);
}
```

```cpp
#include <hip/hip_runtime.h>
#include <hip/hip_bf16.h>
#include <stdint.h>
#include <type_traits>

#define GAS __attribute__((address_space(1)))
#define LAS __attribute__((address_space(3)))
#define RLX_WG __ATOMIC_RELAXED, __HIP_MEMORY_SCOPE_WORKGROUP
typedef unsigned v4u __attribute__((ext_vector_type(4)));
typedef unsigned v2u __attribute__((ext_vector_type(2)));
typedef float f32x4 __attribute__((ext_vector_type(4)));
typedef float f32x2 __attribute__((ext_vector_type(2)));
typedef short bf16x8 __attribute__((ext_vector_type(8)));
typedef short v4s __attribute__((ext_vector_type(4)));
typedef __bf16 bf16x2_t __attribute__((ext_vector_type(2)));
typedef unsigned long long u64;
typedef unsigned short us;

constexpr int DM = 512, HQ = 32, DH = 128, FF = 1792;
constexpr float LOG2E = 1.4426950408889634f;
constexpr float SCALE_L2 = 0.08838834764831845f * LOG2E;
constexpr unsigned SPIN_CAP = 100000u;
constexpr int AUX_NT = 2, AUX_SC1 = 16;
constexpr unsigned EMPTY = 0xffffffffu;
constexpr int TAB_E0 = 108;
constexpr int TAB_H = 24 * 128;

constexpr int CODEPF = 64;
constexpr int KFIRST = 2;
constexpr int PER16 = 474;
constexpr int LEAD = 2;
constexpr int NE = 24;
constexpr int EX16 = 132;
constexpr int WC16 = 260;
constexpr int XWSIG = 5;
constexpr int PVD = 5;
constexpr int XV = 2;
constexpr int PSL = 2;
constexpr int PSLT = 7;
constexpr int NNT = 130;
constexpr int KW = 4;
constexpr int VW = 6;
constexpr int PART = 250;
constexpr int QT1 = 480;
constexpr int QSL = 8;
constexpr int TBO = -7;
constexpr int STG = 30;
constexpr int WP0 = 15;
constexpr int WP1 = 23;
constexpr int WP2 = 35;
constexpr int WP3 = 43;
constexpr int H3D = 4;
constexpr int H3E = 3;
constexpr int H2T = 152;
constexpr int HOTPF = 200;
constexpr int TABT = 1200;
constexpr int sched(int n) {
  return ((n - LEAD) * PER16 + (n < NE ? n : NE) * EX16 + ((n > WP0) + (n > WP1) + (n > WP2) + (n > WP3)) * WC16) >> 4;
}

constexpr int WS_NK = 0;
constexpr int WS_NB = 1024;
constexpr int WS_STAT = 4096;
constexpr int WS_TAB = WS_STAT + 256 * 128;
constexpr int WS_EM = WS_TAB + 256 * 128;
constexpr int WS_PART = WS_EM + 256 * 512;
constexpr int WS_OUTP = WS_PART + 256 * 1024;
constexpr int WS_QD = WS_OUTP + 64 * 2048;
constexpr int WS_END = WS_QD + 64 * 8192;

constexpr int L_W = 0;
constexpr int L_B = 32768;
constexpr int L_X = L_B + 16384;
constexpr int L_P = L_X + 9472;
constexpr int L_T = L_P + 10752;
constexpr int L_Q = L_T + 16384;
constexpr int L_QO = L_Q + 1024;
constexpr int L_ML = L_QO + 1024;
constexpr int L_Z = L_ML + 256;
constexpr int L_FL = L_Z + 64;
constexpr int L_A = L_FL + 64;
constexpr int L_K0 = L_A + 8 * 2304;
constexpr int L_TOTAL = L_K0 + 8 * 2048;
enum { F_Q = 0, F_CNT = 1, F_QO = 2, F_Z = 3, F_P = 4, F_FIN = 5, F_XW = 6, F_B2 = 7, F_CLR = 8, F_TB = 9 };

__device__ __forceinline__ f32x4 mfma16(v4u a, v4u b, f32x4 c) {
  return __builtin_amdgcn_mfma_f32_16x16x32_bf16(__builtin_bit_cast(bf16x8, a), __builtin_bit_cast(bf16x8, b), c, 0, 0, 0);
}
__device__ __forceinline__ unsigned cvtpk(float lo, float hi) {
  return __builtin_bit_cast(unsigned, __builtin_convertvector(f32x2{lo, hi}, bf16x2_t));
}
__device__ __forceinline__ float bflo(unsigned u) { return __builtin_bit_cast(float, u << 16); }
__device__ __forceinline__ float bfhi(unsigned u) { return __builtin_bit_cast(float, u & 0xffff0000u); }
__device__ __forceinline__ float rbf(float x) { return bflo(cvtpk(x, x)); }
__device__ __forceinline__ float xrow_max(float x) {
  auto s = __builtin_amdgcn_permlane16_swap(__float_as_uint(x), __float_as_uint(x), false, false);
  x = fmaxf(__uint_as_float(s[0]), __uint_as_float(s[1]));
  auto t = __builtin_amdgcn_permlane32_swap(__float_as_uint(x), __float_as_uint(x), false, false);
  return fmaxf(__uint_as_float(t[0]), __uint_as_float(t[1]));
}
__device__ __forceinline__ float xrow_sum(float x) {
  auto s = __builtin_amdgcn_permlane16_swap(__float_as_uint(x), __float_as_uint(x), false, false);
  x = __uint_as_float(s[0]) + __uint_as_float(s[1]);
  auto t = __builtin_amdgcn_permlane32_swap(__float_as_uint(x), __float_as_uint(x), false, false);
  return __uint_as_float(t[0]) + __uint_as_float(t[1]);
}
template <int CTRL> __device__ __forceinline__ float dpp(float x) {
  return __builtin_bit_cast(float, __builtin_amdgcn_mov_dpp(__builtin_bit_cast(int, x), CTRL, 0xf, 0xf, true));
}
__device__ __forceinline__ float wave_sum(float x) {
  x += dpp<0xB1>(x); x += dpp<0x4E>(x); x += dpp<0x141>(x); x += dpp<0x128>(x);
  return xrow_sum(x);
}
__device__ __forceinline__ void wave_sum2(float& x, float& y) {
  x += dpp<0xB1>(x); y += dpp<0xB1>(y); __builtin_amdgcn_sched_barrier(0);
  x += dpp<0x4E>(x); y += dpp<0x4E>(y); __builtin_amdgcn_sched_barrier(0);
  x += dpp<0x141>(x); y += dpp<0x141>(y); __builtin_amdgcn_sched_barrier(0);
  x += dpp<0x128>(x); y += dpp<0x128>(y); __builtin_amdgcn_sched_barrier(0);
  auto s = __builtin_amdgcn_permlane16_swap(__float_as_uint(x), __float_as_uint(y), false, false);
  const float z = __uint_as_float(s[0]) + __uint_as_float(s[1]);
  auto t = __builtin_amdgcn_permlane32_swap(__float_as_uint(z), __float_as_uint(z), false, false);
  x = __uint_as_float(t[0]) + __uint_as_float(t[1]);
  y = x;
}
#define SB() __builtin_amdgcn_sched_barrier(0)
#define CFENCE() asm volatile("" ::: "memory")
#define LGKM0() asm volatile("s_waitcnt lgkmcnt(0)" ::: "memory")
#define WAITV(n) asm volatile("s_waitcnt vmcnt(%0)" :: "i"(n) : "memory")
#define WG_BARRIER() do { asm volatile("s_waitcnt lgkmcnt(0)\n\ts_barrier" ::: "memory"); SB(); } while (0)
__device__ __forceinline__ v4u make_srd(const void* p, unsigned bytes) {
  const u64 a = (u64)p;
  v4u d;
  d[0] = __builtin_amdgcn_readfirstlane((unsigned)a);
  d[1] = __builtin_amdgcn_readfirstlane((unsigned)(a >> 32)) & 0xffffu;
  d[2] = bytes;
  d[3] = 0x00020000u;
  return d;
}
template <bool NT>
__device__ __forceinline__ void dma16(unsigned ldsaddr, int voff, v4u srd, int soff) {
  if (NT) asm volatile("s_mov_b32 m0, %0\n\ts_nop 0\n\tbuffer_load_dwordx4 %1, %2, %3 offen nt lds" :: "s"(ldsaddr), "v"(voff), "s"(srd), "s"(soff) : "memory");
  else asm volatile("s_mov_b32 m0, %0\n\ts_nop 0\n\tbuffer_load_dwordx4 %1, %2, %3 offen lds" :: "s"(ldsaddr), "v"(voff), "s"(srd), "s"(soff) : "memory");
}
__device__ __forceinline__ void lds_wait_ne0(LAS unsigned* f) {
  while (__hip_atomic_load(f, RLX_WG) == 0u) __builtin_amdgcn_s_sleep(1);
  CFENCE();
}
template <int TICKS, int SL = PSL, int SLT = PSLT>
__device__ __forceinline__ void pace(unsigned tstart) {
  if (TICKS > 0)
    asm volatile("s_memrealtime vcc\n\ts_waitcnt lgkmcnt(0)\n\ts_sub_i32 vcc_lo, vcc_lo, %0\n"
                 "1:\n\ts_cmpk_ge_i32 vcc_lo, %1\n\ts_cbranch_scc1 2f\n\ts_sleep %2\n\ts_addk_i32 vcc_lo, %3\n\ts_branch 1b\n2:"
                 :: "s"(tstart), "i"(TICKS), "i"(SL), "i"(SLT) : "vcc", "scc", "memory");
}

__device__ __forceinline__ void wait_until(unsigned t0, int ticks) {
  while ((int)((unsigned)__builtin_amdgcn_s_memrealtime() - t0) < ticks) __builtin_amdgcn_s_sleep(3);
  CFENCE();
}

template <int NP>
__device__ __forceinline__ void ffn_eval(LAS unsigned char* lds, int lane, const f32x2 (&t)[NP], f32x2 (&acc)[NP]) {
  constexpr float C0 = -2.f * 0.7978845608028654f * LOG2E, C1 = C0 * 0.044715f;
#pragma unroll
  for (int p = 0; p < NP; ++p) acc[p] = f32x2{0.f, 0.f};
#pragma unroll 1
  for (int j = 0; j < 7; ++j) {
    const v2u ua = *(LAS v2u*)(lds + L_P + j * 512 + lane * 8);
    const v2u ub = *(LAS v2u*)(lds + L_P + 3584 + j * 512 + lane * 8);
    const v2u uw = *(LAS v2u*)(lds + L_P + 7168 + j * 512 + lane * 8);
#pragma unroll
    for (int i = 0; i < 4; ++i) {
      const float a = (i & 1) ? bfhi(ua[i >> 1]) : bflo(ua[i >> 1]);
      const float b = (i & 1) ? bfhi(ub[i >> 1]) : bflo(ub[i >> 1]);
      const float w = (i & 1) ? bfhi(uw[i >> 1]) : bflo(uw[i >> 1]);
#pragma unroll
      for (int p = 0; p < NP; ++p) {
        const f32x2 u = __builtin_elementwise_fma(t[p], f32x2{a, a}, f32x2{b, b});
        const f32x2 y = u * __builtin_elementwise_fma(u * u, f32x2{C1, C1}, f32x2{C0, C0});
        f32x2 d = f32x2{__builtin_amdgcn_exp2f(y[0]), __builtin_amdgcn_exp2f(y[1])} + 1.f;
        d = f32x2{__builtin_amdgcn_rcpf(d[0]), __builtin_amdgcn_rcpf(d[1])};
        acc[p] = __builtin_elementwise_fma(u * d, f32x2{w, w}, acc[p]);
      }
    }
  }
}


template <int I, int N, class F> __device__ __forceinline__ void static_for(F&& f) {
  if constexpr (I < N) { f(std::integral_constant<int, I>{}); static_for<I + 1, N>(f); }
}

__global__ __launch_bounds__(768, 3) void fused_kernel(const us* __restrict__ x, const us* __restrict__ kc, const us* __restrict__ vc,
                                                       const us* __restrict__ wq, const us* __restrict__ wo, const us* __restrict__ w1,
                                                       const us* __restrict__ b1, const us* __restrict__ w2, const us* __restrict__ b2,
                                                       us* __restrict__ out, unsigned char* ws) {
  __shared__ __attribute__((aligned(16))) unsigned char smem[L_TOTAL];
  LAS unsigned char* lds = (LAS unsigned char*)smem;
  const unsigned lds0 = (unsigned)(uintptr_t)lds;
  const int lane = threadIdx.x & 63, wave = __builtin_amdgcn_readfirstlane(threadIdx.x >> 6), g = lane >> 4, r = lane & 15;
  LAS unsigned* flags = (LAS unsigned*)(lds + L_FL);
  asm volatile(".Lpf_begin:");
  const unsigned tstart = (unsigned)__builtin_amdgcn_s_memrealtime();
  asm volatile("" :: "s"(x), "s"(kc), "s"(vc), "s"(wq), "s"(ws));
  const int bid = blockIdx.x, sp = (bid >> 3) & 3, b = ((bid >> 3) & 4) | (bid >> 6), kvh = (bid + 3 * (bid >> 6)) & 7;
  const int voff = lane * 16;
  const int kvo = (((b * 8 + kvh) * 4 + sp) << 18) + ((wave - 4) << 10);
  const int koff = kvo + (r >> 2) * 256 + g * 64 + (r & 3) * 16;
#define KLOAD(i) do { kf[i] = __builtin_amdgcn_raw_buffer_load_b128(krs, koff, (i) * 8192, AUX_NT); SB(); } while (0)
  const __amdgpu_buffer_rsrc_t wsr = __builtin_amdgcn_make_buffer_rsrc((void*)ws, 0, WS_END, 0x00020000);
  const int jk = sp * 8 + b;
  const int jb = sp * 8 + kvh;
  if (wave < 4) {
    const v4u srd = make_srd(x, 8 * DM * 2);
    dma16<false>(lds0 + L_X + wave * 2080, voff, srd, wave * 2048);
    dma16<false>(lds0 + L_X + wave * 2080 + 1040, voff, srd, wave * 2048 + 1024);
    if (wave == 1 && lane < 2)
      __builtin_amdgcn_raw_buffer_store_b32(tstart, wsr, lane ? WS_NB + b * 128 + jb * 4 : WS_NK + kvh * 128 + jk * 4, 0, AUX_SC1);
    if (bid < CODEPF && wave == 3) {
      u64 lo; unsigned back, la, lb;
      asm volatile("s_getpc_b64 %0\n.Lpf_here:\n\ts_mov_b32 %1, .Lpf_here-.Lpf_begin\n\ts_mov_b32 %2, .Lpf_end_a-.Lpf_begin\n\ts_mov_b32 %3, .Lpf_end_b-.Lpf_begin"
                   : "=s"(lo), "=s"(back), "=s"(la), "=s"(lb));
      const v4u cs = make_srd((const void*)(lo - back), la > lb ? la : lb);
      if (lane < 24) asm volatile("s_mov_b32 m0, %0\n\ts_nop 0\n\tbuffer_load_dword %1, %2, 0 offen lds" :: "s"(lds0 + L_T), "v"((lane + 24 * ((bid >> 3) & 7)) * 128), "s"(cs) : "memory");
    }
    if (threadIdx.x < 16) flags[threadIdx.x] = 0u;
    SB();
  } else {
    const v4u ksd = make_srd(kc, 64 << 20);
    dma16<true>(lds0 + L_K0 + (wave - 4) * 2048, koff, ksd, 0);
    dma16<true>(lds0 + L_K0 + (wave - 4) * 2048 + 1024, koff, ksd, 8192);
    const v4u srd = make_srd(wq, DM * HQ * DH * 2);
    const int vo = (sp * 128 + (wave - 4) * 16 + (lane >> 3)) * 8192 + kvh * 1024 + b * 128 + (lane & 7) * 16;
    dma16<true>(lds0 + L_B + (wave - 4) * 2048, vo, srd, 0);
    dma16<true>(lds0 + L_B + (wave - 4) * 2048 + 1024, vo, srd, 8 * 8192);
    SB();
  }
  WG_BARRIER();
  const int wgl = (b << 5) | jb;
  const int cj = (b & 3) * 4 + sp, bh = b >> 2;
  const int partg = WS_PART + (kvh * 2 + bh) * 16384;
  const int me = (b & 3) * 4 + sp;
  const int fjb = (b + 3 * (b & 3)) & 7;
  const bool fin = jb == fjb;

  if (wave < 4) {
    __builtin_amdgcn_s_setprio(3);
    if (wave != 1) {
      WAITV(0);
      if (lane == 0) __hip_atomic_fetch_add(flags + F_XW, 1u, RLX_WG);
    }
    if (wave == 0) {
      *(LAS v4u*)(lds + L_X + 8 * 1040 + lane * 16) = v4u{0x3f803f80u, 0x3f803f80u, 0x3f803f80u, 0x3f803f80u};
      while ((__hip_atomic_load(flags + F_XW, RLX_WG) & 255u) != 4u) __builtin_amdgcn_s_sleep(1);
      CFENCE();
      const int xo = L_X + (r < 8 ? r : 8) * 1040 + g * 16;
      f32x4 d2a = {0.f, 0.f, 0.f, 0.f}, d2b = {0.f, 0.f, 0.f, 0.f};
#pragma unroll
      for (int s = 0; s < 16; s += 2) {
        const v4u xa = *(LAS v4u*)(lds + xo + s * 64), xb = *(LAS v4u*)(lds + xo + s * 64 + 64);
        d2a = mfma16(xa, xa, d2a); d2b = mfma16(xb, xb, d2b);
      }
      d2a += d2b;
      const float dg = (r & 2) ? ((r & 1) ? d2a[3] : d2a[2]) : ((r & 1) ? d2a[1] : d2a[0]);
      const float sxx = xrow_sum(g == (r >> 2) ? dg : 0.f);
      const float mean = xrow_sum(g == 2 ? d2a[0] : 0.f) * (1.f / DM);
      float rstd = rsqrtf(fmaxf(sxx * (1.f / DM) - mean * mean, 0.f) + 1e-5f);
      v4u xb[4];
#pragma unroll
      for (int s = 0; s < 4; ++s) { xb[s] = *(LAS v4u*)(lds + xo + sp * 256 + s * 64); asm volatile("" : "+v"(xb[s])); }
      asm volatile("" : "+v"(rstd));
      while (__hip_atomic_load(flags + F_XW, RLX_WG) != 4u + 8u * 256u) __builtin_amdgcn_s_sleep(1);
      CFENCE();
      f32x4 d[4];
#pragma unroll
      for (int nt = 0; nt < 4; ++nt) d[nt] = f32x4{0.f, 0.f, 0.f, 0.f};
      const int troff = L_B + (8 * g + (r >> 2)) * 128 + (r & 3) * 8;
#pragma unroll
      for (int s = 0; s < 4; ++s)
#pragma unroll
        for (int nt = 0; nt < 4; ++nt) {
          const v2u u0 = __builtin_bit_cast(v2u, __builtin_amdgcn_ds_read_tr16_b64_v4i16((LAS v4s*)(lds + troff + s * 4096 + nt * 32)));
          const v2u u1 = __builtin_bit_cast(v2u, __builtin_amdgcn_ds_read_tr16_b64_v4i16((LAS v4s*)(lds + troff + s * 4096 + 512 + nt * 32)));
          d[nt] = mfma16(v4u{u0.x, u0.y, u1.x, u1.y}, xb[s], d[nt]);
        }
#pragma unroll
      for (int nt = 0; nt < 4; ++nt) {
        float qv[4];
#pragma unroll
        for (int i = 0; i < 4; ++i) qv[i] = (d[nt][i] - mean * dpp<0x158>(d[nt][i])) * rstd;
        if (r < 8) *(LAS v2u*)(lds + L_QO + r * 128 + nt * 32 + g * 8) = v2u{cvtpk(qv[0], qv[1]), cvtpk(qv[2], qv[3])};
      }
      LGKM0();
      if (lane == 0) __hip_atomic_store(flags + F_QO, 1u, RLX_WG);
    } else if (wave == 1) {
      const int emo = WS_EM + wgl * 512 + lane * 8;
      const int qdo = WS_QD + (kvh * 8 + g) * 8192 + jk * 256 + r * 16;
      const int nno = lane < 32 ? WS_NK + kvh * 128 + lane * 4 : WS_NB + b * 128 + (lane - 32) * 4;
      const int eco = WS_EM + (lane < 32 ? ((((lane & 7) << 5) | (lane & 24) | kvh) * 64 + jk) : (((b << 5) | (lane - 32)) * 64 + 32 + jb)) * 8;
      const v4u e4 = {EMPTY, EMPTY, EMPTY, EMPTY};
      __builtin_amdgcn_raw_buffer_store_b128(e4, wsr, partg + me * 1024 + lane * 16, 0, AUX_SC1);
      if (lane < 42)
        __builtin_amdgcn_raw_buffer_store_b128(e4, wsr, (lane < 32 ? WS_OUTP + (b * 8 + kvh) * 2048 + sp * 512 : lane < 36 ? WS_STAT - 512 + wgl * 128 : WS_TAB - 576 + wgl * 96) + lane * 16, 0, AUX_SC1);
      SB();
      v2u em = __builtin_amdgcn_raw_buffer_load_b64(wsr, emo, 0, AUX_SC1);
      v4u s0 = __builtin_amdgcn_raw_buffer_load_b128(wsr, qdo, 0, AUX_SC1);
      v4u s1 = __builtin_amdgcn_raw_buffer_load_b128(wsr, qdo, 4 * 8192, AUX_SC1);
      SB();
      WAITV(6);
      if (lane == 0) __hip_atomic_fetch_add(flags + F_XW, 1u, RLX_WG);
      wait_until(tstart, NNT);
      unsigned nn = __builtin_amdgcn_raw_buffer_load_b32(wsr, nno, 0, AUX_SC1);
      asm volatile("s_waitcnt vmcnt(0)" : "+v"(em), "+v"(s0), "+v"(s1), "+v"(nn) :: "memory");
      unsigned nonce = tstart;
      while (__any((em[1] == nonce) | (s0[1] == nonce) | (s0[3] == nonce) | (s1[1] == nonce) | (s1[3] == nonce))) ++nonce;
      if (nonce != tstart) {
        if (lane < 2) __builtin_amdgcn_raw_buffer_store_b32(nonce, wsr, lane ? WS_NB + b * 128 + jb * 4 : WS_NK + kvh * 128 + jk * 4, 0, AUX_SC1);
        WAITV(0);
      }
      if (lane < 32) __builtin_amdgcn_raw_buffer_store_b64(v2u{nonce, nn}, wsr, eco, 0, AUX_SC1);
      if (lane == 0) __hip_atomic_store(flags + F_CLR, 1u, RLX_WG);
      unsigned cand[8];
#pragma unroll
      for (int i = 0; i < 8; ++i) cand[i] = __builtin_amdgcn_ds_bpermute((4 * i + g) * 4, nn);
      lds_wait_ne0(flags + F_QO);
      {
        const v2u q0 = *(LAS v2u*)(lds + L_QO + lane * 8), q1 = *(LAS v2u*)(lds + L_QO + 512 + lane * 8);
        __builtin_amdgcn_raw_buffer_store_b128(v4u{q0.x, nonce, q0.y, nonce}, wsr, qdo, 0, AUX_SC1);
        __builtin_amdgcn_raw_buffer_store_b128(v4u{q1.x, nonce, q1.y, nonce}, wsr, qdo, 4 * 8192, AUX_SC1);
      }
      const int qio = WS_QD + (kvh * 8 + b) * 8192 + lane * 16;
      v4u gq[8];
#pragma unroll
      for (int i = 0; i < 8; ++i) gq[i] = v4u{0u, ~cand[i], 0u, 0u};
      wait_until(tstart, QT1);
      for (unsigned spin = 0; spin < SPIN_CAP; ++spin) {
        CFENCE();
        const unsigned n2 = __builtin_amdgcn_raw_buffer_load_b32(wsr, nno, 0, AUX_SC1);
        if (lane < 32) em = __builtin_amdgcn_raw_buffer_load_b64(wsr, emo, 0, AUX_SC1);
#pragma unroll
        for (int i = 0; i < 8; ++i)
          if ((gq[i][1] != cand[i]) | (gq[i][3] != cand[i])) gq[i] = __builtin_amdgcn_raw_buffer_load_b128(wsr, qio, i * 1024, AUX_SC1);
        if (__any(n2 != nn)) {
          if (n2 != nn && lane < 32) __builtin_amdgcn_raw_buffer_store_b64(v2u{nonce, n2}, wsr, eco, 0, AUX_SC1);
          nn = n2;
#pragma unroll
          for (int i = 0; i < 8; ++i) cand[i] = __builtin_amdgcn_ds_bpermute((4 * i + g) * 4, nn);
        }
        bool ok = (lane >= 32) | ((em[1] == nonce) & (em[0] == nn));
#pragma unroll
        for (int i = 0; i < 8; ++i) ok = ok & (gq[i][1] == cand[i]) & (gq[i][3] == cand[i]);
        if (__all(ok)) break;
        __builtin_amdgcn_s_sleep(QSL);
      }
#pragma unroll
      for (int hf = 0; hf < 2; ++hf) {
        float q0 = 0.f, q1 = 0.f, q2 = 0.f, q3 = 0.f;
#pragma unroll
        for (int kq = 0; kq < 4; ++kq) {
          const v4u t4 = gq[2 * kq + hf];
          q0 += bflo(t4[0]); q1 += bfhi(t4[0]); q2 += bflo(t4[2]); q3 += bfhi(t4[2]);
        }
        *(LAS v2u*)(lds + L_Q + hf * 512 + lane * 8) = v2u{cvtpk(q0, q1), cvtpk(q2, q3)};
      }
      {
        const int df = (int)(nn - tstart);
        const bool use = lane >= 32 && df > -256 && df < 256;
        const float sd = wave_sum(use ? (float)df : 0.f), cn = wave_sum(use ? 1.f : 0.f);
        if (lane == 0) flags[F_TB] = tstart + (unsigned)(int)(sd * __builtin_amdgcn_rcpf(fmaxf(cn, 1.f)));
      }
      LGKM0();
      if (lane == 0) __hip_atomic_store(flags + F_Q, 1u, RLX_WG);
      const bool r2 = lane >= 32 && (fin || lane - 32 == fjb);
      if (r2) __builtin_amdgcn_raw_buffer_store_b64(v2u{nonce, nn}, wsr, eco, 0, AUX_SC1);
      for (unsigned spin = 0; spin < SPIN_CAP; ++spin) {
        __builtin_amdgcn_s_sleep(48);
        CFENCE();
        unsigned n2 = nn;
        if (r2) {
          n2 = __builtin_amdgcn_raw_buffer_load_b32(wsr, nno, 0, AUX_SC1);
          em = __builtin_amdgcn_raw_buffer_load_b64(wsr, emo, 0, AUX_SC1);
          if (n2 != nn) __builtin_amdgcn_raw_buffer_store_b64(v2u{nonce, n2}, wsr, eco, 0, AUX_SC1);
        }
        nn = n2;
        if (__all(!r2 | ((em[1] == nonce) & (em[0] == nn)))) break;
      }
      if (fin) {
        wait_until(tstart, TABT);
#pragma unroll 1
        for (int c = 0; c < 3; ++c) {
          for (unsigned spin = 0; spin < SPIN_CAP; ++spin) {
            CFENCE();
            v4u tv[8];
#pragma unroll
            for (int i = 0; i < 8; ++i) tv[i] = __builtin_amdgcn_raw_buffer_load_b128(wsr, WS_TAB + c * 8192 + lane * 16, i * 1024, AUX_SC1);
            bool ok = true;
#pragma unroll
            for (int i = 0; i < 8; ++i) {
              ok = ok & (tv[i][1] == 1u) & (tv[i][3] == 1u);
              *(LAS v2u*)(lds + L_T + c * 4096 + i * 512 + lane * 8) = v2u{tv[i][0], tv[i][2]};
            }
            if (__all(ok)) break;
            __builtin_amdgcn_s_sleep(16);
          }
        }
        LGKM0();
        if (lane == 0) __hip_atomic_store(flags + F_FIN, 1u, RLX_WG);
        asm volatile(".Lpf_end_a:");
      }
    } else if (wave == 2) {
      wait_until(tstart, PART);
      const v4u s1 = make_srd(w1, FF * 2), s2 = make_srd(b1, FF * 2), s3 = make_srd(w2, FF * 2);
#pragma unroll
      for (int i = 0; i < 3; ++i) {
        dma16<false>(lds0 + L_P + i * 1024, voff, s1, i * 1024);
        dma16<false>(lds0 + L_P + 3584 + i * 1024, voff, s2, i * 1024);
        dma16<false>(lds0 + L_P + 7168 + i * 1024, voff, s3, i * 1024);
      }
      if (lane < 32) {
        dma16<false>(lds0 + L_P + 3072, voff, s1, 3072);
        dma16<false>(lds0 + L_P + 3584 + 3072, voff, s2, 3072);
        dma16<false>(lds0 + L_P + 7168 + 3072, voff, s3, 3072);
      }
      const unsigned b2u = b2[0];
      if (lane == 0) flags[F_B2] = b2u;
      asm volatile("s_waitcnt vmcnt(0) lgkmcnt(0)" ::: "memory");
      if (lane == 0) __hip_atomic_store(flags + F_P, 1u, RLX_WG);
    }
    if (wave == 3) {
      wait_until(tstart, HOTPF);
      const v4u hs = make_srd(wo, HQ * DH * DM * 2), vs = make_srd(vc, 64 << 20);
      if (lane == 0)
        asm volatile("s_mov_b32 m0, %0\n\ts_nop 0\n\tbuffer_load_dword %1, %2, 0 offen nt lds" :: "s"(lds0 + L_A), "v"(kvo + 1024), "s"(vs) : "memory");
      if (lane < 17)
        asm volatile("s_mov_b32 m0, %0\n\ts_nop 0\n\tbuffer_load_dword %1, %2, 0 offen lds\n\ts_waitcnt vmcnt(0)"
                     :: "s"(lds0 + L_A), "v"(lane < 16 ? ((((bid + 3) & 7) * 512 + (bid >> 3) * 16 + lane) << 10) + 384 : (kvh << 19) + cj * 64), "s"(hs) : "memory");
    }
    if (wave != 1) {
      __builtin_amdgcn_s_setprio(0);
      lds_wait_ne0(flags + F_P);
      lds_wait_ne0(flags + F_CLR);
      const int p0 = wave == 0 ? 0 : 2 * wave - 2;
#pragma unroll 1
      for (int ps = p0; ps < p0 + 2; ++ps) {
        const unsigned bits = (unsigned)((wgl & 127) * 24 + ps * 4 + (TAB_E0 << 7)) | (unsigned)((wgl >> 7) << 15);
        f32x2 t[2], a[2];
#pragma unroll
        for (int p = 0; p < 2; ++p) t[p] = f32x2{bflo(bits + 2 * p), bflo(bits + 2 * p + 1)};
        ffn_eval<2>(lds, lane, t, a);
        const unsigned k0 = cvtpk(wave_sum(a[0][0]), wave_sum(a[0][1])), k1 = cvtpk(wave_sum(a[1][0]), wave_sum(a[1][1]));
        __builtin_amdgcn_s_setprio(3);
        if (lane == 0) __builtin_amdgcn_raw_buffer_store_b128(v4u{k0, 1u, k1, 1u}, wsr, WS_TAB + wgl * 96 + ps * 16, 0, AUX_SC1);
        __builtin_amdgcn_s_setprio(0);
      }
      __builtin_amdgcn_s_setprio(3);
    }
    if (wave == 0) {
      while (__hip_atomic_load(flags + F_CNT, RLX_WG) != 8u) __builtin_amdgcn_s_sleep(2);
      CFENCE();
      const int hd = lane & 3;
      float M = -INFINITY, L = 0.f;
#pragma unroll
      for (int w = 0; w < 8; ++w) M = fmaxf(M, ((LAS float*)(lds + L_ML))[w * 8 + hd]);
#pragma unroll
      for (int w = 0; w < 8; ++w) L = fmaf(__builtin_amdgcn_exp2f(((LAS float*)(lds + L_ML))[w * 8 + hd] - M), ((LAS float*)(lds + L_ML))[w * 8 + 4 + hd], L);
      if (lane < 4) __builtin_amdgcn_raw_buffer_store_b128(v4u{__float_as_uint(M), 1u, __float_as_uint(L), 1u}, wsr, WS_STAT + wgl * 128 + lane * 16, 0, AUX_SC1);
      v4u sv = {0u, 1u, 0u, 1u};
      for (unsigned spin = 0; spin < SPIN_CAP; ++spin) {
        __builtin_amdgcn_s_sleep(8);
        CFENCE();
        if (lane < 16) sv = __builtin_amdgcn_raw_buffer_load_b128(wsr, WS_STAT + ((b << 5) | ((lane >> 2) << 3) | kvh) * 128 + hd * 16, 0, AUX_SC1);
        if (__all((sv[1] == 1u) & (sv[3] == 1u))) break;
      }
      const float ms = __uint_as_float(sv[0]);
      float Mg = fmaxf(ms, dpp<0x124>(ms)); Mg = fmaxf(Mg, dpp<0x128>(Mg));
      float Lg = __builtin_amdgcn_exp2f(ms - Mg) * __uint_as_float(sv[2]);
      Lg += dpp<0x128>(Lg); Lg += dpp<0x124>(Lg);
      if (lane < 4) ((LAS float*)(lds + L_Z))[lane] = Mg + __builtin_amdgcn_logf(Lg);
      LGKM0();
      if (lane == 0) __hip_atomic_store(flags + F_Z, 1u, RLX_WG);
    }
  } else {
    const int sw = wave - 4, tid = threadIdx.x - 256;
    const __amdgpu_buffer_rsrc_t vrs = __builtin_amdgcn_make_buffer_rsrc((void*)vc, 0, 64 << 20, 0x00020000);
    const int vvo = kvo + voff;
    const v4u wsd = make_srd(wo, HQ * DH * DM * 2);
    const int wo_ = (kvh * 512 + sw + ((lane >> 2) & 3) * 128 + (lane >> 4) * 8) * 1024 + cj * 64 + (lane & 3) * 16;
#define WPIECE(k) do { dma16<true>(lds0 + L_W + sw * 4096 + (k) * 1024, wo_, wsd, (k) * 32 * 1024); SB(); } while (0)
#define VLOAD(i) do { WAITV(VW - 1); pace<sched(32 + (i))>(tb); vf[i] = __builtin_amdgcn_raw_buffer_load_b128(vrs, vvo, (i) * 8192, AUX_NT); SB(); \
    if (32 + (i) == WP0) WPIECE(0); if (32 + (i) == WP1) WPIECE(1); if (32 + (i) == WP2) WPIECE(2); if (32 + (i) == WP3) WPIECE(3); } while (0)
    const __amdgpu_buffer_rsrc_t krs = __builtin_amdgcn_make_buffer_rsrc((void*)kc, 0, 64 << 20, 0x00020000);
    v4u kf[32], vf[32];
    __builtin_amdgcn_s_setprio(2);
    unsigned tb = tstart + (STG * sw >> 4);
    static_for<KFIRST, 32>([&](auto ic) {
      constexpr int i = ic.value;
      WAITV(KW - 1);
      pace<sched(i)>(tb);
      if (i == XWSIG) { WAITV(XWSIG - KFIRST); if (lane == 0) __hip_atomic_fetch_add(flags + F_XW, 256u, RLX_WG); }
      KLOAD(i);
      if (i == WP0) WPIECE(0);
      if (i == WP1) WPIECE(1);
    });
    static_for<0, XV>([&](auto ic) { VLOAD(ic.value); });
    lds_wait_ne0(flags + F_Q);
    const unsigned tb0 = __builtin_amdgcn_readfirstlane(flags[F_TB]);
    tb = tb0 + TBO + (STG * sw >> 4);
    SB();
    const v4u qf = *(LAS v4u*)(lds + L_Q + (r & 3) * 256 + g * 64 + (r >> 2) * 16);
    kf[0] = *(LAS v4u*)(lds + L_K0 + sw * 2048 + voff);
    kf[1] = *(LAS v4u*)(lds + L_K0 + sw * 2048 + 1024 + voff);
    float s[32];
    static_for<0, 32>([&](auto ic) {
      constexpr int i = ic.value;
      const f32x4 a = mfma16(kf[i], qf, f32x4{0.f, 0.f, 0.f, 0.f});
      float t_ = a[0] + dpp<0x104>(a[1]); asm volatile("" : "+v"(t_));
      t_ += dpp<0x108>(a[2]); asm volatile("" : "+v"(t_));
      s[i] = t_ + dpp<0x10C>(a[3]); asm volatile("" : "+v"(s[i]));
      if constexpr ((i & 3) == 3) { SB(); VLOAD(XV + (i >> 2)); }
    });
    SB();
    float m = s[0];
#pragma unroll
    for (int i = 1; i < 32; ++i) m = fmaxf(m, s[i]);
    m = xrow_max(m) * SCALE_L2;
    asm volatile("" : "+v"(m));
    SB(); VLOAD(XV + 8);
    v4u pf[4];
#define SOFTMAX(G, mm) do { float p_[8]; \
    _Pragma("unroll") for (int j = 0; j < 8; ++j) p_[j] = __builtin_amdgcn_exp2f(fmaf(s[8 * (G) + j], SCALE_L2, -(mm))); \
    pf[G] = v4u{cvtpk(p_[0], p_[1]), cvtpk(p_[2], p_[3]), cvtpk(p_[4], p_[5]), cvtpk(p_[6], p_[7])}; } while (0)
    {
      SOFTMAX(0, m); SOFTMAX(1, m);
      asm volatile("" : "+v"(pf[0]), "+v"(pf[1]));
      SB(); VLOAD(XV + 9);
      SOFTMAX(2, m); SOFTMAX(3, m);
      asm volatile("" : "+v"(pf[2]), "+v"(pf[3]));
      SB(); VLOAD(XV + 10);
      const v4u ones = {0x3f803f80u, 0x3f803f80u, 0x3f803f80u, 0x3f803f80u};
      f32x4 l4 = {0.f, 0.f, 0.f, 0.f};
#pragma unroll
      for (int G = 0; G < 4; ++G) l4 = mfma16(pf[G], ones, l4);
      if (lane < 4) ((LAS float*)(lds + L_ML))[sw * 8 + lane] = m;
      if (lane == 0) *(LAS f32x4*)(lds + L_ML + sw * 32 + 16) = l4;
      LGKM0();
      if (lane == 0) __hip_atomic_fetch_add(flags + F_CNT, 1u, RLX_WG);
    }
    f32x4 acc[8];
#pragma unroll
    for (int j = 0; j < 8; ++j) acc[j] = f32x4{0.f, 0.f, 0.f, 0.f};
#define PV(G) do { \
    _Pragma("unroll") for (int d = 0; d < 4; ++d) { v4u lo_, hi_; \
      _Pragma("unroll") for (int jj = 0; jj < 4; ++jj) { \
        lo_[jj] = __builtin_amdgcn_perm(vf[8 * (G) + 2 * jj + 1][d], vf[8 * (G) + 2 * jj][d], 0x05040100u); \
        hi_[jj] = __builtin_amdgcn_perm(vf[8 * (G) + 2 * jj + 1][d], vf[8 * (G) + 2 * jj][d], 0x07060302u); } \
      acc[2 * d] = mfma16(pf[G], lo_, acc[2 * d]); acc[2 * d + 1] = mfma16(pf[G], hi_, acc[2 * d + 1]); } SB(); } while (0)
    static_for<XV + 11, 32>([&](auto ic) {
      constexpr int i = ic.value;
      VLOAD(i);
      if constexpr (i == XV + 11) PV(0);
      if constexpr (i == 15 + PVD) PV(1);
      if constexpr (i == 23 + PVD) PV(2);
    });
    v4u wf[2][2];
    {
      const int wtr = L_W + sw * 4096 + (g * 8 + (r >> 2)) * 64 + (r & 3) * 8;
#pragma unroll
      for (int ks = 0; ks < 2; ++ks)
#pragma unroll
        for (int nt = 0; nt < 2; ++nt) {
          const v2u u0 = __builtin_bit_cast(v2u, __builtin_amdgcn_ds_read_tr16_b64_v4i16((LAS v4s*)(lds + wtr + ks * 2048 + nt * 32)));
          const v2u u1 = __builtin_bit_cast(v2u, __builtin_amdgcn_ds_read_tr16_b64_v4i16((LAS v4s*)(lds + wtr + ks * 2048 + 256 + nt * 32)));
          wf[ks][nt] = v4u{u0.x, u0.y, u1.x, u1.y};
          asm volatile("" : "+v"(wf[ks][nt]));
        }
      SB();
    }
    unsigned a_mw = lds0 + L_B + sw * 2048 + voff, a_mr = lds0 + L_B + sw * 256 + voff;
    unsigned a_own = lds0 + L_A + sw * 2304 + me * 144 + lane * 8, a_put = lds0 + L_A + sw * 2304 + (lane >> 3) * 144 + (lane & 7) * 16;
    unsigned a_get = lds0 + L_A + sw * 2304 + r * 144 + g * 16;
    unsigned a_rw = lds0 + L_W + sw * 512 + lane * 4, a_rr = lds0 + L_W + ((g & 1) * 64 + (2 * sw + (g >> 1)) * 16 + r) * 4;
    int o_part = partg + me * 1024 + sw * 128 + lane * 8, o_get = partg + (lane >> 3) * 1024 + sw * 128 + (lane & 7) * 16;
    int o_outp = WS_OUTP + (((bh * 4 + 2 * sw + (g >> 1)) * 8 + kvh) * 512 + cj * 32 + (lane & 31)) * 4, o_fin = WS_OUTP + b * 16384 + tid * 4;
    us* outp = out + b * DM + tid;
    float xres = 0.f, b2v = 0.f;
    if (fin) {
      lds_wait_ne0(flags + F_FIN);
      xres = bflo(((LAS us*)(lds + L_X))[b * 520 + tid]); b2v = bflo(flags[F_B2]);
    }
    asm volatile("" : "+v"(a_mw), "+v"(a_mr), "+v"(a_own), "+v"(a_put), "+v"(a_get), "+v"(a_rw), "+v"(a_rr));
    asm volatile("" : "+v"(o_part), "+v"(o_get), "+v"(o_outp), "+v"(o_fin), "+v"(outp), "+v"(xres), "+v"(b2v));
#define LDSP(T, a) ((LAS T*)(uintptr_t)(a))
    lds_wait_ne0(flags + F_Z);
    SB();
    {
      const f32x4 Z4 = *(LAS f32x4*)(lds + L_Z);
      const f32x4 mo = *(LAS f32x4*)(lds + L_ML + sw * 32);
      const float zl = ((LAS float*)(lds + L_Z))[lane & 3];
      f32x4 f4;
#pragma unroll
      for (int i = 0; i < 4; ++i) f4[i] = __builtin_amdgcn_exp2f(mo[i] - Z4[i]);
#pragma unroll
      for (int j = 0; j < 8; ++j) acc[j] *= f4;
      const float zz = (r < 4) ? zl : m;
      SOFTMAX(3, zz);
      SB();
    }
    {
      v4u lo_[4], hi_[4];
#pragma unroll
      for (int jj = 0; jj < 4; ++jj) {
#pragma unroll
        for (int d = 0; d < 4; ++d) {
          lo_[d][jj] = __builtin_amdgcn_perm(vf[24 + 2 * jj + 1][d], vf[24 + 2 * jj][d], 0x05040100u);
          hi_[d][jj] = __builtin_amdgcn_perm(vf[24 + 2 * jj + 1][d], vf[24 + 2 * jj][d], 0x07060302u);
        }
        SB();
      }
#pragma unroll
      for (int d = 0; d < 4; ++d) { acc[2 * d] = mfma16(pf[3], lo_[d], acc[2 * d]); acc[2 * d + 1] = mfma16(pf[3], hi_[d], acc[2 * d + 1]); }
      SB();
    }
    if (lane < 16) {
#pragma unroll
      for (int j = 0; j < 8; ++j) LDSP(f32x4, a_mw)[j * 16] = acc[j];
    }
    WG_BARRIER();
    if (lane < 16) {
      f32x4 ow[8];
#pragma unroll
      for (int w = 0; w < 8; ++w) ow[w] = LDSP(f32x4, a_mr)[w * 128];
      const f32x4 o = ((ow[0] + ow[1]) + (ow[2] + ow[3])) + ((ow[4] + ow[5]) + (ow[6] + ow[7]));
      const v2u op = {cvtpk(o[0], o[1]), min(cvtpk(o[2], o[3]), EMPTY - 1u)};
      __builtin_amdgcn_raw_buffer_store_b64(op, wsr, o_part, 0, AUX_SC1);
      *LDSP(v2u, a_own) = op;
    }
    SB();
    {
      bool ok0 = (lane >> 3) == me, ok1 = (lane >> 3) + 8 == me;
      pace<sched(63) + H2T, 1, 4>(tb0);
      for (unsigned spin = 0; spin < SPIN_CAP; ++spin) {
        CFENCE();
        const v4u p0 = __builtin_amdgcn_raw_buffer_load_b128(wsr, ok0 ? WS_END : o_get, 0, AUX_SC1);
        const v4u p1 = __builtin_amdgcn_raw_buffer_load_b128(wsr, ok1 ? WS_END : o_get + 8192, 0, AUX_SC1);
        if (!ok0) *LDSP(v4u, a_put) = p0;
        if (!ok1) *LDSP(v4u, a_put + 1152) = p1;
        ok0 = max(p0[1], p0[3]) != EMPTY;
        ok1 = max(p1[1], p1[3]) != EMPTY;
        if (__all(ok0 & ok1)) break;
      }
      f32x4 c0 = {0.f, 0.f, 0.f, 0.f}, c1 = {0.f, 0.f, 0.f, 0.f};
#pragma unroll
      for (int ks = 0; ks < 2; ++ks) {
        const v4u af = LDSP(v4u, a_get)[ks * 4];
        c0 = mfma16(af, wf[ks][0], c0);
        c1 = mfma16(af, wf[ks][1], c1);
      }
      LDSP(float, a_rw)[0] = (c0[0] + c0[1]) + (c0[2] + c0[3]);
      LDSP(float, a_rw)[64] = (c1[0] + c1[1]) + (c1[2] + c1[3]);
    }
    WG_BARRIER();
    if (sw < 2) {
      LAS float* red = LDSP(float, a_rr);
      const float a = ((red[0] + red[128]) + (red[256] + red[384])) + ((red[512] + red[640]) + (red[768] + red[896]));
      __builtin_amdgcn_raw_buffer_store_b32(min(__float_as_uint(a), EMPTY - 1u), wsr, o_outp, 0, AUX_SC1);
    }
    if (fin) {
      unsigned pv[8];
      if (sw >= 2) __builtin_amdgcn_s_sleep(H3E);
      __builtin_amdgcn_s_sleep(H3D);
      for (unsigned spin = 0; spin < SPIN_CAP; ++spin) {
        CFENCE();
#pragma unroll
        for (int k = 0; k < 8; ++k) pv[k] = __builtin_amdgcn_raw_buffer_load_b32(wsr, o_fin, k * 2048, AUX_SC1);
        const unsigned mx = max(max(max(pv[0], pv[1]), max(pv[2], pv[3])), max(max(pv[4], pv[5]), max(pv[6], pv[7])));
        if (__all(mx != EMPTY)) break;
      }
      const float a = ((__uint_as_float(pv[0]) + __uint_as_float(pv[1])) + (__uint_as_float(pv[2]) + __uint_as_float(pv[3]))) +
                      ((__uint_as_float(pv[4]) + __uint_as_float(pv[5])) + (__uint_as_float(pv[6]) + __uint_as_float(pv[7])));
      const float h = rbf(xres + rbf(a));
      float s1 = h, s2 = h * h;
      wave_sum2(s1, s2);
      if ((lane & 47) == 0) ((LAS float*)(lds + L_B))[sw * 2 + g] = s1;
      WG_BARRIER();
      const f32x4 q0 = *(LAS f32x4*)(lds + L_B), q1 = *(LAS f32x4*)(lds + L_B + 16), q2 = *(LAS f32x4*)(lds + L_B + 32), q3 = *(LAS f32x4*)(lds + L_B + 48);
      const f32x4 qs = (q0 + q1) + (q2 + q3);
      const float mean = (qs[0] + qs[2]) * (1.f / DM);
      const float var = fmaxf((qs[1] + qs[3]) * (1.f / DM) - mean * mean, 0.f);
      const float r1 = __builtin_amdgcn_rsqf(var + 1e-5f);
      const float r2 = __builtin_amdgcn_rsqf(fmaf(var * r1, r1, 1e-5f));
      const float t = ((h - mean) * r1) * r2;
      const unsigned tb = cvtpk(t, t) & 0xffffu;
      const unsigned rel = (tb & 0x7fffu) - (unsigned)(TAB_E0 << 7);
      const bool inr = rel < (unsigned)TAB_H;
      float y = bflo(((LAS us*)(lds + L_T))[(inr ? rel : 0u) + (tb >> 15) * TAB_H]);
      u64 miss = __ballot(!inr);
      while (miss) {
        const int l = __builtin_ctzll(miss);
        miss &= miss - 1;
        const float ts = bflo((unsigned)__builtin_amdgcn_readlane((int)tb, l));
        const f32x2 tt[1] = {f32x2{ts, ts}};
        f32x2 aa[1];
        ffn_eval<1>(lds, lane, tt, aa);
        const float ys = rbf(wave_sum(aa[0][0]));
        if (lane == l) y = ys;
      }
      const float ov = h + rbf(y + b2v);
      *(GAS us*)outp = (us)(cvtpk(ov, ov) & 0xffffu);
    }
  }
  asm volatile(".Lpf_end_b:");
}

extern "C" void kernel_launch(void* const* d_in, const int* in_sizes, int n_in, void* d_out, int out_size, void* d_ws, size_t ws_size, hipStream_t stream) {
  fused_kernel<<<256, 768, 0, stream>>>((const us*)d_in[0], (const us*)d_in[1], (const us*)d_in[2], (const us*)d_in[3], (const us*)d_in[4],
                                        (const us*)d_in[6], (const us*)d_in[7], (const us*)d_in[8], (const us*)d_in[9], (us*)d_out, (unsigned char*)d_ws);
}
```

```cpp
#include <hip/hip_runtime.h>
#include <hip/hip_bf16.h>
#include <stdint.h>
#include <type_traits>

#define GAS __attribute__((address_space(1)))
#define LAS __attribute__((address_space(3)))
#define RLX_WG __ATOMIC_RELAXED, __HIP_MEMORY_SCOPE_WORKGROUP
typedef unsigned v4u __attribute__((ext_vector_type(4)));
typedef unsigned v2u __attribute__((ext_vector_type(2)));
typedef float f32x4 __attribute__((ext_vector_type(4)));
typedef float f32x2 __attribute__((ext_vector_type(2)));
typedef short bf16x8 __attribute__((ext_vector_type(8)));
typedef short v4s __attribute__((ext_vector_type(4)));
typedef __bf16 bf16x2_t __attribute__((ext_vector_type(2)));
typedef unsigned long long u64;
typedef unsigned short us;

constexpr int DM = 512, HQ = 32, DH = 128, FF = 1792;
constexpr float LOG2E = 1.4426950408889634f;
constexpr float SCALE_L2 = 0.08838834764831845f * LOG2E;
constexpr unsigned SPIN_CAP = 100000u;
constexpr int AUX_NT = 2, AUX_SC1 = 16;
constexpr unsigned EMPTY = 0xffffffffu;
constexpr int TAB_E0 = 108;
constexpr int TAB_H = 24 * 128;

constexpr int CODEPF = 64;
constexpr int KFIRST = 2;
constexpr int PER16 = 474;
constexpr int LEAD = 2;
constexpr int NE = 24;
constexpr int EX16 = 132;
constexpr int WC16 = 260;
constexpr int XWSIG = 5;
constexpr int PVD = 5;
constexpr int XV = 2;
constexpr int NNT = 130;
constexpr int KW = 4;
constexpr int VW = 6;
constexpr int PART = 250;
constexpr int QT1 = 480;
constexpr int QSL = 8;
constexpr int TBO = -7;
constexpr int STG = 30;
constexpr int WP0 = 15;
constexpr int WP1 = 23;
constexpr int WP2 = 35;
constexpr int WP3 = 43;
constexpr int H3D = 4;
constexpr int H3E = 3;
constexpr int H2T = 152;
constexpr int HOTPF = 200;
constexpr int EADJ = 6;
constexpr int ESL = 1;
constexpr int TABT = 1200;
constexpr int sched(int n) {
  return ((n - LEAD) * PER16 + (n < NE ? n : NE) * EX16 + ((n > WP0) + (n > WP1) + (n > WP2) + (n > WP3)) * WC16) >> 4;
}

constexpr int WS_NK = 0;
constexpr int WS_NB = 1024;
constexpr int WS_STAT = 4096;
constexpr int WS_TAB = WS_STAT + 256 * 128;
constexpr int WS_EM = WS_TAB + 256 * 128;
constexpr int WS_PART = WS_EM + 256 * 512;
constexpr int WS_OUTP = WS_PART + 256 * 1024;
constexpr int WS_QD = WS_OUTP + 64 * 2048;
constexpr int WS_END = WS_QD + 64 * 8192;

constexpr int L_W = 0;
constexpr int L_B = 32768;
constexpr int L_X = L_B + 16384;
constexpr int L_P = L_X + 9472;
constexpr int L_T = L_P + 10752;
constexpr int L_Q = L_T + 16384;
constexpr int L_QO = L_Q + 1024;
constexpr int L_ML = L_QO + 1024;
constexpr int L_Z = L_ML + 256;
constexpr int L_FL = L_Z + 64;
constexpr int L_A = L_FL + 64;
constexpr int L_K0 = L_A + 8 * 2304;
constexpr int L_TOTAL = L_K0 + 8 * 2048;
enum { F_Q = 0, F_CNT = 1, F_QO = 2, F_Z = 3, F_P = 4, F_FIN = 5, F_XW = 6, F_B2 = 7, F_CLR = 8, F_TB = 9 };

__device__ __forceinline__ f32x4 mfma16(v4u a, v4u b, f32x4 c) {
  return __builtin_amdgcn_mfma_f32_16x16x32_bf16(__builtin_bit_cast(bf16x8, a), __builtin_bit_cast(bf16x8, b), c, 0, 0, 0);
}
__device__ __forceinline__ unsigned cvtpk(float lo, float hi) {
  return __builtin_bit_cast(unsigned, __builtin_convertvector(f32x2{lo, hi}, bf16x2_t));
}
__device__ __forceinline__ float bflo(unsigned u) { return __builtin_bit_cast(float, u << 16); }
__device__ __forceinline__ float bfhi(unsigned u) { return __builtin_bit_cast(float, u & 0xffff0000u); }
__device__ __forceinline__ float rbf(float x) { return bflo(cvtpk(x, x)); }
__device__ __forceinline__ float xrow_max(float x) {
  auto s = __builtin_amdgcn_permlane16_swap(__float_as_uint(x), __float_as_uint(x), false, false);
  x = fmaxf(__uint_as_float(s[0]), __uint_as_float(s[1]));
  auto t = __builtin_amdgcn_permlane32_swap(__float_as_uint(x), __float_as_uint(x), false, false);
  return fmaxf(__uint_as_float(t[0]), __uint_as_float(t[1]));
}
__device__ __forceinline__ float xrow_sum(float x) {
  auto s = __builtin_amdgcn_permlane16_swap(__float_as_uint(x), __float_as_uint(x), false, false);
  x = __uint_as_float(s[0]) + __uint_as_float(s[1]);
  auto t = __builtin_amdgcn_permlane32_swap(__float_as_uint(x), __float_as_uint(x), false, false);
  return __uint_as_float(t[0]) + __uint_as_float(t[1]);
}
template <int CTRL> __device__ __forceinline__ float dpp(float x) {
  return __builtin_bit_cast(float, __builtin_amdgcn_mov_dpp(__builtin_bit_cast(int, x), CTRL, 0xf, 0xf, true));
}
__device__ __forceinline__ float wave_sum(float x) {
  x += dpp<0xB1>(x); x += dpp<0x4E>(x); x += dpp<0x141>(x); x += dpp<0x128>(x);
  return xrow_sum(x);
}
__device__ __forceinline__ void wave_sum2(float& x, float& y) {
  x += dpp<0xB1>(x); y += dpp<0xB1>(y); __builtin_amdgcn_sched_barrier(0);
  x += dpp<0x4E>(x); y += dpp<0x4E>(y); __builtin_amdgcn_sched_barrier(0);
  x += dpp<0x141>(x); y += dpp<0x141>(y); __builtin_amdgcn_sched_barrier(0);
  x += dpp<0x128>(x); y += dpp<0x128>(y); __builtin_amdgcn_sched_barrier(0);
  auto s = __builtin_amdgcn_permlane16_swap(__float_as_uint(x), __float_as_uint(y), false, false);
  const float z = __uint_as_float(s[0]) + __uint_as_float(s[1]);
  auto t = __builtin_amdgcn_permlane32_swap(__float_as_uint(z), __float_as_uint(z), false, false);
  x = __uint_as_float(t[0]) + __uint_as_float(t[1]);
  y = x;
}
#define SB() __builtin_amdgcn_sched_barrier(0)
#define CFENCE() asm volatile("" ::: "memory")
#define LGKM0() asm volatile("s_waitcnt lgkmcnt(0)" ::: "memory")
#define WAITV(n) asm volatile("s_waitcnt vmcnt(%0)" :: "i"(n) : "memory")
#define WG_BARRIER() do { asm volatile("s_waitcnt lgkmcnt(0)\n\ts_barrier" ::: "memory"); SB(); } while (0)
__device__ __forceinline__ v4u make_srd(const void* p, unsigned bytes) {
  const u64 a = (u64)p;
  v4u d;
  d[0] = __builtin_amdgcn_readfirstlane((unsigned)a);
  d[1] = __builtin_amdgcn_readfirstlane((unsigned)(a >> 32)) & 0xffffu;
  d[2] = bytes;
  d[3] = 0x00020000u;
  return d;
}
template <bool NT>
__device__ __forceinline__ void dma16(unsigned ldsaddr, int voff, v4u srd, int soff) {
  if (NT) asm volatile("s_mov_b32 m0, %0\n\ts_nop 0\n\tbuffer_load_dwordx4 %1, %2, %3 offen nt lds" :: "s"(ldsaddr), "v"(voff), "s"(srd), "s"(soff) : "memory");
  else asm volatile("s_mov_b32 m0, %0\n\ts_nop 0\n\tbuffer_load_dwordx4 %1, %2, %3 offen lds" :: "s"(ldsaddr), "v"(voff), "s"(srd), "s"(soff) : "memory");
}
__device__ __forceinline__ void lds_wait_ne0(LAS unsigned* f) {
  while (__hip_atomic_load(f, RLX_WG) == 0u) __builtin_amdgcn_s_sleep(1);
  CFENCE();
}
template <int TICKS>
__device__ __forceinline__ void pace(unsigned tstart) {
  if (TICKS > 0)
    asm volatile("s_branch 2f\n1:\n\ts_sleep %2\n2:\n\ts_memrealtime vcc\n\ts_waitcnt lgkmcnt(0)\n\ts_sub_i32 vcc_lo, vcc_lo, %0\n\t"
                 "s_cmpk_lt_i32 vcc_lo, %1\n\ts_cbranch_scc1 1b"
                 :: "s"(tstart), "i"(TICKS - EADJ), "i"(ESL) : "vcc", "scc", "memory");
}

__device__ __forceinline__ void wait_until(unsigned t0, int ticks) {
  while ((int)((unsigned)__builtin_amdgcn_s_memrealtime() - t0) < ticks) __builtin_amdgcn_s_sleep(3);
  CFENCE();
}

template <int NP>
__device__ __forceinline__ void ffn_eval(LAS unsigned char* lds, int lane, const f32x2 (&t)[NP], f32x2 (&acc)[NP]) {
  constexpr float C0 = -2.f * 0.7978845608028654f * LOG2E, C1 = C0 * 0.044715f;
#pragma unroll
  for (int p = 0; p < NP; ++p) acc[p] = f32x2{0.f, 0.f};
#pragma unroll 1
  for (int j = 0; j < 7; ++j) {
    const v2u ua = *(LAS v2u*)(lds + L_P + j * 512 + lane * 8);
    const v2u ub = *(LAS v2u*)(lds + L_P + 3584 + j * 512 + lane * 8);
    const v2u uw = *(LAS v2u*)(lds + L_P + 7168 + j * 512 + lane * 8);
#pragma unroll
    for (int i = 0; i < 4; ++i) {
      const float a = (i & 1) ? bfhi(ua[i >> 1]) : bflo(ua[i >> 1]);
      const float b = (i & 1) ? bfhi(ub[i >> 1]) : bflo(ub[i >> 1]);
      const float w = (i & 1) ? bfhi(uw[i >> 1]) : bflo(uw[i >> 1]);
#pragma unroll
      for (int p = 0; p < NP; ++p) {
        const f32x2 u = __builtin_elementwise_fma(t[p], f32x2{a, a}, f32x2{b, b});
        const f32x2 y = u * __builtin_elementwise_fma(u * u, f32x2{C1, C1}, f32x2{C0, C0});
        f32x2 d = f32x2{__builtin_amdgcn_exp2f(y[0]), __builtin_amdgcn_exp2f(y[1])} + 1.f;
        d = f32x2{__builtin_amdgcn_rcpf(d[0]), __builtin_amdgcn_rcpf(d[1])};
        acc[p] = __builtin_elementwise_fma(u * d, f32x2{w, w}, acc[p]);
      }
    }
  }
}


template <int I, int N, class F> __device__ __forceinline__ void static_for(F&& f) {
  if constexpr (I < N) { f(std::integral_constant<int, I>{}); static_for<I + 1, N>(f); }
}

__global__ __launch_bounds__(768, 3) void fused_kernel(const us* __restrict__ x, const us* __restrict__ kc, const us* __restrict__ vc,
                                                       const us* __restrict__ wq, const us* __restrict__ wo, const us* __restrict__ w1,
                                                       const us* __restrict__ b1, const us* __restrict__ w2, const us* __restrict__ b2,
                                                       us* __restrict__ out, unsigned char* ws) {
  __shared__ __attribute__((aligned(16))) unsigned char smem[L_TOTAL];
  LAS unsigned char* lds = (LAS unsigned char*)smem;
  const unsigned lds0 = (unsigned)(uintptr_t)lds;
  const int lane = threadIdx.x & 63, wave = __builtin_amdgcn_readfirstlane(threadIdx.x >> 6), g = lane >> 4, r = lane & 15;
  LAS unsigned* flags = (LAS unsigned*)(lds + L_FL);
  asm volatile(".Lpf_begin:");
  const unsigned tstart = (unsigned)__builtin_amdgcn_s_memrealtime();
  asm volatile("" :: "s"(x), "s"(kc), "s"(vc), "s"(wq), "s"(ws));
  const int bid = blockIdx.x, sp = (bid >> 3) & 3, b = ((bid >> 3) & 4) | (bid >> 6), kvh = (bid + 3 * (bid >> 6)) & 7;
  const int voff = lane * 16;
  const int kvo = (((b * 8 + kvh) * 4 + sp) << 18) + ((wave - 4) << 10);
  const int koff = kvo + (r >> 2) * 256 + g * 64 + (r & 3) * 16;
#define KLOAD(i) do { kf[i] = __builtin_amdgcn_raw_buffer_load_b128(krs, koff, (i) * 8192, AUX_NT); SB(); } while (0)
  const __amdgpu_buffer_rsrc_t wsr = __builtin_amdgcn_make_buffer_rsrc((void*)ws, 0, WS_END, 0x00020000);
  const int jk = sp * 8 + b;
  const int jb = sp * 8 + kvh;
  if (wave < 4) {
    const v4u srd = make_srd(x, 8 * DM * 2);
    dma16<false>(lds0 + L_X + wave * 2080, voff, srd, wave * 2048);
    dma16<false>(lds0 + L_X + wave * 2080 + 1040, voff, srd, wave * 2048 + 1024);
    if (wave == 1 && lane < 2)
      __builtin_amdgcn_raw_buffer_store_b32(tstart, wsr, lane ? WS_NB + b * 128 + jb * 4 : WS_NK + kvh * 128 + jk * 4, 0, AUX_SC1);
    if (bid < CODEPF && wave == 3) {
      u64 lo; unsigned back, la, lb;
      asm volatile("s_getpc_b64 %0\n.Lpf_here:\n\ts_mov_b32 %1, .Lpf_here-.Lpf_begin\n\ts_mov_b32 %2, .Lpf_end_a-.Lpf_begin\n\ts_mov_b32 %3, .Lpf_end_b-.Lpf_begin"
                   : "=s"(lo), "=s"(back), "=s"(la), "=s"(lb));
      const v4u cs = make_srd((const void*)(lo - back), la > lb ? la : lb);
      if (lane < 24) asm volatile("s_mov_b32 m0, %0\n\ts_nop 0\n\tbuffer_load_dword %1, %2, 0 offen lds" :: "s"(lds0 + L_T), "v"((lane + 24 * ((bid >> 3) & 7)) * 128), "s"(cs) : "memory");
    }
    if (threadIdx.x < 16) flags[threadIdx.x] = 0u;
    SB();
  } else {
    const v4u ksd = make_srd(kc, 64 << 20);
    dma16<true>(lds0 + L_K0 + (wave - 4) * 2048, koff, ksd, 0);
    dma16<true>(lds0 + L_K0 + (wave - 4) * 2048 + 1024, koff, ksd, 8192);
    const v4u srd = make_srd(wq, DM * HQ * DH * 2);
    const int vo = (sp * 128 + (wave - 4) * 16 + (lane >> 3)) * 8192 + kvh * 1024 + b * 128 + (lane & 7) * 16;
    dma16<true>(lds0 + L_B + (wave - 4) * 2048, vo, srd, 0);
    dma16<true>(lds0 + L_B + (wave - 4) * 2048 + 1024, vo, srd, 8 * 8192);
    SB();
  }
  WG_BARRIER();
  const int wgl = (b << 5) | jb;
  const int cj = (b & 3) * 4 + sp, bh = b >> 2;
  const int partg = WS_PART + (kvh * 2 + bh) * 16384;
  const int me = (b & 3) * 4 + sp;
  const int fjb = (b + 3 * (b & 3)) & 7;
  const bool fin = jb == fjb;

  if (wave < 4) {
    __builtin_amdgcn_s_setprio(3);
    if (wave != 1) {
      WAITV(0);
      if (lane == 0) __hip_atomic_fetch_add(flags + F_XW, 1u, RLX_WG);
    }
    if (wave == 0) {
      *(LAS v4u*)(lds + L_X + 8 * 1040 + lane * 16) = v4u{0x3f803f80u, 0x3f803f80u, 0x3f803f80u, 0x3f803f80u};
      while ((__hip_atomic_load(flags + F_XW, RLX_WG) & 255u) != 4u) __builtin_amdgcn_s_sleep(1);
      CFENCE();
      const int xo = L_X + (r < 8 ? r : 8) * 1040 + g * 16;
      f32x4 d2a = {0.f, 0.f, 0.f, 0.f}, d2b = {0.f, 0.f, 0.f, 0.f};
#pragma unroll
      for (int s = 0; s < 16; s += 2) {
        const v4u xa = *(LAS v4u*)(lds + xo + s * 64), xb = *(LAS v4u*)(lds + xo + s * 64 + 64);
        d2a = mfma16(xa, xa, d2a); d2b = mfma16(xb, xb, d2b);
      }
      d2a += d2b;
      const float dg = (r & 2) ? ((r & 1) ? d2a[3] : d2a[2]) : ((r & 1) ? d2a[1] : d2a[0]);
      const float sxx = xrow_sum(g == (r >> 2) ? dg : 0.f);
      const float mean = xrow_sum(g == 2 ? d2a[0] : 0.f) * (1.f / DM);
      float rstd = rsqrtf(fmaxf(sxx * (1.f / DM) - mean * mean, 0.f) + 1e-5f);
      v4u xb[4];
#pragma unroll
      for (int s = 0; s < 4; ++s) { xb[s] = *(LAS v4u*)(lds + xo + sp * 256 + s * 64); asm volatile("" : "+v"(xb[s])); }
      asm volatile("" : "+v"(rstd));
      while (__hip_atomic_load(flags + F_XW, RLX_WG) != 4u + 8u * 256u) __builtin_amdgcn_s_sleep(1);
      CFENCE();
      f32x4 d[4];
#pragma unroll
      for (int nt = 0; nt < 4; ++nt) d[nt] = f32x4{0.f, 0.f, 0.f, 0.f};
      const int troff = L_B + (8 * g + (r >> 2)) * 128 + (r & 3) * 8;
#pragma unroll
      for (int s = 0; s < 4; ++s)
#pragma unroll
        for (int nt = 0; nt < 4; ++nt) {
          const v2u u0 = __builtin_bit_cast(v2u, __builtin_amdgcn_ds_read_tr16_b64_v4i16((LAS v4s*)(lds + troff + s * 4096 + nt * 32)));
          const v2u u1 = __builtin_bit_cast(v2u, __builtin_amdgcn_ds_read_tr16_b64_v4i16((LAS v4s*)(lds + troff + s * 4096 + 512 + nt * 32)));
          d[nt] = mfma16(v4u{u0.x, u0.y, u1.x, u1.y}, xb[s], d[nt]);
        }
#pragma unroll
      for (int nt = 0; nt < 4; ++nt) {
        float qv[4];
#pragma unroll
        for (int i = 0; i < 4; ++i) qv[i] = (d[nt][i] - mean * dpp<0x158>(d[nt][i])) * rstd;
        if (r < 8) *(LAS v2u*)(lds + L_QO + r * 128 + nt * 32 + g * 8) = v2u{cvtpk(qv[0], qv[1]), cvtpk(qv[2], qv[3])};
      }
      LGKM0();
      if (lane == 0) __hip_atomic_store(flags + F_QO, 1u, RLX_WG);
    } else if (wave == 1) {
      const int emo = WS_EM + wgl * 512 + lane * 8;
      const int qdo = WS_QD + (kvh * 8 + g) * 8192 + jk * 256 + r * 16;
      const int nno = lane < 32 ? WS_NK + kvh * 128 + lane * 4 : WS_NB + b * 128 + (lane - 32) * 4;
      const int eco = WS_EM + (lane < 32 ? ((((lane & 7) << 5) | (lane & 24) | kvh) * 64 + jk) : (((b << 5) | (lane - 32)) * 64 + 32 + jb)) * 8;
      const v4u e4 = {EMPTY, EMPTY, EMPTY, EMPTY};
      __builtin_amdgcn_raw_buffer_store_b128(e4, wsr, partg + me * 1024 + lane * 16, 0, AUX_SC1);
      if (lane < 42)
        __builtin_amdgcn_raw_buffer_store_b128(e4, wsr, (lane < 32 ? WS_OUTP + (b * 8 + kvh) * 2048 + sp * 512 : lane < 36 ? WS_STAT - 512 + wgl * 128 : WS_TAB - 576 + wgl * 96) + lane * 16, 0, AUX_SC1);
      SB();
      v2u em = __builtin_amdgcn_raw_buffer_load_b64(wsr, emo, 0, AUX_SC1);
      v4u s0 = __builtin_amdgcn_raw_buffer_load_b128(wsr, qdo, 0, AUX_SC1);
      v4u s1 = __builtin_amdgcn_raw_buffer_load_b128(wsr, qdo, 4 * 8192, AUX_SC1);
      SB();
      WAITV(6);
      if (lane == 0) __hip_atomic_fetch_add(flags + F_XW, 1u, RLX_WG);
      wait_until(tstart, NNT);
      unsigned nn = __builtin_amdgcn_raw_buffer_load_b32(wsr, nno, 0, AUX_SC1);
      asm volatile("s_waitcnt vmcnt(0)" : "+v"(em), "+v"(s0), "+v"(s1), "+v"(nn) :: "memory");
      unsigned nonce = tstart;
      while (__any((em[1] == nonce) | (s0[1] == nonce) | (s0[3] == nonce) | (s1[1] == nonce) | (s1[3] == nonce))) ++nonce;
      if (nonce != tstart) {
        if (lane < 2) __builtin_amdgcn_raw_buffer_store_b32(nonce, wsr, lane ? WS_NB + b * 128 + jb * 4 : WS_NK + kvh * 128 + jk * 4, 0, AUX_SC1);
        WAITV(0);
      }
      if (lane < 32) __builtin_amdgcn_raw_buffer_store_b64(v2u{nonce, nn}, wsr, eco, 0, AUX_SC1);
      if (lane == 0) __hip_atomic_store(flags + F_CLR, 1u, RLX_WG);
      unsigned cand[8];
#pragma unroll
      for (int i = 0; i < 8; ++i) cand[i] = __builtin_amdgcn_ds_bpermute((4 * i + g) * 4, nn);
      lds_wait_ne0(flags + F_QO);
      {
        const v2u q0 = *(LAS v2u*)(lds + L_QO + lane * 8), q1 = *(LAS v2u*)(lds + L_QO + 512 + lane * 8);
        __builtin_amdgcn_raw_buffer_store_b128(v4u{q0.x, nonce, q0.y, nonce}, wsr, qdo, 0, AUX_SC1);
        __builtin_amdgcn_raw_buffer_store_b128(v4u{q1.x, nonce, q1.y, nonce}, wsr, qdo, 4 * 8192, AUX_SC1);
      }
      const int qio = WS_QD + (kvh * 8 + b) * 8192 + lane * 16;
      v4u gq[8];
#pragma unroll
      for (int i = 0; i < 8; ++i) gq[i] = v4u{0u, ~cand[i], 0u, 0u};
      wait_until(tstart, QT1);
      for (unsigned spin = 0; spin < SPIN_CAP; ++spin) {
        CFENCE();
        const unsigned n2 = __builtin_amdgcn_raw_buffer_load_b32(wsr, nno, 0, AUX_SC1);
        if (lane < 32) em = __builtin_amdgcn_raw_buffer_load_b64(wsr, emo, 0, AUX_SC1);
#pragma unroll
        for (int i = 0; i < 8; ++i)
          if ((gq[i][1] != cand[i]) | (gq[i][3] != cand[i])) gq[i] = __builtin_amdgcn_raw_buffer_load_b128(wsr, qio, i * 1024, AUX_SC1);
        if (__any(n2 != nn)) {
          if (n2 != nn && lane < 32) __builtin_amdgcn_raw_buffer_store_b64(v2u{nonce, n2}, wsr, eco, 0, AUX_SC1);
          nn = n2;
#pragma unroll
          for (int i = 0; i < 8; ++i) cand[i] = __builtin_amdgcn_ds_bpermute((4 * i + g) * 4, nn);
        }
        bool ok = (lane >= 32) | ((em[1] == nonce) & (em[0] == nn));
#pragma unroll
        for (int i = 0; i < 8; ++i) ok = ok & (gq[i][1] == cand[i]) & (gq[i][3] == cand[i]);
        if (__all(ok)) break;
        __builtin_amdgcn_s_sleep(QSL);
      }
#pragma unroll
      for (int hf = 0; hf < 2; ++hf) {
        float q0 = 0.f, q1 = 0.f, q2 = 0.f, q3 = 0.f;
#pragma unroll
        for (int kq = 0; kq < 4; ++kq) {
          const v4u t4 = gq[2 * kq + hf];
          q0 += bflo(t4[0]); q1 += bfhi(t4[0]); q2 += bflo(t4[2]); q3 += bfhi(t4[2]);
        }
        *(LAS v2u*)(lds + L_Q + hf * 512 + lane * 8) = v2u{cvtpk(q0, q1), cvtpk(q2, q3)};
      }
      {
        const int df = (int)(nn - tstart);
        const bool use = lane >= 32 && df > -256 && df < 256;
        const float sd = wave_sum(use ? (float)df : 0.f), cn = wave_sum(use ? 1.f : 0.f);
        if (lane == 0) flags[F_TB] = tstart + (unsigned)(int)(sd * __builtin_amdgcn_rcpf(fmaxf(cn, 1.f)));
      }
      LGKM0();
      if (lane == 0) __hip_atomic_store(flags + F_Q, 1u, RLX_WG);
      const bool r2 = lane >= 32 && (fin || lane - 32 == fjb);
      if (r2) __builtin_amdgcn_raw_buffer_store_b64(v2u{nonce, nn}, wsr, eco, 0, AUX_SC1);
      for (unsigned spin = 0; spin < SPIN_CAP; ++spin) {
        __builtin_amdgcn_s_sleep(48);
        CFENCE();
        unsigned n2 = nn;
        if (r2) {
          n2 = __builtin_amdgcn_raw_buffer_load_b32(wsr, nno, 0, AUX_SC1);
          em = __builtin_amdgcn_raw_buffer_load_b64(wsr, emo, 0, AUX_SC1);
          if (n2 != nn) __builtin_amdgcn_raw_buffer_store_b64(v2u{nonce, n2}, wsr, eco, 0, AUX_SC1);
        }
        nn = n2;
        if (__all(!r2 | ((em[1] == nonce) & (em[0] == nn)))) break;
      }
      if (fin) {
        wait_until(tstart, TABT);
#pragma unroll 1
        for (int c = 0; c < 3; ++c) {
          for (unsigned spin = 0; spin < SPIN_CAP; ++spin) {
            CFENCE();
            v4u tv[8];
#pragma unroll
            for (int i = 0; i < 8; ++i) tv[i] = __builtin_amdgcn_raw_buffer_load_b128(wsr, WS_TAB + c * 8192 + lane * 16, i * 1024, AUX_SC1);
            bool ok = true;
#pragma unroll
            for (int i = 0; i < 8; ++i) {
              ok = ok & (tv[i][1] == 1u) & (tv[i][3] == 1u);
              *(LAS v2u*)(lds + L_T + c * 4096 + i * 512 + lane * 8) = v2u{tv[i][0], tv[i][2]};
            }
            if (__all(ok)) break;
            __builtin_amdgcn_s_sleep(16);
          }
        }
        LGKM0();
        if (lane == 0) __hip_atomic_store(flags + F_FIN, 1u, RLX_WG);
        asm volatile(".Lpf_end_a:");
      }
    } else if (wave == 2) {
      wait_until(tstart, PART);
      const v4u s1 = make_srd(w1, FF * 2), s2 = make_srd(b1, FF * 2), s3 = make_srd(w2, FF * 2);
#pragma unroll
      for (int i = 0; i < 3; ++i) {
        dma16<false>(lds0 + L_P + i * 1024, voff, s1, i * 1024);
        dma16<false>(lds0 + L_P + 3584 + i * 1024, voff, s2, i * 1024);
        dma16<false>(lds0 + L_P + 7168 + i * 1024, voff, s3, i * 1024);
      }
      if (lane < 32) {
        dma16<false>(lds0 + L_P + 3072, voff, s1, 3072);
        dma16<false>(lds0 + L_P + 3584 + 3072, voff, s2, 3072);
        dma16<false>(lds0 + L_P + 7168 + 3072, voff, s3, 3072);
      }
      const unsigned b2u = b2[0];
      if (lane == 0) flags[F_B2] = b2u;
      asm volatile("s_waitcnt vmcnt(0) lgkmcnt(0)" ::: "memory");
      if (lane == 0) __hip_atomic_store(flags + F_P, 1u, RLX_WG);
    }
    if (wave == 3) {
      wait_until(tstart, HOTPF);
      const v4u hs = make_srd(wo, HQ * DH * DM * 2);
      if (lane < 16)
        asm volatile("s_mov_b32 m0, %0\n\ts_nop 0\n\tbuffer_load_dword %1, %2, 0 offen lds\n\ts_waitcnt vmcnt(0)"
                     :: "s"(lds0 + L_A), "v"(((((bid + 3) & 7) * 512 + (bid >> 3) * 16 + lane) << 10) + 384), "s"(hs) : "memory");
    }
    if (wave != 1) {
      __builtin_amdgcn_s_setprio(0);
      lds_wait_ne0(flags + F_P);
      lds_wait_ne0(flags + F_CLR);
      const int p0 = wave == 0 ? 0 : 2 * wave - 2;
#pragma unroll 1
      for (int ps = p0; ps < p0 + 2; ++ps) {
        const unsigned bits = (unsigned)((wgl & 127) * 24 + ps * 4 + (TAB_E0 << 7)) | (unsigned)((wgl >> 7) << 15);
        f32x2 t[2], a[2];
#pragma unroll
        for (int p = 0; p < 2; ++p) t[p] = f32x2{bflo(bits + 2 * p), bflo(bits + 2 * p + 1)};
        ffn_eval<2>(lds, lane, t, a);
        const unsigned k0 = cvtpk(wave_sum(a[0][0]), wave_sum(a[0][1])), k1 = cvtpk(wave_sum(a[1][0]), wave_sum(a[1][1]));
        __builtin_amdgcn_s_setprio(3);
        if (lane == 0) __builtin_amdgcn_raw_buffer_store_b128(v4u{k0, 1u, k1, 1u}, wsr, WS_TAB + wgl * 96 + ps * 16, 0, AUX_SC1);
        __builtin_amdgcn_s_setprio(0);
      }
      __builtin_amdgcn_s_setprio(3);
    }
    if (wave == 0) {
      while (__hip_atomic_load(flags + F_CNT, RLX_WG) != 8u) __builtin_amdgcn_s_sleep(2);
      CFENCE();
      const int hd = lane & 3;
      float M = -INFINITY, L = 0.f;
#pragma unroll
      for (int w = 0; w < 8; ++w) M = fmaxf(M, ((LAS float*)(lds + L_ML))[w * 8 + hd]);
#pragma unroll
      for (int w = 0; w < 8; ++w) L = fmaf(__builtin_amdgcn_exp2f(((LAS float*)(lds + L_ML))[w * 8 + hd] - M), ((LAS float*)(lds + L_ML))[w * 8 + 4 + hd], L);
      if (lane < 4) __builtin_amdgcn_raw_buffer_store_b128(v4u{__float_as_uint(M), 1u, __float_as_uint(L), 1u}, wsr, WS_STAT + wgl * 128 + lane * 16, 0, AUX_SC1);
      v4u sv = {0u, 1u, 0u, 1u};
      for (unsigned spin = 0; spin < SPIN_CAP; ++spin) {
        __builtin_amdgcn_s_sleep(8);
        CFENCE();
        if (lane < 16) sv = __builtin_amdgcn_raw_buffer_load_b128(wsr, WS_STAT + ((b << 5) | ((lane >> 2) << 3) | kvh) * 128 + hd * 16, 0, AUX_SC1);
        if (__all((sv[1] == 1u) & (sv[3] == 1u))) break;
      }
      const float ms = __uint_as_float(sv[0]);
      float Mg = fmaxf(ms, dpp<0x124>(ms)); Mg = fmaxf(Mg, dpp<0x128>(Mg));
      float Lg = __builtin_amdgcn_exp2f(ms - Mg) * __uint_as_float(sv[2]);
      Lg += dpp<0x128>(Lg); Lg += dpp<0x124>(Lg);
      if (lane < 4) ((LAS float*)(lds + L_Z))[lane] = Mg + __builtin_amdgcn_logf(Lg);
      LGKM0();
      if (lane == 0) __hip_atomic_store(flags + F_Z, 1u, RLX_WG);
    }
  } else {
    const int sw = wave - 4, tid = threadIdx.x - 256;
    const __amdgpu_buffer_rsrc_t vrs = __builtin_amdgcn_make_buffer_rsrc((void*)vc, 0, 64 << 20, 0x00020000);
    const int vvo = kvo + voff;
    const v4u wsd = make_srd(wo, HQ * DH * DM * 2);
    const int wo_ = (kvh * 512 + sw + ((lane >> 2) & 3) * 128 + (lane >> 4) * 8) * 1024 + cj * 64 + (lane & 3) * 16;
#define WPIECE(k) do { dma16<true>(lds0 + L_W + sw * 4096 + (k) * 1024, wo_, wsd, (k) * 32 * 1024); SB(); } while (0)
#define VLOAD(i) do { WAITV(VW - 1); pace<sched(32 + (i))>(tb); vf[i] = __builtin_amdgcn_raw_buffer_load_b128(vrs, vvo, (i) * 8192, AUX_NT); SB(); \
    if (32 + (i) == WP0) WPIECE(0); if (32 + (i) == WP1) WPIECE(1); if (32 + (i) == WP2) WPIECE(2); if (32 + (i) == WP3) WPIECE(3); } while (0)
    const __amdgpu_buffer_rsrc_t krs = __builtin_amdgcn_make_buffer_rsrc((void*)kc, 0, 64 << 20, 0x00020000);
    v4u kf[32], vf[32];
    __builtin_amdgcn_s_setprio(2);
    unsigned tb = tstart + (STG * sw >> 4);
    static_for<KFIRST, 32>([&](auto ic) {
      constexpr int i = ic.value;
      WAITV(KW - 1);
      pace<sched(i)>(tb);
      if (i == XWSIG) { WAITV(XWSIG - KFIRST); if (lane == 0) __hip_atomic_fetch_add(flags + F_XW, 256u, RLX_WG); }
      KLOAD(i);
      if (i == WP0) WPIECE(0);
      if (i == WP1) WPIECE(1);
    });
    static_for<0, XV>([&](auto ic) { VLOAD(ic.value); });
    lds_wait_ne0(flags + F_Q);
    const unsigned tb0 = __builtin_amdgcn_readfirstlane(flags[F_TB]);
    tb = tb0 + TBO + (STG * sw >> 4);
    SB();
    const v4u qf = *(LAS v4u*)(lds + L_Q + (r & 3) * 256 + g * 64 + (r >> 2) * 16);
    kf[0] = *(LAS v4u*)(lds + L_K0 + sw * 2048 + voff);
    kf[1] = *(LAS v4u*)(lds + L_K0 + sw * 2048 + 1024 + voff);
    float s[32];
    static_for<0, 32>([&](auto ic) {
      constexpr int i = ic.value;
      const f32x4 a = mfma16(kf[i], qf, f32x4{0.f, 0.f, 0.f, 0.f});
      float t_ = a[0] + dpp<0x104>(a[1]); asm volatile("" : "+v"(t_));
      t_ += dpp<0x108>(a[2]); asm volatile("" : "+v"(t_));
      s[i] = t_ + dpp<0x10C>(a[3]); asm volatile("" : "+v"(s[i]));
      if constexpr ((i & 3) == 3) { SB(); VLOAD(XV + (i >> 2)); }
    });
    SB();
    float m = s[0];
#pragma unroll
    for (int i = 1; i < 32; ++i) m = fmaxf(m, s[i]);
    m = xrow_max(m) * SCALE_L2;
    asm volatile("" : "+v"(m));
    SB(); VLOAD(XV + 8);
    v4u pf[4];
#define SOFTMAX(G, mm) do { float p_[8]; \
    _Pragma("unroll") for (int j = 0; j < 8; ++j) p_[j] = __builtin_amdgcn_exp2f(fmaf(s[8 * (G) + j], SCALE_L2, -(mm))); \
    pf[G] = v4u{cvtpk(p_[0], p_[1]), cvtpk(p_[2], p_[3]), cvtpk(p_[4], p_[5]), cvtpk(p_[6], p_[7])}; } while (0)
    {
      SOFTMAX(0, m); SOFTMAX(1, m);
      asm volatile("" : "+v"(pf[0]), "+v"(pf[1]));
      SB(); VLOAD(XV + 9);
      SOFTMAX(2, m); SOFTMAX(3, m);
      asm volatile("" : "+v"(pf[2]), "+v"(pf[3]));
      SB(); VLOAD(XV + 10);
      const v4u ones = {0x3f803f80u, 0x3f803f80u, 0x3f803f80u, 0x3f803f80u};
      f32x4 l4 = {0.f, 0.f, 0.f, 0.f};
#pragma unroll
      for (int G = 0; G < 4; ++G) l4 = mfma16(pf[G], ones, l4);
      if (lane < 4) ((LAS float*)(lds + L_ML))[sw * 8 + lane] = m;
      if (lane == 0) *(LAS f32x4*)(lds + L_ML + sw * 32 + 16) = l4;
      LGKM0();
      if (lane == 0) __hip_atomic_fetch_add(flags + F_CNT, 1u, RLX_WG);
    }
    f32x4 acc[8];
#pragma unroll
    for (int j = 0; j < 8; ++j) acc[j] = f32x4{0.f, 0.f, 0.f, 0.f};
#define PV(G) do { \
    _Pragma("unroll") for (int d = 0; d < 4; ++d) { v4u lo_, hi_; \
      _Pragma("unroll") for (int jj = 0; jj < 4; ++jj) { \
        lo_[jj] = __builtin_amdgcn_perm(vf[8 * (G) + 2 * jj + 1][d], vf[8 * (G) + 2 * jj][d], 0x05040100u); \
        hi_[jj] = __builtin_amdgcn_perm(vf[8 * (G) + 2 * jj + 1][d], vf[8 * (G) + 2 * jj][d], 0x07060302u); } \
      acc[2 * d] = mfma16(pf[G], lo_, acc[2 * d]); acc[2 * d + 1] = mfma16(pf[G], hi_, acc[2 * d + 1]); } SB(); } while (0)
    static_for<XV + 11, 32>([&](auto ic) {
      constexpr int i = ic.value;
      VLOAD(i);
      if constexpr (i == XV + 11) PV(0);
      if constexpr (i == 15 + PVD) PV(1);
      if constexpr (i == 23 + PVD) PV(2);
    });
    v4u wf[2][2];
    {
      const int wtr = L_W + sw * 4096 + (g * 8 + (r >> 2)) * 64 + (r & 3) * 8;
#pragma unroll
      for (int ks = 0; ks < 2; ++ks)
#pragma unroll
        for (int nt = 0; nt < 2; ++nt) {
          const v2u u0 = __builtin_bit_cast(v2u, __builtin_amdgcn_ds_read_tr16_b64_v4i16((LAS v4s*)(lds + wtr + ks * 2048 + nt * 32)));
          const v2u u1 = __builtin_bit_cast(v2u, __builtin_amdgcn_ds_read_tr16_b64_v4i16((LAS v4s*)(lds + wtr + ks * 2048 + 256 + nt * 32)));
          wf[ks][nt] = v4u{u0.x, u0.y, u1.x, u1.y};
          asm volatile("" : "+v"(wf[ks][nt]));
        }
      SB();
    }
    unsigned a_mw = lds0 + L_B + sw * 2048 + voff, a_mr = lds0 + L_B + sw * 256 + voff;
    unsigned a_own = lds0 + L_A + sw * 2304 + me * 144 + lane * 8, a_put = lds0 + L_A + sw * 2304 + (lane >> 3) * 144 + (lane & 7) * 16;
    unsigned a_get = lds0 + L_A + sw * 2304 + r * 144 + g * 16;
    unsigned a_rw = lds0 + L_W + sw * 512 + lane * 4, a_rr = lds0 + L_W + ((g & 1) * 64 + (2 * sw + (g >> 1)) * 16 + r) * 4;
    int o_part = partg + me * 1024 + sw * 128 + lane * 8, o_get = partg + (lane >> 3) * 1024 + sw * 128 + (lane & 7) * 16;
    int o_outp = WS_OUTP + (((bh * 4 + 2 * sw + (g >> 1)) * 8 + kvh) * 512 + cj * 32 + (lane & 31)) * 4, o_fin = WS_OUTP + b * 16384 + tid * 4;
    us* outp = out + b * DM + tid;
    float xres = 0.f, b2v = 0.f;
    if (fin) {
      lds_wait_ne0(flags + F_FIN);
      xres = bflo(((LAS us*)(lds + L_X))[b * 520 + tid]); b2v = bflo(flags[F_B2]);
    }
    asm volatile("" : "+v"(a_mw), "+v"(a_mr), "+v"(a_own), "+v"(a_put), "+v"(a_get), "+v"(a_rw), "+v"(a_rr));
    asm volatile("" : "+v"(o_part), "+v"(o_get), "+v"(o_outp), "+v"(o_fin), "+v"(outp), "+v"(xres), "+v"(b2v));
#define LDSP(T, a) ((LAS T*)(uintptr_t)(a))
    lds_wait_ne0(flags + F_Z);
    SB();
    {
      const f32x4 Z4 = *(LAS f32x4*)(lds + L_Z);
      const f32x4 mo = *(LAS f32x4*)(lds + L_ML + sw * 32);
      const float zl = ((LAS float*)(lds + L_Z))[lane & 3];
      f32x4 f4;
#pragma unroll
      for (int i = 0; i < 4; ++i) f4[i] = __builtin_amdgcn_exp2f(mo[i] - Z4[i]);
#pragma unroll
      for (int j = 0; j < 8; ++j) acc[j] *= f4;
      const float zz = (r < 4) ? zl : m;
      SOFTMAX(3, zz);
      SB();
    }
    {
      v4u lo_[4], hi_[4];
#pragma unroll
      for (int jj = 0; jj < 4; ++jj) {
#pragma unroll
        for (int d = 0; d < 4; ++d) {
          lo_[d][jj] = __builtin_amdgcn_perm(vf[24 + 2 * jj + 1][d], vf[24 + 2 * jj][d], 0x05040100u);
          hi_[d][jj] = __builtin_amdgcn_perm(vf[24 + 2 * jj + 1][d], vf[24 + 2 * jj][d], 0x07060302u);
        }
        SB();
      }
#pragma unroll
      for (int d = 0; d < 4; ++d) { acc[2 * d] = mfma16(pf[3], lo_[d], acc[2 * d]); acc[2 * d + 1] = mfma16(pf[3], hi_[d], acc[2 * d + 1]); }
      SB();
    }
    if (lane < 16) {
#pragma unroll
      for (int j = 0; j < 8; ++j) LDSP(f32x4, a_mw)[j * 16] = acc[j];
    }
    WG_BARRIER();
    if (lane < 16) {
      f32x4 ow[8];
#pragma unroll
      for (int w = 0; w < 8; ++w) ow[w] = LDSP(f32x4, a_mr)[w * 128];
      const f32x4 o = ((ow[0] + ow[1]) + (ow[2] + ow[3])) + ((ow[4] + ow[5]) + (ow[6] + ow[7]));
      const v2u op = {cvtpk(o[0], o[1]), min(cvtpk(o[2], o[3]), EMPTY - 1u)};
      __builtin_amdgcn_raw_buffer_store_b64(op, wsr, o_part, 0, AUX_SC1);
      *LDSP(v2u, a_own) = op;
    }
    SB();
    {
      bool ok0 = (lane >> 3) == me, ok1 = (lane >> 3) + 8 == me;
      pace<sched(63) + H2T>(tb0);
      for (unsigned spin = 0; spin < SPIN_CAP; ++spin) {
        CFENCE();
        const v4u p0 = __builtin_amdgcn_raw_buffer_load_b128(wsr, ok0 ? WS_END : o_get, 0, AUX_SC1);
        const v4u p1 = __builtin_amdgcn_raw_buffer_load_b128(wsr, ok1 ? WS_END : o_get + 8192, 0, AUX_SC1);
        if (!ok0) *LDSP(v4u, a_put) = p0;
        if (!ok1) *LDSP(v4u, a_put + 1152) = p1;
        ok0 = max(p0[1], p0[3]) != EMPTY;
        ok1 = max(p1[1], p1[3]) != EMPTY;
        if (__all(ok0 & ok1)) break;
      }
      f32x4 c0 = {0.f, 0.f, 0.f, 0.f}, c1 = {0.f, 0.f, 0.f, 0.f};
#pragma unroll
      for (int ks = 0; ks < 2; ++ks) {
        const v4u af = LDSP(v4u, a_get)[ks * 4];
        c0 = mfma16(af, wf[ks][0], c0);
        c1 = mfma16(af, wf[ks][1], c1);
      }
      LDSP(float, a_rw)[0] = (c0[0] + c0[1]) + (c0[2] + c0[3]);
      LDSP(float, a_rw)[64] = (c1[0] + c1[1]) + (c1[2] + c1[3]);
    }
    WG_BARRIER();
    if (sw < 2) {
      LAS float* red = LDSP(float, a_rr);
      const float a = ((red[0] + red[128]) + (red[256] + red[384])) + ((red[512] + red[640]) + (red[768] + red[896]));
      __builtin_amdgcn_raw_buffer_store_b32(min(__float_as_uint(a), EMPTY - 1u), wsr, o_outp, 0, AUX_SC1);
    }
    if (fin) {
      unsigned pv[8];
      if (sw >= 2) __builtin_amdgcn_s_sleep(H3E);
      __builtin_amdgcn_s_sleep(H3D);
      for (unsigned spin = 0; spin < SPIN_CAP; ++spin) {
        CFENCE();
#pragma unroll
        for (int k = 0; k < 8; ++k) pv[k] = __builtin_amdgcn_raw_buffer_load_b32(wsr, o_fin, k * 2048, AUX_SC1);
        const unsigned mx = max(max(max(pv[0], pv[1]), max(pv[2], pv[3])), max(max(pv[4], pv[5]), max(pv[6], pv[7])));
        if (__all(mx != EMPTY)) break;
      }
      const float a = ((__uint_as_float(pv[0]) + __uint_as_float(pv[1])) + (__uint_as_float(pv[2]) + __uint_as_float(pv[3]))) +
                      ((__uint_as_float(pv[4]) + __uint_as_float(pv[5])) + (__uint_as_float(pv[6]) + __uint_as_float(pv[7])));
      const float h = rbf(xres + rbf(a));
      float s1 = h, s2 = h * h;
      wave_sum2(s1, s2);
      if ((lane & 47) == 0) ((LAS float*)(lds + L_B))[sw * 2 + g] = s1;
      WG_BARRIER();
      const f32x4 q0 = *(LAS f32x4*)(lds + L_B), q1 = *(LAS f32x4*)(lds + L_B + 16), q2 = *(LAS f32x4*)(lds + L_B + 32), q3 = *(LAS f32x4*)(lds + L_B + 48);
      const f32x4 qs = (q0 + q1) + (q2 + q3);
      const float mean = (qs[0] + qs[2]) * (1.f / DM);
      const float var = fmaxf((qs[1] + qs[3]) * (1.f / DM) - mean * mean, 0.f);
      const float r1 = __builtin_amdgcn_rsqf(var + 1e-5f);
      const float r2 = __builtin_amdgcn_rsqf(fmaf(var * r1, r1, 1e-5f));
      const float t = ((h - mean) * r1) * r2;
      const unsigned tb = cvtpk(t, t) & 0xffffu;
      const unsigned rel = (tb & 0x7fffu) - (unsigned)(TAB_E0 << 7);
      const bool inr = rel < (unsigned)TAB_H;
      float y = bflo(((LAS us*)(lds + L_T))[(inr ? rel : 0u) + (tb >> 15) * TAB_H]);
      u64 miss = __ballot(!inr);
      while (__builtin_expect(miss != 0, 0)) {
        const int l = __builtin_ctzll(miss);
        miss &= miss - 1;
        const float ts = bflo((unsigned)__builtin_amdgcn_readlane((int)tb, l));
        const f32x2 tt[1] = {f32x2{ts, ts}};
        f32x2 aa[1];
        ffn_eval<1>(lds, lane, tt, aa);
        const float ys = rbf(wave_sum(aa[0][0]));
        if (lane == l) y = ys;
      }
      const float ov = h + rbf(y + b2v);
      *(GAS us*)outp = (us)(cvtpk(ov, ov) & 0xffffu);
    }
  }
  asm volatile(".Lpf_end_b:");
}

extern "C" void kernel_launch(void* const* d_in, const int* in_sizes, int n_in, void* d_out, int out_size, void* d_ws, size_t ws_size, hipStream_t stream) {
  fused_kernel<<<256, 768, 0, stream>>>((const us*)d_in[0], (const us*)d_in[1], (const us*)d_in[2], (const us*)d_in[3], (const us*)d_in[4],
                                        (const us*)d_in[6], (const us*)d_in[7], (const us*)d_in[8], (const us*)d_in[9], (us*)d_out, (unsigned char*)d_ws);
}
```

```cpp
#include <hip/hip_runtime.h>
#include <hip/hip_bf16.h>
#include <stdint.h>
#include <type_traits>

#define GAS __attribute__((address_space(1)))
#define LAS __attribute__((address_space(3)))
#define RLX_WG __ATOMIC_RELAXED, __HIP_MEMORY_SCOPE_WORKGROUP
typedef unsigned v4u __attribute__((ext_vector_type(4)));
typedef unsigned v2u __attribute__((ext_vector_type(2)));
typedef float f32x4 __attribute__((ext_vector_type(4)));
typedef float f32x2 __attribute__((ext_vector_type(2)));
typedef short bf16x8 __attribute__((ext_vector_type(8)));
typedef short v4s __attribute__((ext_vector_type(4)));
typedef __bf16 bf16x2_t __attribute__((ext_vector_type(2)));
typedef unsigned long long u64;
typedef unsigned short us;

constexpr int DM = 512, HQ = 32, DH = 128, FF = 1792;
constexpr float LOG2E = 1.4426950408889634f;
constexpr float SCALE_L2 = 0.08838834764831845f * LOG2E;
constexpr unsigned SPIN_CAP = 100000u;
constexpr int AUX_NT = 2, AUX_SC1 = 16;
constexpr unsigned EMPTY = 0xffffffffu;
constexpr int TAB_E0 = 108;
constexpr int TAB_H = 24 * 128;

constexpr int CODEPF = 64;
constexpr int KFIRST = 2;
constexpr int PER16 = 474;
constexpr int LEAD = 2;
constexpr int NE = 24;
constexpr int EX16 = 132;
constexpr int XWSIG = 5;
constexpr int PVD = 5;
constexpr int XV = 2;
constexpr int NNT = 130;
constexpr int KW = 4;
constexpr int VW = 6;
constexpr int PART = 250;
constexpr int QT1 = 480;
constexpr int QSL = 8;
constexpr int TBO = -7;
constexpr int STG = 30;
constexpr int H3D = 4;
constexpr int H3E = 3;
constexpr int H2T = 168;
constexpr int HOTPF = 200;
constexpr int EADJ = 6;
constexpr int ESL = 1;
constexpr int WEV = 3;
constexpr int TABT = 1200;
constexpr int sched(int n) {
  return ((n - LEAD) * PER16 + (n < NE ? n : NE) * EX16) >> 4;
}

constexpr int WS_NK = 0;
constexpr int WS_NB = 1024;
constexpr int WS_STAT = 4096;
constexpr int WS_TAB = WS_STAT + 256 * 128;
constexpr int WS_EM = WS_TAB + 256 * 128;
constexpr int WS_PART = WS_EM + 256 * 512;
constexpr int WS_OUTP = WS_PART + 256 * 1024;
constexpr int WS_QD = WS_OUTP + 64 * 2048;
constexpr int WS_END = WS_QD + 64 * 8192;

constexpr int L_W = 0;
constexpr int L_B = 32768;
constexpr int L_X = L_B + 16384;
constexpr int L_P = L_X + 9472;
constexpr int L_T = L_P + 10752;
constexpr int L_Q = L_T + 16384;
constexpr int L_QO = L_Q + 1024;
constexpr int L_ML = L_QO + 1024;
constexpr int L_Z = L_ML + 256;
constexpr int L_FL = L_Z + 64;
constexpr int L_A = L_FL + 64;
constexpr int L_K0 = L_A + 8 * 2304;
constexpr int L_TOTAL = L_K0 + 8 * 2048;
enum { F_Q = 0, F_CNT = 1, F_QO = 2, F_Z = 3, F_P = 4, F_FIN = 5, F_XW = 6, F_B2 = 7, F_CLR = 8, F_TB = 9, F_W = 10, F_VD = 11 };

__device__ __forceinline__ f32x4 mfma16(v4u a, v4u b, f32x4 c) {
  return __builtin_amdgcn_mfma_f32_16x16x32_bf16(__builtin_bit_cast(bf16x8, a), __builtin_bit_cast(bf16x8, b), c, 0, 0, 0);
}
__device__ __forceinline__ unsigned cvtpk(float lo, float hi) {
  return __builtin_bit_cast(unsigned, __builtin_convertvector(f32x2{lo, hi}, bf16x2_t));
}
__device__ __forceinline__ float bflo(unsigned u) { return __builtin_bit_cast(float, u << 16); }
__device__ __forceinline__ float bfhi(unsigned u) { return __builtin_bit_cast(float, u & 0xffff0000u); }
__device__ __forceinline__ float rbf(float x) { return bflo(cvtpk(x, x)); }
__device__ __forceinline__ float xrow_max(float x) {
  auto s = __builtin_amdgcn_permlane16_swap(__float_as_uint(x), __float_as_uint(x), false, false);
  x = fmaxf(__uint_as_float(s[0]), __uint_as_float(s[1]));
  auto t = __builtin_amdgcn_permlane32_swap(__float_as_uint(x), __float_as_uint(x), false, false);
  return fmaxf(__uint_as_float(t[0]), __uint_as_float(t[1]));
}
__device__ __forceinline__ float xrow_sum(float x) {
  auto s = __builtin_amdgcn_permlane16_swap(__float_as_uint(x), __float_as_uint(x), false, false);
  x = __uint_as_float(s[0]) + __uint_as_float(s[1]);
  auto t = __builtin_amdgcn_permlane32_swap(__float_as_uint(x), __float_as_uint(x), false, false);
  return __uint_as_float(t[0]) + __uint_as_float(t[1]);
}
template <int CTRL> __device__ __forceinline__ float dpp(float x) {
  return __builtin_bit_cast(float, __builtin_amdgcn_mov_dpp(__builtin_bit_cast(int, x), CTRL, 0xf, 0xf, true));
}
__device__ __forceinline__ float wave_sum(float x) {
  x += dpp<0xB1>(x); x += dpp<0x4E>(x); x += dpp<0x141>(x); x += dpp<0x128>(x);
  return xrow_sum(x);
}
__device__ __forceinline__ void wave_sum2(float& x, float& y) {
  x += dpp<0xB1>(x); y += dpp<0xB1>(y); __builtin_amdgcn_sched_barrier(0);
  x += dpp<0x4E>(x); y += dpp<0x4E>(y); __builtin_amdgcn_sched_barrier(0);
  x += dpp<0x141>(x); y += dpp<0x141>(y); __builtin_amdgcn_sched_barrier(0);
  x += dpp<0x128>(x); y += dpp<0x128>(y); __builtin_amdgcn_sched_barrier(0);
  auto s = __builtin_amdgcn_permlane16_swap(__float_as_uint(x), __float_as_uint(y), false, false);
  const float z = __uint_as_float(s[0]) + __uint_as_float(s[1]);
  auto t = __builtin_amdgcn_permlane32_swap(__float_as_uint(z), __float_as_uint(z), false, false);
  x = __uint_as_float(t[0]) + __uint_as_float(t[1]);
  y = x;
}
#define SB() __builtin_amdgcn_sched_barrier(0)
#define CFENCE() asm volatile("" ::: "memory")
#define LGKM0() asm volatile("s_waitcnt lgkmcnt(0)" ::: "memory")
#define WAITV(n) asm volatile("s_waitcnt vmcnt(%0)" :: "i"(n) : "memory")
#define WG_BARRIER() do { asm volatile("s_waitcnt lgkmcnt(0)\n\ts_barrier" ::: "memory"); SB(); } while (0)
__device__ __forceinline__ v4u make_srd(const void* p, unsigned bytes) {
  const u64 a = (u64)p;
  v4u d;
  d[0] = __builtin_amdgcn_readfirstlane((unsigned)a);
  d[1] = __builtin_amdgcn_readfirstlane((unsigned)(a >> 32)) & 0xffffu;
  d[2] = bytes;
  d[3] = 0x00020000u;
  return d;
}
template <bool NT>
__device__ __forceinline__ void dma16(unsigned ldsaddr, int voff, v4u srd, int soff) {
  if (NT) asm volatile("s_mov_b32 m0, %0\n\ts_nop 0\n\tbuffer_load_dwordx4 %1, %2, %3 offen nt lds" :: "s"(ldsaddr), "v"(voff), "s"(srd), "s"(soff) : "memory");
  else asm volatile("s_mov_b32 m0, %0\n\ts_nop 0\n\tbuffer_load_dwordx4 %1, %2, %3 offen lds" :: "s"(ldsaddr), "v"(voff), "s"(srd), "s"(soff) : "memory");
}
__device__ __forceinline__ void lds_wait_ne0(LAS unsigned* f) {
  while (__hip_atomic_load(f, RLX_WG) == 0u) __builtin_amdgcn_s_sleep(1);
  CFENCE();
}
template <int TICKS>
__device__ __forceinline__ void pace(unsigned tstart) {
  if (TICKS > 0)
    asm volatile("s_branch 2f\n1:\n\ts_sleep %2\n2:\n\ts_memrealtime vcc\n\ts_waitcnt lgkmcnt(0)\n\ts_sub_i32 vcc_lo, vcc_lo, %0\n\t"
                 "s_cmpk_lt_i32 vcc_lo, %1\n\ts_cbranch_scc1 1b"
                 :: "s"(tstart), "i"(TICKS - EADJ), "i"(ESL) : "vcc", "scc", "memory");
}

__device__ __forceinline__ void wait_until(unsigned t0, int ticks) {
  while ((int)((unsigned)__builtin_amdgcn_s_memrealtime() - t0) < ticks) __builtin_amdgcn_s_sleep(3);
  CFENCE();
}

template <int NP>
__device__ __forceinline__ void ffn_eval(LAS unsigned char* lds, int lane, const f32x2 (&t)[NP], f32x2 (&acc)[NP]) {
  constexpr float C0 = -2.f * 0.7978845608028654f * LOG2E, C1 = C0 * 0.044715f;
#pragma unroll
  for (int p = 0; p < NP; ++p) acc[p] = f32x2{0.f, 0.f};
#pragma unroll 1
  for (int j = 0; j < 7; ++j) {
    const v2u ua = *(LAS v2u*)(lds + L_P + j * 512 + lane * 8);
    const v2u ub = *(LAS v2u*)(lds + L_P + 3584 + j * 512 + lane * 8);
    const v2u uw = *(LAS v2u*)(lds + L_P + 7168 + j * 512 + lane * 8);
#pragma unroll
    for (int i = 0; i < 4; ++i) {
      const float a = (i & 1) ? bfhi(ua[i >> 1]) : bflo(ua[i >> 1]);
      const float b = (i & 1) ? bfhi(ub[i >> 1]) : bflo(ub[i >> 1]);
      const float w = (i & 1) ? bfhi(uw[i >> 1]) : bflo(uw[i >> 1]);
#pragma unroll
      for (int p = 0; p < NP; ++p) {
        const f32x2 u = __builtin_elementwise_fma(t[p], f32x2{a, a}, f32x2{b, b});
        const f32x2 y = u * __builtin_elementwise_fma(u * u, f32x2{C1, C1}, f32x2{C0, C0});
        f32x2 d = f32x2{__builtin_amdgcn_exp2f(y[0]), __builtin_amdgcn_exp2f(y[1])} + 1.f;
        d = f32x2{__builtin_amdgcn_rcpf(d[0]), __builtin_amdgcn_rcpf(d[1])};
        acc[p] = __builtin_elementwise_fma(u * d, f32x2{w, w}, acc[p]);
      }
    }
  }
}


template <int I, int N, class F> __device__ __forceinline__ void static_for(F&& f) {
  if constexpr (I < N) { f(std::integral_constant<int, I>{}); static_for<I + 1, N>(f); }
}

__global__ __launch_bounds__(768, 3) void fused_kernel(const us* __restrict__ x, const us* __restrict__ kc, const us* __restrict__ vc,
                                                       const us* __restrict__ wq, const us* __restrict__ wo, const us* __restrict__ w1,
                                                       const us* __restrict__ b1, const us* __restrict__ w2, const us* __restrict__ b2,
                                                       us* __restrict__ out, unsigned char* ws) {
  __shared__ __attribute__((aligned(16))) unsigned char smem[L_TOTAL];
  LAS unsigned char* lds = (LAS unsigned char*)smem;
  const unsigned lds0 = (unsigned)(uintptr_t)lds;
  const int lane = threadIdx.x & 63, wave = __builtin_amdgcn_readfirstlane(threadIdx.x >> 6), g = lane >> 4, r = lane & 15;
  LAS unsigned* flags = (LAS unsigned*)(lds + L_FL);
  asm volatile(".Lpf_begin:");
  const unsigned tstart = (unsigned)__builtin_amdgcn_s_memrealtime();
  asm volatile("" :: "s"(x), "s"(kc), "s"(vc), "s"(wq), "s"(ws));
  const int bid = blockIdx.x, sp = (bid >> 3) & 3, b = ((bid >> 3) & 4) | (bid >> 6), kvh = (bid + 3 * (bid >> 6)) & 7;
  const int voff = lane * 16;
  const int kvo = (((b * 8 + kvh) * 4 + sp) << 18) + ((wave - 4) << 10);
  const int koff = kvo + (r >> 2) * 256 + g * 64 + (r & 3) * 16;
#define KLOAD(i) do { kf[i] = __builtin_amdgcn_raw_buffer_load_b128(krs, koff, (i) * 8192, AUX_NT); SB(); } while (0)
  const __amdgpu_buffer_rsrc_t wsr = __builtin_amdgcn_make_buffer_rsrc((void*)ws, 0, WS_END, 0x00020000);
  const int jk = sp * 8 + b;
  const int jb = sp * 8 + kvh;
  if (wave < 4) {
    const v4u srd = make_srd(x, 8 * DM * 2);
    dma16<false>(lds0 + L_X + wave * 2080, voff, srd, wave * 2048);
    dma16<false>(lds0 + L_X + wave * 2080 + 1040, voff, srd, wave * 2048 + 1024);
    if (wave == 1 && lane < 2)
      __builtin_amdgcn_raw_buffer_store_b32(tstart, wsr, lane ? WS_NB + b * 128 + jb * 4 : WS_NK + kvh * 128 + jk * 4, 0, AUX_SC1);
    if (bid < CODEPF && wave == 3) {
      u64 lo; unsigned back, la, lb;
      asm volatile("s_getpc_b64 %0\n.Lpf_here:\n\ts_mov_b32 %1, .Lpf_here-.Lpf_begin\n\ts_mov_b32 %2, .Lpf_end_a-.Lpf_begin\n\ts_mov_b32 %3, .Lpf_end_b-.Lpf_begin"
                   : "=s"(lo), "=s"(back), "=s"(la), "=s"(lb));
      const v4u cs = make_srd((const void*)(lo - back), la > lb ? la : lb);
      if (lane < 24) asm volatile("s_mov_b32 m0, %0\n\ts_nop 0\n\tbuffer_load_dword %1, %2, 0 offen lds" :: "s"(lds0 + L_T), "v"((lane + 24 * ((bid >> 3) & 7)) * 128), "s"(cs) : "memory");
    }
    if (threadIdx.x < 16) flags[threadIdx.x] = 0u;
    SB();
  } else {
    const v4u ksd = make_srd(kc, 64 << 20);
    dma16<true>(lds0 + L_K0 + (wave - 4) * 2048, koff, ksd, 0);
    dma16<true>(lds0 + L_K0 + (wave - 4) * 2048 + 1024, koff, ksd, 8192);
    const v4u srd = make_srd(wq, DM * HQ * DH * 2);
    const int vo = (sp * 128 + (wave - 4) * 16 + (lane >> 3)) * 8192 + kvh * 1024 + b * 128 + (lane & 7) * 16;
    dma16<true>(lds0 + L_B + (wave - 4) * 2048, vo, srd, 0);
    dma16<true>(lds0 + L_B + (wave - 4) * 2048 + 1024, vo, srd, 8 * 8192);
    SB();
  }
  WG_BARRIER();
  const int wgl = (b << 5) | jb;
  const int cj = (b & 3) * 4 + sp, bh = b >> 2;
  const int partg = WS_PART + (kvh * 2 + bh) * 16384;
  const int me = (b & 3) * 4 + sp;
  const int fjb = (b + 3 * (b & 3)) & 7;
  const bool fin = jb == fjb;

  if (wave < 4) {
    __builtin_amdgcn_s_setprio(3);
    if (wave != 1) {
      WAITV(0);
      if (lane == 0) __hip_atomic_fetch_add(flags + F_XW, 1u, RLX_WG);
    }
    if (wave == 0) {
      *(LAS v4u*)(lds + L_X + 8 * 1040 + lane * 16) = v4u{0x3f803f80u, 0x3f803f80u, 0x3f803f80u, 0x3f803f80u};
      while ((__hip_atomic_load(flags + F_XW, RLX_WG) & 255u) != 4u) __builtin_amdgcn_s_sleep(1);
      CFENCE();
      const int xo = L_X + (r < 8 ? r : 8) * 1040 + g * 16;
      f32x4 d2a = {0.f, 0.f, 0.f, 0.f}, d2b = {0.f, 0.f, 0.f, 0.f};
#pragma unroll
      for (int s = 0; s < 16; s += 2) {
        const v4u xa = *(LAS v4u*)(lds + xo + s * 64), xb = *(LAS v4u*)(lds + xo + s * 64 + 64);
        d2a = mfma16(xa, xa, d2a); d2b = mfma16(xb, xb, d2b);
      }
      d2a += d2b;
      const float dg = (r & 2) ? ((r & 1) ? d2a[3] : d2a[2]) : ((r & 1) ? d2a[1] : d2a[0]);
      const float sxx = xrow_sum(g == (r >> 2) ? dg : 0.f);
      const float mean = xrow_sum(g == 2 ? d2a[0] : 0.f) * (1.f / DM);
      float rstd = rsqrtf(fmaxf(sxx * (1.f / DM) - mean * mean, 0.f) + 1e-5f);
      v4u xb[4];
#pragma unroll
      for (int s = 0; s < 4; ++s) { xb[s] = *(LAS v4u*)(lds + xo + sp * 256 + s * 64); asm volatile("" : "+v"(xb[s])); }
      asm volatile("" : "+v"(rstd));
      while (__hip_atomic_load(flags + F_XW, RLX_WG) != 4u + 8u * 256u) __builtin_amdgcn_s_sleep(1);
      CFENCE();
      f32x4 d[4];
#pragma unroll
      for (int nt = 0; nt < 4; ++nt) d[nt] = f32x4{0.f, 0.f, 0.f, 0.f};
      const int troff = L_B + (8 * g + (r >> 2)) * 128 + (r & 3) * 8;
#pragma unroll
      for (int s = 0; s < 4; ++s)
#pragma unroll
        for (int nt = 0; nt < 4; ++nt) {
          const v2u u0 = __builtin_bit_cast(v2u, __builtin_amdgcn_ds_read_tr16_b64_v4i16((LAS v4s*)(lds + troff + s * 4096 + nt * 32)));
          const v2u u1 = __builtin_bit_cast(v2u, __builtin_amdgcn_ds_read_tr16_b64_v4i16((LAS v4s*)(lds + troff + s * 4096 + 512 + nt * 32)));
          d[nt] = mfma16(v4u{u0.x, u0.y, u1.x, u1.y}, xb[s], d[nt]);
        }
#pragma unroll
      for (int nt = 0; nt < 4; ++nt) {
        float qv[4];
#pragma unroll
        for (int i = 0; i < 4; ++i) qv[i] = (d[nt][i] - mean * dpp<0x158>(d[nt][i])) * rstd;
        if (r < 8) *(LAS v2u*)(lds + L_QO + r * 128 + nt * 32 + g * 8) = v2u{cvtpk(qv[0], qv[1]), cvtpk(qv[2], qv[3])};
      }
      LGKM0();
      if (lane == 0) __hip_atomic_store(flags + F_QO, 1u, RLX_WG);
    } else if (wave == 1) {
      const int emo = WS_EM + wgl * 512 + lane * 8;
      const int qdo = WS_QD + (kvh * 8 + g) * 8192 + jk * 256 + r * 16;
      const int nno = lane < 32 ? WS_NK + kvh * 128 + lane * 4 : WS_NB + b * 128 + (lane - 32) * 4;
      const int eco = WS_EM + (lane < 32 ? ((((lane & 7) << 5) | (lane & 24) | kvh) * 64 + jk) : (((b << 5) | (lane - 32)) * 64 + 32 + jb)) * 8;
      const v4u e4 = {EMPTY, EMPTY, EMPTY, EMPTY};
      __builtin_amdgcn_raw_buffer_store_b128(e4, wsr, partg + me * 1024 + lane * 16, 0, AUX_SC1);
      if (lane < 42)
        __builtin_amdgcn_raw_buffer_store_b128(e4, wsr, (lane < 32 ? WS_OUTP + (b * 8 + kvh) * 2048 + sp * 512 : lane < 36 ? WS_STAT - 512 + wgl * 128 : WS_TAB - 576 + wgl * 96) + lane * 16, 0, AUX_SC1);
      SB();
      v2u em = __builtin_amdgcn_raw_buffer_load_b64(wsr, emo, 0, AUX_SC1);
      v4u s0 = __builtin_amdgcn_raw_buffer_load_b128(wsr, qdo, 0, AUX_SC1);
      v4u s1 = __builtin_amdgcn_raw_buffer_load_b128(wsr, qdo, 4 * 8192, AUX_SC1);
      SB();
      WAITV(6);
      if (lane == 0) __hip_atomic_fetch_add(flags + F_XW, 1u, RLX_WG);
      wait_until(tstart, NNT);
      unsigned nn = __builtin_amdgcn_raw_buffer_load_b32(wsr, nno, 0, AUX_SC1);
      asm volatile("s_waitcnt vmcnt(0)" : "+v"(em), "+v"(s0), "+v"(s1), "+v"(nn) :: "memory");
      unsigned nonce = tstart;
      while (__any((em[1] == nonce) | (s0[1] == nonce) | (s0[3] == nonce) | (s1[1] == nonce) | (s1[3] == nonce))) ++nonce;
      if (nonce != tstart) {
        if (lane < 2) __builtin_amdgcn_raw_buffer_store_b32(nonce, wsr, lane ? WS_NB + b * 128 + jb * 4 : WS_NK + kvh * 128 + jk * 4, 0, AUX_SC1);
        WAITV(0);
      }
      if (lane < 32) __builtin_amdgcn_raw_buffer_store_b64(v2u{nonce, nn}, wsr, eco, 0, AUX_SC1);
      if (lane == 0) __hip_atomic_store(flags + F_CLR, 1u, RLX_WG);
      unsigned cand[8];
#pragma unroll
      for (int i = 0; i < 8; ++i) cand[i] = __builtin_amdgcn_ds_bpermute((4 * i + g) * 4, nn);
      lds_wait_ne0(flags + F_QO);
      {
        const v2u q0 = *(LAS v2u*)(lds + L_QO + lane * 8), q1 = *(LAS v2u*)(lds + L_QO + 512 + lane * 8);
        __builtin_amdgcn_raw_buffer_store_b128(v4u{q0.x, nonce, q0.y, nonce}, wsr, qdo, 0, AUX_SC1);
        __builtin_amdgcn_raw_buffer_store_b128(v4u{q1.x, nonce, q1.y, nonce}, wsr, qdo, 4 * 8192, AUX_SC1);
      }
      const int qio = WS_QD + (kvh * 8 + b) * 8192 + lane * 16;
      v4u gq[8];
#pragma unroll
      for (int i = 0; i < 8; ++i) gq[i] = v4u{0u, ~cand[i], 0u, 0u};
      wait_until(tstart, QT1);
      for (unsigned spin = 0; spin < SPIN_CAP; ++spin) {
        CFENCE();
        const unsigned n2 = __builtin_amdgcn_raw_buffer_load_b32(wsr, nno, 0, AUX_SC1);
        if (lane < 32) em = __builtin_amdgcn_raw_buffer_load_b64(wsr, emo, 0, AUX_SC1);
#pragma unroll
        for (int i = 0; i < 8; ++i)
          if ((gq[i][1] != cand[i]) | (gq[i][3] != cand[i])) gq[i] = __builtin_amdgcn_raw_buffer_load_b128(wsr, qio, i * 1024, AUX_SC1);
        if (__any(n2 != nn)) {
          if (n2 != nn && lane < 32) __builtin_amdgcn_raw_buffer_store_b64(v2u{nonce, n2}, wsr, eco, 0, AUX_SC1);
          nn = n2;
#pragma unroll
          for (int i = 0; i < 8; ++i) cand[i] = __builtin_amdgcn_ds_bpermute((4 * i + g) * 4, nn);
        }
        bool ok = (lane >= 32) | ((em[1] == nonce) & (em[0] == nn));
#pragma unroll
        for (int i = 0; i < 8; ++i) ok = ok & (gq[i][1] == cand[i]) & (gq[i][3] == cand[i]);
        if (__all(ok)) break;
        __builtin_amdgcn_s_sleep(QSL);
      }
#pragma unroll
      for (int hf = 0; hf < 2; ++hf) {
        float q0 = 0.f, q1 = 0.f, q2 = 0.f, q3 = 0.f;
#pragma unroll
        for (int kq = 0; kq < 4; ++kq) {
          const v4u t4 = gq[2 * kq + hf];
          q0 += bflo(t4[0]); q1 += bfhi(t4[0]); q2 += bflo(t4[2]); q3 += bfhi(t4[2]);
        }
        *(LAS v2u*)(lds + L_Q + hf * 512 + lane * 8) = v2u{cvtpk(q0, q1), cvtpk(q2, q3)};
      }
      {
        const int df = (int)(nn - tstart);
        const bool use = lane >= 32 && df > -256 && df < 256;
        const float sd = wave_sum(use ? (float)df : 0.f), cn = wave_sum(use ? 1.f : 0.f);
        if (lane == 0) flags[F_TB] = tstart + (unsigned)(int)(sd * __builtin_amdgcn_rcpf(fmaxf(cn, 1.f)));
      }
      LGKM0();
      if (lane == 0) __hip_atomic_store(flags + F_Q, 1u, RLX_WG);
      const bool r2 = lane >= 32 && (fin || lane - 32 == fjb);
      if (r2) __builtin_amdgcn_raw_buffer_store_b64(v2u{nonce, nn}, wsr, eco, 0, AUX_SC1);
      for (unsigned spin = 0; spin < SPIN_CAP; ++spin) {
        __builtin_amdgcn_s_sleep(48);
        CFENCE();
        unsigned n2 = nn;
        if (r2) {
          n2 = __builtin_amdgcn_raw_buffer_load_b32(wsr, nno, 0, AUX_SC1);
          em = __builtin_amdgcn_raw_buffer_load_b64(wsr, emo, 0, AUX_SC1);
          if (n2 != nn) __builtin_amdgcn_raw_buffer_store_b64(v2u{nonce, n2}, wsr, eco, 0, AUX_SC1);
        }
        nn = n2;
        if (__all(!r2 | ((em[1] == nonce) & (em[0] == nn)))) break;
      }
      if (fin) {
        wait_until(tstart, TABT);
#pragma unroll 1
        for (int c = 0; c < 3; ++c) {
          for (unsigned spin = 0; spin < SPIN_CAP; ++spin) {
            CFENCE();
            v4u tv[8];
#pragma unroll
            for (int i = 0; i < 8; ++i) tv[i] = __builtin_amdgcn_raw_buffer_load_b128(wsr, WS_TAB + c * 8192 + lane * 16, i * 1024, AUX_SC1);
            bool ok = true;
#pragma unroll
            for (int i = 0; i < 8; ++i) {
              ok = ok & (tv[i][1] == 1u) & (tv[i][3] == 1u);
              *(LAS v2u*)(lds + L_T + c * 4096 + i * 512 + lane * 8) = v2u{tv[i][0], tv[i][2]};
            }
            if (__all(ok)) break;
            __builtin_amdgcn_s_sleep(16);
          }
        }
        LGKM0();
        if (lane == 0) __hip_atomic_store(flags + F_FIN, 1u, RLX_WG);
        asm volatile(".Lpf_end_a:");
      }
    } else if (wave == 2) {
      wait_until(tstart, PART);
      const v4u s1 = make_srd(w1, FF * 2), s2 = make_srd(b1, FF * 2), s3 = make_srd(w2, FF * 2);
#pragma unroll
      for (int i = 0; i < 3; ++i) {
        dma16<false>(lds0 + L_P + i * 1024, voff, s1, i * 1024);
        dma16<false>(lds0 + L_P + 3584 + i * 1024, voff, s2, i * 1024);
        dma16<false>(lds0 + L_P + 7168 + i * 1024, voff, s3, i * 1024);
      }
      if (lane < 32) {
        dma16<false>(lds0 + L_P + 3072, voff, s1, 3072);
        dma16<false>(lds0 + L_P + 3584 + 3072, voff, s2, 3072);
        dma16<false>(lds0 + L_P + 7168 + 3072, voff, s3, 3072);
      }
      const unsigned b2u = b2[0];
      if (lane == 0) flags[F_B2] = b2u;
      asm volatile("s_waitcnt vmcnt(0) lgkmcnt(0)" ::: "memory");
      if (lane == 0) __hip_atomic_store(flags + F_P, 1u, RLX_WG);
    }
    if (wave == 3) {
      wait_until(tstart, HOTPF);
      const v4u hs = make_srd(wo, HQ * DH * DM * 2);
      if (lane < 16)
        asm volatile("s_mov_b32 m0, %0\n\ts_nop 0\n\tbuffer_load_dword %1, %2, 0 offen lds\n\ts_waitcnt vmcnt(0)"
                     :: "s"(lds0 + L_A), "v"(((((bid + 3) & 7) * 512 + (bid >> 3) * 16 + lane) << 10) + 384), "s"(hs) : "memory");
    }
    if (wave != 1) {
      __builtin_amdgcn_s_setprio(0);
      lds_wait_ne0(flags + F_P);
      lds_wait_ne0(flags + F_CLR);
      const int p0 = wave == 0 ? 0 : 2 * wave - 2;
#pragma unroll 1
      for (int ps = p0; ps < p0 + 2; ++ps) {
        const unsigned bits = (unsigned)((wgl & 127) * 24 + ps * 4 + (TAB_E0 << 7)) | (unsigned)((wgl >> 7) << 15);
        f32x2 t[2], a[2];
#pragma unroll
        for (int p = 0; p < 2; ++p) t[p] = f32x2{bflo(bits + 2 * p), bflo(bits + 2 * p + 1)};
        ffn_eval<2>(lds, lane, t, a);
        const unsigned k0 = cvtpk(wave_sum(a[0][0]), wave_sum(a[0][1])), k1 = cvtpk(wave_sum(a[1][0]), wave_sum(a[1][1]));
        __builtin_amdgcn_s_setprio(3);
        if (lane == 0) __builtin_amdgcn_raw_buffer_store_b128(v4u{k0, 1u, k1, 1u}, wsr, WS_TAB + wgl * 96 + ps * 16, 0, AUX_SC1);
        __builtin_amdgcn_s_setprio(0);
      }
      __builtin_amdgcn_s_setprio(3);
    }
    if (wave == 3) {
      const v4u wsd = make_srd(wo, HQ * DH * DM * 2);
      const int wo_ = (kvh * 512 + ((lane >> 2) & 3) * 128 + (lane >> 4) * 8) * 1024 + cj * 64 + (lane & 3) * 16;
      __builtin_amdgcn_s_setprio(0);
      while (__hip_atomic_load(flags + F_VD, RLX_WG) != 8u) __builtin_amdgcn_s_sleep(WEV);
      CFENCE();
#pragma unroll 1
      for (int u = 0; u < 8; ++u) {
#pragma unroll
        for (int k = 0; k < 4; ++k) dma16<true>(lds0 + L_W + u * 4096 + k * 1024, wo_, wsd, u * 1024 + k * 32 * 1024);
      }
      __builtin_amdgcn_s_barrier();
      WAITV(0);
      if (lane == 0) __hip_atomic_store(flags + F_W, 1u, RLX_WG);
    }
    if (wave == 0) {
      while (__hip_atomic_load(flags + F_CNT, RLX_WG) != 8u) __builtin_amdgcn_s_sleep(2);
      CFENCE();
      const int hd = lane & 3;
      float M = -INFINITY, L = 0.f;
#pragma unroll
      for (int w = 0; w < 8; ++w) M = fmaxf(M, ((LAS float*)(lds + L_ML))[w * 8 + hd]);
#pragma unroll
      for (int w = 0; w < 8; ++w) L = fmaf(__builtin_amdgcn_exp2f(((LAS float*)(lds + L_ML))[w * 8 + hd] - M), ((LAS float*)(lds + L_ML))[w * 8 + 4 + hd], L);
      if (lane < 4) __builtin_amdgcn_raw_buffer_store_b128(v4u{__float_as_uint(M), 1u, __float_as_uint(L), 1u}, wsr, WS_STAT + wgl * 128 + lane * 16, 0, AUX_SC1);
      v4u sv = {0u, 1u, 0u, 1u};
      for (unsigned spin = 0; spin < SPIN_CAP; ++spin) {
        __builtin_amdgcn_s_sleep(8);
        CFENCE();
        if (lane < 16) sv = __builtin_amdgcn_raw_buffer_load_b128(wsr, WS_STAT + ((b << 5) | ((lane >> 2) << 3) | kvh) * 128 + hd * 16, 0, AUX_SC1);
        if (__all((sv[1] == 1u) & (sv[3] == 1u))) break;
      }
      const float ms = __uint_as_float(sv[0]);
      float Mg = fmaxf(ms, dpp<0x124>(ms)); Mg = fmaxf(Mg, dpp<0x128>(Mg));
      float Lg = __builtin_amdgcn_exp2f(ms - Mg) * __uint_as_float(sv[2]);
      Lg += dpp<0x128>(Lg); Lg += dpp<0x124>(Lg);
      if (lane < 4) ((LAS float*)(lds + L_Z))[lane] = Mg + __builtin_amdgcn_logf(Lg);
      LGKM0();
      if (lane == 0) __hip_atomic_store(flags + F_Z, 1u, RLX_WG);
    }
  } else {
    const int sw = wave - 4, tid = threadIdx.x - 256;
    const __amdgpu_buffer_rsrc_t vrs = __builtin_amdgcn_make_buffer_rsrc((void*)vc, 0, 64 << 20, 0x00020000);
    const int vvo = kvo + voff;
#define VLOAD(i) do { WAITV(VW - 1); pace<sched(32 + (i))>(tb); vf[i] = __builtin_amdgcn_raw_buffer_load_b128(vrs, vvo, (i) * 8192, AUX_NT); SB(); } while (0)
    const __amdgpu_buffer_rsrc_t krs = __builtin_amdgcn_make_buffer_rsrc((void*)kc, 0, 64 << 20, 0x00020000);
    v4u kf[32], vf[32];
    __builtin_amdgcn_s_setprio(2);
    unsigned tb = tstart + (STG * sw >> 4);
    static_for<KFIRST, 32>([&](auto ic) {
      constexpr int i = ic.value;
      WAITV(KW - 1);
      pace<sched(i)>(tb);
      if (i == XWSIG) { WAITV(XWSIG - KFIRST); if (lane == 0) __hip_atomic_fetch_add(flags + F_XW, 256u, RLX_WG); }
      KLOAD(i);
    });
    static_for<0, XV>([&](auto ic) { VLOAD(ic.value); });
    lds_wait_ne0(flags + F_Q);
    const unsigned tb0 = __builtin_amdgcn_readfirstlane(flags[F_TB]);
    tb = tb0 + TBO + (STG * sw >> 4);
    SB();
    const v4u qf = *(LAS v4u*)(lds + L_Q + (r & 3) * 256 + g * 64 + (r >> 2) * 16);
    kf[0] = *(LAS v4u*)(lds + L_K0 + sw * 2048 + voff);
    kf[1] = *(LAS v4u*)(lds + L_K0 + sw * 2048 + 1024 + voff);
    float s[32];
    static_for<0, 32>([&](auto ic) {
      constexpr int i = ic.value;
      const f32x4 a = mfma16(kf[i], qf, f32x4{0.f, 0.f, 0.f, 0.f});
      float t_ = a[0] + dpp<0x104>(a[1]); asm volatile("" : "+v"(t_));
      t_ += dpp<0x108>(a[2]); asm volatile("" : "+v"(t_));
      s[i] = t_ + dpp<0x10C>(a[3]); asm volatile("" : "+v"(s[i]));
      if constexpr ((i & 3) == 3) { SB(); VLOAD(XV + (i >> 2)); }
    });
    SB();
    float m = s[0];
#pragma unroll
    for (int i = 1; i < 32; ++i) m = fmaxf(m, s[i]);
    m = xrow_max(m) * SCALE_L2;
    asm volatile("" : "+v"(m));
    SB(); VLOAD(XV + 8);
    v4u pf[4];
#define SOFTMAX(G, mm) do { float p_[8]; \
    _Pragma("unroll") for (int j = 0; j < 8; ++j) p_[j] = __builtin_amdgcn_exp2f(fmaf(s[8 * (G) + j], SCALE_L2, -(mm))); \
    pf[G] = v4u{cvtpk(p_[0], p_[1]), cvtpk(p_[2], p_[3]), cvtpk(p_[4], p_[5]), cvtpk(p_[6], p_[7])}; } while (0)
    {
      SOFTMAX(0, m); SOFTMAX(1, m);
      asm volatile("" : "+v"(pf[0]), "+v"(pf[1]));
      SB(); VLOAD(XV + 9);
      SOFTMAX(2, m); SOFTMAX(3, m);
      asm volatile("" : "+v"(pf[2]), "+v"(pf[3]));
      SB(); VLOAD(XV + 10);
      const v4u ones = {0x3f803f80u, 0x3f803f80u, 0x3f803f80u, 0x3f803f80u};
      f32x4 l4 = {0.f, 0.f, 0.f, 0.f};
#pragma unroll
      for (int G = 0; G < 4; ++G) l4 = mfma16(pf[G], ones, l4);
      if (lane < 4) ((LAS float*)(lds + L_ML))[sw * 8 + lane] = m;
      if (lane == 0) *(LAS f32x4*)(lds + L_ML + sw * 32 + 16) = l4;
      LGKM0();
      if (lane == 0) __hip_atomic_fetch_add(flags + F_CNT, 1u, RLX_WG);
    }
    f32x4 acc[8];
#pragma unroll
    for (int j = 0; j < 8; ++j) acc[j] = f32x4{0.f, 0.f, 0.f, 0.f};
#define PV(G) do { \
    _Pragma("unroll") for (int d = 0; d < 4; ++d) { v4u lo_, hi_; \
      _Pragma("unroll") for (int jj = 0; jj < 4; ++jj) { \
        lo_[jj] = __builtin_amdgcn_perm(vf[8 * (G) + 2 * jj + 1][d], vf[8 * (G) + 2 * jj][d], 0x05040100u); \
        hi_[jj] = __builtin_amdgcn_perm(vf[8 * (G) + 2 * jj + 1][d], vf[8 * (G) + 2 * jj][d], 0x07060302u); } \
      acc[2 * d] = mfma16(pf[G], lo_, acc[2 * d]); acc[2 * d + 1] = mfma16(pf[G], hi_, acc[2 * d + 1]); } SB(); } while (0)
    static_for<XV + 11, 32>([&](auto ic) {
      constexpr int i = ic.value;
      VLOAD(i);
      if constexpr (i == XV + 11) PV(0);
      if constexpr (i == 15 + PVD) PV(1);
      if constexpr (i == 23 + PVD) PV(2);
    });
    if (lane == 0) __hip_atomic_fetch_add(flags + F_VD, 1u, RLX_WG);
    v4u wf[2][2];
    unsigned a_wtr = lds0 + L_W + sw * 4096 + (g * 8 + (r >> 2)) * 64 + (r & 3) * 8;
#define WFRAG(ks) do { _Pragma("unroll") for (int nt = 0; nt < 2; ++nt) { \
      const v2u u0 = __builtin_bit_cast(v2u, __builtin_amdgcn_ds_read_tr16_b64_v4i16((LAS v4s*)(uintptr_t)(a_wtr + (ks) * 2048 + nt * 32))); \
      const v2u u1 = __builtin_bit_cast(v2u, __builtin_amdgcn_ds_read_tr16_b64_v4i16((LAS v4s*)(uintptr_t)(a_wtr + (ks) * 2048 + 256 + nt * 32))); \
      wf[ks][nt] = v4u{u0.x, u0.y, u1.x, u1.y}; } } while (0)
    asm volatile("" : "+v"(a_wtr));
    unsigned a_mw = lds0 + L_B + sw * 2048 + voff, a_mr = lds0 + L_B + sw * 256 + voff;
    unsigned a_own = lds0 + L_A + sw * 2304 + me * 144 + lane * 8, a_put = lds0 + L_A + sw * 2304 + (lane >> 3) * 144 + (lane & 7) * 16;
    unsigned a_get = lds0 + L_A + sw * 2304 + r * 144 + g * 16;
    unsigned a_rw = lds0 + L_K0 + sw * 512 + lane * 4, a_rr = lds0 + L_K0 + ((g & 1) * 64 + (2 * sw + (g >> 1)) * 16 + r) * 4;
    int o_part = partg + me * 1024 + sw * 128 + lane * 8, o_get = partg + (lane >> 3) * 1024 + sw * 128 + (lane & 7) * 16;
    int o_outp = WS_OUTP + (((bh * 4 + 2 * sw + (g >> 1)) * 8 + kvh) * 512 + cj * 32 + (lane & 31)) * 4, o_fin = WS_OUTP + b * 16384 + tid * 4;
    us* outp = out + b * DM + tid;
    float xres = 0.f, b2v = 0.f;
    if (fin) {
      lds_wait_ne0(flags + F_FIN);
      xres = bflo(((LAS us*)(lds + L_X))[b * 520 + tid]); b2v = bflo(flags[F_B2]);
    }
    asm volatile("" : "+v"(a_mw), "+v"(a_mr), "+v"(a_own), "+v"(a_put), "+v"(a_get), "+v"(a_rw), "+v"(a_rr));
    asm volatile("" : "+v"(o_part), "+v"(o_get), "+v"(o_outp), "+v"(o_fin), "+v"(outp), "+v"(xres), "+v"(b2v));
#define LDSP(T, a) ((LAS T*)(uintptr_t)(a))
    lds_wait_ne0(flags + F_Z);
    SB();
    {
      const f32x4 Z4 = *(LAS f32x4*)(lds + L_Z);
      const f32x4 mo = *(LAS f32x4*)(lds + L_ML + sw * 32);
      const float zl = ((LAS float*)(lds + L_Z))[lane & 3];
      f32x4 f4;
#pragma unroll
      for (int i = 0; i < 4; ++i) f4[i] = __builtin_amdgcn_exp2f(mo[i] - Z4[i]);
#pragma unroll
      for (int j = 0; j < 8; ++j) acc[j] *= f4;
      const float zz = (r < 4) ? zl : m;
      SOFTMAX(3, zz);
      SB();
    }
    {
      v4u lo_[4], hi_[4];
#pragma unroll
      for (int jj = 0; jj < 4; ++jj) {
#pragma unroll
        for (int d = 0; d < 4; ++d) {
          lo_[d][jj] = __builtin_amdgcn_perm(vf[24 + 2 * jj + 1][d], vf[24 + 2 * jj][d], 0x05040100u);
          hi_[d][jj] = __builtin_amdgcn_perm(vf[24 + 2 * jj + 1][d], vf[24 + 2 * jj][d], 0x07060302u);
        }
        SB();
      }
#pragma unroll
      for (int d = 0; d < 4; ++d) { acc[2 * d] = mfma16(pf[3], lo_[d], acc[2 * d]); acc[2 * d + 1] = mfma16(pf[3], hi_[d], acc[2 * d + 1]); }
      SB();
    }
    if (lane < 16) {
#pragma unroll
      for (int j = 0; j < 8; ++j) LDSP(f32x4, a_mw)[j * 16] = acc[j];
    }
    WG_BARRIER();
    if (lane < 16) {
      f32x4 ow[8];
#pragma unroll
      for (int w = 0; w < 8; ++w) ow[w] = LDSP(f32x4, a_mr)[w * 128];
      const f32x4 o = ((ow[0] + ow[1]) + (ow[2] + ow[3])) + ((ow[4] + ow[5]) + (ow[6] + ow[7]));
      const v2u op = {cvtpk(o[0], o[1]), min(cvtpk(o[2], o[3]), EMPTY - 1u)};
      __builtin_amdgcn_raw_buffer_store_b64(op, wsr, o_part, 0, AUX_SC1);
      *LDSP(v2u, a_own) = op;
    }
    SB();
    {
      bool ok0 = (lane >> 3) == me, ok1 = (lane >> 3) + 8 == me;
      pace<sched(63) + H2T>(tb0);
      v4u p0 = __builtin_amdgcn_raw_buffer_load_b128(wsr, ok0 ? WS_END : o_get, 0, AUX_SC1);
      v4u p1 = __builtin_amdgcn_raw_buffer_load_b128(wsr, ok1 ? WS_END : o_get + 8192, 0, AUX_SC1);
      SB();
      lds_wait_ne0(flags + F_W);
      WFRAG(0);
      WFRAG(1);
      asm volatile("" : "+v"(wf[0][0]), "+v"(wf[0][1]), "+v"(wf[1][0]), "+v"(wf[1][1]));
      SB();
#pragma unroll 1
      for (unsigned spin = 0; spin < SPIN_CAP; ++spin) {
        if (!ok0) *LDSP(v4u, a_put) = p0;
        if (!ok1) *LDSP(v4u, a_put + 1152) = p1;
        ok0 = max(p0[1], p0[3]) != EMPTY;
        ok1 = max(p1[1], p1[3]) != EMPTY;
        if (__builtin_expect(__all(ok0 & ok1), 1)) break;
        CFENCE();
        p0 = __builtin_amdgcn_raw_buffer_load_b128(wsr, ok0 ? WS_END : o_get, 0, AUX_SC1);
        p1 = __builtin_amdgcn_raw_buffer_load_b128(wsr, ok1 ? WS_END : o_get + 8192, 0, AUX_SC1);
      }
      f32x4 c0 = {0.f, 0.f, 0.f, 0.f}, c1 = {0.f, 0.f, 0.f, 0.f};
#pragma unroll
      for (int ks = 0; ks < 2; ++ks) {
        const v4u af = LDSP(v4u, a_get)[ks * 4];
        c0 = mfma16(af, wf[ks][0], c0);
        c1 = mfma16(af, wf[ks][1], c1);
      }
      LDSP(float, a_rw)[0] = (c0[0] + c0[1]) + (c0[2] + c0[3]);
      LDSP(float, a_rw)[64] = (c1[0] + c1[1]) + (c1[2] + c1[3]);
    }
    WG_BARRIER();
    if (sw < 2) {
      LAS float* red = LDSP(float, a_rr);
      const float a = ((red[0] + red[128]) + (red[256] + red[384])) + ((red[512] + red[640]) + (red[768] + red[896]));
      __builtin_amdgcn_raw_buffer_store_b32(min(__float_as_uint(a), EMPTY - 1u), wsr, o_outp, 0, AUX_SC1);
    }
    if (fin) {
      unsigned pv[8];
      if (sw >= 2) __builtin_amdgcn_s_sleep(H3E);
      __builtin_amdgcn_s_sleep(H3D);
      for (unsigned spin = 0; spin < SPIN_CAP; ++spin) {
        CFENCE();
#pragma unroll
        for (int k = 0; k < 8; ++k) pv[k] = __builtin_amdgcn_raw_buffer_load_b32(wsr, o_fin, k * 2048, AUX_SC1);
        const unsigned mx = max(max(max(pv[0], pv[1]), max(pv[2], pv[3])), max(max(pv[4], pv[5]), max(pv[6], pv[7])));
        if (__all(mx != EMPTY)) break;
      }
      const float a = ((__uint_as_float(pv[0]) + __uint_as_float(pv[1])) + (__uint_as_float(pv[2]) + __uint_as_float(pv[3]))) +
                      ((__uint_as_float(pv[4]) + __uint_as_float(pv[5])) + (__uint_as_float(pv[6]) + __uint_as_float(pv[7])));
      const float h = rbf(xres + rbf(a));
      float s1 = h, s2 = h * h;
      wave_sum2(s1, s2);
      if ((lane & 47) == 0) ((LAS float*)(lds + L_B))[sw * 2 + g] = s1;
      WG_BARRIER();
      const f32x4 q0 = *(LAS f32x4*)(lds + L_B), q1 = *(LAS f32x4*)(lds + L_B + 16), q2 = *(LAS f32x4*)(lds + L_B + 32), q3 = *(LAS f32x4*)(lds + L_B + 48);
      const f32x4 qs = (q0 + q1) + (q2 + q3);
      const float mean = (qs[0] + qs[2]) * (1.f / DM);
      const float var = fmaxf((qs[1] + qs[3]) * (1.f / DM) - mean * mean, 0.f);
      const float r1 = __builtin_amdgcn_rsqf(var + 1e-5f);
      const float r2 = __builtin_amdgcn_rsqf(fmaf(var * r1, r1, 1e-5f));
      const float t = ((h - mean) * r1) * r2;
      const unsigned tb = cvtpk(t, t) & 0xffffu;
      const unsigned rel = (tb & 0x7fffu) - (unsigned)(TAB_E0 << 7);
      const bool inr = rel < (unsigned)TAB_H;
      float y = bflo(((LAS us*)(lds + L_T))[(inr ? rel : 0u) + (tb >> 15) * TAB_H]);
      u64 miss = __ballot(!inr);
      while (__builtin_expect(miss != 0, 0)) {
        const int l = __builtin_ctzll(miss);
        miss &= miss - 1;
        const float ts = bflo((unsigned)__builtin_amdgcn_readlane((int)tb, l));
        const f32x2 tt[1] = {f32x2{ts, ts}};
        f32x2 aa[1];
        ffn_eval<1>(lds, lane, tt, aa);
        const float ys = rbf(wave_sum(aa[0][0]));
        if (lane == l) y = ys;
      }
      const float ov = h + rbf(y + b2v);
      *(GAS us*)outp = (us)(cvtpk(ov, ov) & 0xffffu);
    }
  }
  asm volatile(".Lpf_end_b:");
}

extern "C" void kernel_launch(void* const* d_in, const int* in_sizes, int n_in, void* d_out, int out_size, void* d_ws, size_t ws_size, hipStream_t stream) {
  fused_kernel<<<256, 768, 0, stream>>>((const us*)d_in[0], (const us*)d_in[1], (const us*)d_in[2], (const us*)d_in[3], (const us*)d_in[4],
                                        (const us*)d_in[6], (const us*)d_in[7], (const us*)d_in[8], (const us*)d_in[9], (us*)d_out, (unsigned char*)d_ws);
}
```
